```python
import torch

def _softmax(x, axis=-1):
    return torch.softmax(x, dim=axis)
def _gelu(x):
    return torch.nn.functional.gelu(x, approximate="tanh")
def _silu(x):
    return torch.nn.functional.silu(x)
def _sigmoid(x):
    return torch.sigmoid(x)
def _layernorm(x, axis=-1, eps=1e-5):
    m = x.mean(dim=axis, keepdim=True)
    v = x.var(dim=axis, keepdim=True, unbiased=False)
    return (x - m) / torch.sqrt(v + eps)
def _scatter_add(tgt, idx, src, axis=0):
    return tgt.clone().index_add_(axis, idx, src)
def _segment_sum(src, seg, n):
    out = torch.zeros((n,) + src.shape[1:], dtype=src.dtype)
    return out.index_add_(0, seg, src)
def _pad(x, pw):
    flat = tuple(v for pair in reversed(pw) for v in pair)
    return torch.nn.functional.pad(x, flat)

kernel_name = 'parallel_sum__sliding_local_dilated__dense_activation_squared_relu__prompt'

def setup_inputs():

    _w = (lambda a: (lambda t: t.to(torch.bfloat16) if t.is_floating_point() else t)(torch.as_tensor(a)))
    def _A_setup():
        B, S, D = 1, 2048, 448
        return {
            "x": _w((lambda *s: torch.randn(*s, dtype=torch.bfloat16) * 0.05)(B, S, D)),
            "wqkv": _w((lambda *s: torch.randn(*s, dtype=torch.bfloat16) * 0.1)(D, 3 * D)),
            "wo": _w((lambda *s: torch.randn(*s, dtype=torch.bfloat16) * 0.05)(D, D)),
        }
    _A_in = _A_setup()

    def _B_setup():
        B, S, D, F = 1, 2048, 448, 1792
        return {
            "x": _w((lambda *s: torch.randn(*s, dtype=torch.bfloat16) * 0.05)(B, S, D)),
            "w1": _w((lambda *s: torch.randn(*s, dtype=torch.bfloat16) * 0.05)(D, F)),
            "b1": _w((lambda *s: torch.randn(*s, dtype=torch.bfloat16) * 0.2)(F)),
            "w2": _w((lambda *s: torch.randn(*s, dtype=torch.bfloat16) * 0.05)(F, D)),
            "b2": _w((lambda *s: torch.randn(*s, dtype=torch.bfloat16) * 0.8)(D)),
        }
    _B_in = _B_setup()

    return {
        "A_x": _A_in["x"],
        "A_wqkv": _A_in["wqkv"],
        "A_wo": _A_in["wo"],
        "B_x": _B_in["x"],
        "B_w1": _B_in["w1"],
        "B_b1": _B_in["b1"],
        "B_w2": _B_in["w2"],
        "B_b2": _B_in["b2"],
    }

def reference(A_x, A_wqkv, A_wo, B_x, B_w1, B_b1, B_w2, B_b2):
    def _A_ref(x, wqkv, wo):
        B, S, D = x.shape
        H = 16; Dh = D // H
        W, dil = 8, 2
        qkv = (lambda x, *d: x.permute(*d))((x @ wqkv).reshape(B, S, 3, H, Dh), 2, 0, 3, 1, 4)
        q, k, v = qkv[0], qkv[1], qkv[2]
        scale = Dh ** -0.5
        pos = torch.arange(S)
        off = torch.arange(W) * dil
        idx = pos[:, None] - off[None, :]
        mask = idx >= 0
        idx_c = torch.where(mask, idx, 0).reshape(-1)
        kg = k[:, :, idx_c, :].reshape(B, H, S, W, Dh)
        vg = v[:, :, idx_c, :].reshape(B, H, S, W, Dh)
        logits = torch.einsum("bhsd,bhswd->bhsw", q, kg) * scale
        logits = torch.where(mask[None, None, :, :], logits, -1e9)
        attn = _softmax(logits, axis=-1)
        o = torch.einsum("bhsw,bhswd->bhsd", attn, vg)
        return (lambda x, *d: x.permute(*d))(o, 0, 2, 1, 3).reshape(B, S, D) @ wo

    def _B_ref(x, w1, b1, w2, b2):
        h = torch.einsum("bsd,df->bsf", x, w1) + b1
        zero = torch.zeros_like(h)
        r = torch.where(h > zero, h, zero)
        h = r * r
        out = torch.einsum("bsf,fd->bsd", h, w2) + b2
        return out

    x = A_x
    xn = _layernorm(x, axis=-1)
    return x + _A_ref(xn, A_wqkv, A_wo) + _B_ref(xn, B_w1, B_b1, B_w2, B_b2)
```

```cpp
#include <hip/hip_runtime.h>
#include <hip/hip_bf16.h>
#include <stdint.h>
typedef unsigned short u16;
__device__ __forceinline__ float bf2f(u16 v){ return __uint_as_float(((unsigned)v)<<16); }
__device__ __forceinline__ u16 f2bf(float f){ __hip_bfloat16 b = __float2bfloat16(f); return *(u16*)&b; }
constexpr int S=2048, D=448, F=1792, H=16, DH=28;
__global__ void k_ln(const u16* x, float* xn){
    int r = blockIdx.x; int l = threadIdx.x;
    float v[7]; float s=0;
    for(int i=0;i<7;i++){ v[i]=bf2f(x[r*D+l+64*i]); s+=v[i]; }
    for(int m=32;m>=1;m>>=1) s+=__shfl_xor(s,m);
    float mean=s/D; float q=0;
    for(int i=0;i<7;i++){ float d=v[i]-mean; q+=d*d; }
    for(int m=32;m>=1;m>>=1) q+=__shfl_xor(q,m);
    float rs=rsqrtf(q/D+1e-5f);
    for(int i=0;i<7;i++) xn[r*D+l+64*i]=(v[i]-mean)*rs;
}
template<int MODE>
__global__ void k_gemm(const float* A, int lda, const u16* W, int K, int N, float* C, int ldc, const u16* bias){
    __shared__ float As[16][17]; __shared__ float Bs[16][17];
    int tx=threadIdx.x&15, ty=threadIdx.x>>4;
    int row=blockIdx.y*16+ty, col=blockIdx.x*16+tx;
    float acc=0;
    for(int k0=0;k0<K;k0+=16){
        As[ty][tx]=A[row*lda+k0+tx];
        Bs[ty][tx]=bf2f(W[(k0+ty)*N+col]);
        __syncthreads();
        for(int k=0;k<16;k++) acc+=As[ty][k]*Bs[k][tx];
        __syncthreads();
    }
    if(MODE==1){ acc+=bf2f(bias[col]); acc=acc>0?acc:0; acc=acc*acc; }
    if(MODE==2){ acc+=C[row*ldc+col]; }
    C[row*ldc+col]=acc;
}
__global__ void k_attn(const float* qkv, float* o){
    int s=blockIdx.x; int h=threadIdx.x; if(h>=H) return;
    const float* q=qkv+s*3*D+h*DH;
    float lg[8]; float mx=-1e30f;
    for(int w=0;w<8;w++){ int j=s-2*w; float a=-1e9f; if(j>=0){ const float* k=qkv+j*3*D+D+h*DH; a=0; for(int d=0;d<DH;d++) a+=q[d]*k[d]; a*=rsqrtf((float)DH);} lg[w]=a; mx=fmaxf(mx,a);}
    float sum=0; for(int w=0;w<8;w++){ lg[w]=__expf(lg[w]-mx); sum+=lg[w]; }
    for(int d=0;d<DH;d++){ float a=0; for(int w=0;w<8;w++){ int j=s-2*w; if(j<0) j=0; a+=lg[w]*qkv[j*3*D+2*D+h*DH+d]; } o[s*D+h*DH+d]=a/sum; }
}
__global__ void k_fin(const u16* x, const float* a, const u16* b2, u16* out){
    int i=blockIdx.x*256+threadIdx.x; out[i]=f2bf(bf2f(x[i])+a[i]+bf2f(b2[i%D]));
}
extern "C" void kernel_launch(void* const* d_in, const int* in_sizes, int n_in,
                              void* d_out, int out_size, void* d_ws, size_t ws_size,
                              hipStream_t stream) {
    const u16* x=(const u16*)d_in[0]; const u16* wqkv=(const u16*)d_in[1]; const u16* wo=(const u16*)d_in[2];
    const u16* w1=(const u16*)d_in[4]; const u16* b1=(const u16*)d_in[5]; const u16* w2=(const u16*)d_in[6]; const u16* b2=(const u16*)d_in[7];
    float* ws=(float*)d_ws;
    float* xn=ws; float* qkv=xn+S*D; float* o=qkv+S*3*D; float* h=o+S*D; float* acc=h+S*F;
    k_ln<<<S,64,0,stream>>>(x,xn);
    k_gemm<0><<<dim3(3*D/16,S/16),256,0,stream>>>(xn,D,wqkv,D,3*D,qkv,3*D,nullptr);
    k_attn<<<S,64,0,stream>>>(qkv,o);
    k_gemm<1><<<dim3(F/16,S/16),256,0,stream>>>(xn,D,w1,D,F,h,F,b1);
    k_gemm<0><<<dim3(D/16,S/16),256,0,stream>>>(o,D,wo,D,D,acc,D,nullptr);
    k_gemm<2><<<dim3(D/16,S/16),256,0,stream>>>(h,F,w2,F,D,acc,D,nullptr);
    k_fin<<<S*D/256,256,0,stream>>>(x,acc,b2,(u16*)d_out);
}
```

```cpp
#include <hip/hip_runtime.h>
#include <stdint.h>

struct Buf { const void* p; uint32_t bytes; uint32_t flags; };
struct Args { Buf x, wqkv, w1, w2, wo, ws, out, b1, b2; };

__global__ void __launch_bounds__(256, 1) fused_block_kernel(Args args) {
#if defined(__HIP_DEVICE_COMPILE__)
    __shared__ __attribute__((aligned(16))) char lds[163840];
    unsigned tid = threadIdx.x;
    asm volatile(
        "  s_lshr_b32 s45, %[wg], 6\n"
        "  s_cmp_ge_u32 s45, 2\n"
        "  s_cselect_b32 s54, 32, 16\n"
        "  s_cselect_b32 s55, 32, 0\n"
        "  s_load_dwordx4 s[4:7], %[ka], 0\n"
        "  s_load_dwordx4 s[8:11], %[ka], s54\n"
        "  s_load_dwordx4 s[12:15], %[ka], s55\n"
        "  s_load_dwordx16 s[16:31], %[ka], 48\n"
        "  s_load_dwordx8 s[32:39], %[ka], 112\n"
        "  s_cselect_b32 s94, 7, 0\n"
        "  s_cselect_b32 s53, 64, 56\n"
        "  s_cselect_b32 s52, 32, 24\n"
        "  s_cselect_b32 s95, 1, 7\n"
        "  v_lshrrev_b32 v1, 6, v0\n"
        "  v_and_b32 v204, 63, v0\n"
        "  s_lshr_b32 s42, %[wg], 3\n"
        "  v_readfirstlane_b32 s40, v1\n"
        "  s_add_u32 s94, s94, 21\n"
        "  s_lshl_b32 s94, s94, 7\n"
        "  s_lshl_b32 s95, s95, 7\n"
        "  s_getpc_b64 s[92:93]\n"
        ".Lpc:\n"
        "  s_lshl2_add_u32 s54, s42, s40\n"
        "  s_mul_i32 s54, s54, 384\n"
        "  s_min_u32 s54, s54, .Lend-.Lpc-384\n"
        "  v_lshl_add_u32 v211, v204, 7, s54\n"
        "  s_mov_b64 exec, 7\n"
        "  global_load_dword v187, v211, s[92:93]\n"
        "  s_mov_b64 exec, -1\n"
        "  s_and_b32 s41, %[wg], 7\n"
        "  s_and_b32 s43, s42, 15\n"
        "  s_lshr_b32 s44, s41, 1\n"
        "  s_and_b32 s45, s45, 2\n"
        "  s_and_b32 s54, s41, 1\n"
        "  s_or_b32 s45, s45, s54\n"
        "  s_lshr_b32 s46, s40, 1\n"
        "  s_and_b32 s47, s40, 1\n"
        "  v_lshrrev_b32 v208, 3, v204\n"
        "  v_and_b32 v209, 7, v204\n"
        "  v_lshrrev_b32 v206, 4, v204\n"
        "  v_xor_b32 v211, v209, v206\n"
        "  v_mul_u32_u24 v210, 1792, v208\n"
        "  v_lshl_add_u32 v148, v211, 4, v210\n"
        "  v_xor_b32 v211, 64, v148\n"
        "  v_add_u32 v149, 13312, v211\n"
        "  v_add_u32 v150, 26624, v148\n"
        "  v_add_u32 v151, 39936, v211\n"
        "  s_lshl_b32 s54, s43, 7\n"
        "  s_lshl_b32 s55, s47, 6\n"
        "  s_add_u32 s54, s54, s46\n"
        "  s_add_u32 s55, s55, s54\n"
        "  s_mul_i32 s48, s55, 896\n"
        "  s_lshr_b32 s89, s45, 1\n"
        "  s_mul_i32 s89, s89, 0\n"
        "  s_add_u32 s88, s43, s89\n"
        "  s_and_b32 s89, s41, 3\n"
        "  s_mul_i32 s89, s89, 2\n"
        "  s_add_u32 s88, s88, s89\n"
        "  s_mul_i32 s89, s88, 37\n"
        "  s_lshr_b32 s89, s89, 8\n"
        "  s_mul_i32 s89, s89, 7\n"
        "  s_sub_u32 s88, s88, s89\n"
        "  s_mov_b32 s91, s88\n"
        "  s_lshl_b32 s89, s88, 7\n"
        "  s_add_u32 s48, s48, s89\n"
        "  s_lshl_b32 s56, s40, 12\n"
        "  s_mov_b32 s51, 0\n"
        "  s_mov_b32 m0, s56\n"
        "  v_bfe_u32 v212, v204, 3, 2\n"
        "  v_lshrrev_b32 v211, 5, v204\n"
        "  v_lshl_add_u32 v212, v211, 3, v212\n"
        "  v_bfe_u32 v213, v204, 1, 2\n"
        "  v_xor_b32 v213, v213, v206\n"
        "  v_and_b32 v214, 1, v204\n"
        "  v_mul_u32_u24 v211, s94, v212\n"
        "  v_lshrrev_b32 v210, 1, v213\n"
        "  v_mad_u32_u24 v211, v210, s53, v211\n"
        "  v_and_b32 v210, 1, v213\n"
        "  v_mad_u32_u24 v211, v210, s52, v211\n"
        "  v_lshl_add_u32 v211, v214, 4, v211\n"
        "  s_waitcnt lgkmcnt(0)\n"
        "  buffer_load_dwordx4 v148, s[4:7], s48 offen offset:0 lds\n"
        "  buffer_load_dwordx4 v149, s[4:7], s48 offen offset:1024 lds\n"
        "  buffer_load_dwordx4 v150, s[4:7], s48 offen offset:2048 lds\n"
        "  buffer_load_dwordx4 v151, s[4:7], s48 offen offset:3072 lds\n"
        "  s_sub_u32 s8, s8, 4096\n"
        "  s_subb_u32 s9, s9, 0\n"
        "  s_add_u32 s10, s10, 4096\n"
        "  s_and_b32 s55, s45, 1\n"
        "  s_lshl1_add_u32 s53, s44, s55\n"
        "  s_cmp_ge_u32 s45, 2\n"
        "  s_cselect_b32 s52, 2, 0\n"
        "  s_cselect_b32 s55, s55, 0\n"
        "  s_cselect_b32 s87, 7, 6\n"
        "  s_mul_i32 s53, s53, 112\n"
        "  s_lshl_b32 s53, s53, s52\n"
        "  s_lshl_b32 s49, s40, 4\n"
        "  s_mul_i32 s49, s49, s94\n"
        "  s_add_u32 s49, s49, s53\n"
        "  s_lshl2_add_u32 s50, s47, s46\n"
        "  s_lshl_b32 s50, s50, 2\n"
        "  s_mul_i32 s50, s50, s94\n"
        "  s_lshl_b32 s52, s55, 6\n"
        "  s_add_u32 s53, s53, s52\n"
        "  s_add_u32 s50, s50, s53\n"
        "  s_mul_i32 s57, s87, s47\n"
        "  s_mul_i32 s53, s46, 3\n"
        "  s_add_u32 s57, s57, s53\n"
        "  s_lshl_b32 s57, s57, 10\n"
        "  s_add_u32 s57, s57, 18432\n"
        "  s_mul_i32 s58, s87, s40\n"
        "  s_lshl_b32 s87, s87, 11\n"
        "  s_add_u32 s87, s87, s57\n"
        "  s_lshl_b32 s85, s94, 6\n"
        "  s_lshl_b32 s52, s94, 5\n"
        "  v_add_u32 v153, 4096, v211\n"
        "  s_sub_u32 s95, s95, 1024\n"
        "  v_add_u32 v154, s95, v153\n"
        "  v_add_u32 v155, s95, v154\n"
        "  s_mul_i32 s90, s91, s85\n"
        "  s_add_u32 s50, s50, s90\n"
        "  s_add_u32 s88, s88, 1\n"
        "  s_cmp_eq_u32 s88, 7\n"
        "  s_cselect_b32 s88, 0, s88\n"
        "  s_cselect_b32 s89, -6, 1\n"
        "  s_add_u32 m0, s51, s57\n"
        "  s_nop 0  ; hazard\n"
        "  buffer_load_dwordx4 v153, s[8:11], s50 offen offset:0 lds\n"
        "  s_lshl_b32 s90, s89, 7\n"
        "  s_add_u32 s48, s48, s90\n"
        "  buffer_load_dwordx4 v154, s[8:11], s50 offen offset:1024 lds\n"
        "  s_mov_b32 s68, s50\n"
        "  s_mov_b32 s69, s51\n"
        "  buffer_load_dwordx4 v155, s[8:11], s50 offen offset:2048 lds\n"
        "  v_add_u32 v156, s52, v153\n"
        "  v_add_u32 v157, s52, v154\n"
        "  v_add_u32 v158, s52, v155\n"
        "  s_lshl_b32 s53, s47, 2\n"
        "  v_add_u32 v211, s53, v208\n"
        "  s_sub_u32 s54, s54, 16\n"
        "  v_lshl_add_u32 v210, v211, 1, s54\n"
        "  v_max_i32 v210, 0, v210\n"
        "  v_mul_u32_u24 v210, 896, v210\n"
        "  v_bfe_u32 v211, v211, 1, 3\n"
        "  v_xor_b32 v211, v211, v209\n"
        "  v_lshl_add_u32 v152, v211, 4, v210\n"
        "  v_bfe_u32 v211, v204, 2, 2\n"
        "  v_and_b32 v210, 1, v206\n"
        "  v_lshl_add_u32 v211, v210, 3, v211\n"
        "  v_lshrrev_b32 v215, 5, v204\n"
        "  v_lshl_add_u32 v211, v215, 2, v211\n"
        "  v_mul_u32_u24 v211, 3584, v211\n"
        "  v_bfe_u32 v215, v204, 1, 1\n"
        "  v_xor_b32 v210, v210, v215\n"
        "  v_lshl_add_u32 v211, v210, 5, v211\n"
        "  v_lshl_add_u32 v211, v214, 4, v211\n"
        "  s_mul_i32 s53, s55, -384\n"
        "  s_add_u32 s53, s53, 384\n"
        "  s_add_u32 s49, s49, s53\n"
        "  s_cmp_eq_u32 s47, 0\n"
        "  s_cselect_b32 s64, 0xffffff00, -1\n"
        "  s_lshl_b32 s53, s58, 10\n"
        "  s_add_u32 s53, s53, 24576\n"
        "  s_lshl_b32 s58, s40, 9\n"
        "  s_add_u32 s58, s58, 16384\n"
        "  s_cmp_ge_u32 s45, 2\n"
        "  s_cselect_b64 vcc, -1, 0\n"
        "  s_cselect_b32 s64, -1, s64\n"
        "  s_cselect_b32 s65, -1, 0\n"
        "  s_cselect_b32 s58, s53, s58\n"
        "  s_cselect_b32 s49, s49, 0\n"
        "  s_cselect_b32 s84, s85, 0x80\n"
        "  v_cndmask_b32 v152, v152, v211, vcc\n"
        "  s_mul_i32 s94, s91, s84\n"
        "  s_add_u32 s49, s49, s94\n"
        "  s_add_u32 m0, s51, s58\n"
        "  s_mul_i32 s90, s89, s85\n"
        "  s_add_u32 s50, s50, s90\n"
        "  s_mov_b64 exec, s[64:65]\n"
        "  buffer_load_dwordx4 v152, s[12:15], s49 offen offset:0 lds\n"
        "  s_mov_b64 exec, -1\n"
        "  s_mul_i32 s90, s89, s84\n"
        "  s_add_u32 s49, s49, s90\n"
        "  s_add_u32 m0, s69, s87\n"
        "  s_nop 0  ; hazard\n"
        "  buffer_load_dwordx4 v156, s[8:11], s68 offen offset:0 lds\n"
        "  buffer_load_dwordx4 v157, s[8:11], s68 offen offset:1024 lds\n"
        "  buffer_load_dwordx4 v158, s[8:11], s68 offen offset:2048 lds\n"
        "  s_mov_b32 s51, 49152\n"
        "  s_cmp_ge_u32 s45, 2\n"
        "  s_cbranch_scc1 .Lffn\n"
        "  s_sub_u32 s94, s42, 4\n"
        "  s_cmp_lt_u32 s94, 28\n"
        "  s_cselect_b32 s86, 1, 0\n"
        "  s_cselect_b32 s94, s94, 0\n"
        "  s_cselect_b32 s76, 15, 0\n"
        "  s_mov_b32 s77, 0\n"
        "  s_cmp_eq_u32 s40, 3\n"
        "  s_cselect_b32 s78, 0x30, 0xf0\n"
        "  s_mov_b32 s79, 0\n"
        "  s_or_b64 s[80:81], s[76:77], s[78:79]\n"
        "  s_and_b32 s97, s94, 1\n"
        "  s_bfe_u32 s95, s94, 0x10001\n"
        "  s_lshl1_add_u32 s96, s41, s95\n"
        "  s_lshr_b32 s94, s94, 2\n"
        "  s_lshl_b32 s63, s94, 7\n"
        "  s_lshl2_add_u32 s95, %[wg], s40\n"
        "  s_lshl_b32 s95, s95, 7\n"
        "  s_add_u32 s95, s95, 9437184\n"
        "  v_and_b32 v216, 3, v204\n"
        "  v_lshrrev_b32 v217, 2, v204\n"
        "  v_lshlrev_b32 v217, 17, v217\n"
        "  v_lshl_add_u32 v217, v216, 3, v217\n"
        "  v_add_u32 v184, s95, v217\n"
        "  v_lshlrev_b32 v182, 4, v204\n"
        "  s_lshl1_add_u32 s94, s94, s97\n"
        "  s_lshl2_add_u32 s95, s96, s40\n"
        "  s_add_u32 m0, s51, s56\n"
        "  s_nop 0  ; hazard\n"
        "  buffer_load_dwordx4 v148, s[4:7], s48 offen offset:0 lds\n"
        "  s_lshl_b32 s95, s95, 10\n"
        "  s_lshr_b32 s55, s94, 2\n"
        "  s_lshl_b32 s55, s55, 7\n"
        "  s_add_u32 s95, s95, s55\n"
        "  s_and_b32 s55, s94, 3\n"
        "  s_lshl3_add_u32 s95, s55, s95\n"
        "  s_add_u32 s95, s95, 9568256\n"
        "  v_and_b32 v217, 2, v216\n"
        "  v_mul_u32_u24 v217, 63, v217\n"
        "  v_add_u32 v217, v217, v216\n"
        "  v_lshl_add_u32 v217, v217, 9, s95\n"
        "  s_and_b32 s55, s43, 1\n"
        "  s_lshl_b32 s55, s55, 1\n"
        "  s_lshl3_add_u32 s95, s40, s55\n"
        "  s_add_u32 s95, s95, 4\n"
        "  s_lshl_b32 s95, s95, 12\n"
        "  s_lshr_b32 s55, s43, 1\n"
        "  s_lshl2_add_u32 s55, s55, s44\n"
        "  s_lshl_b32 s55, s55, 7\n"
        "  s_add_u32 s95, s95, s55\n"
        "  s_lshl3_add_u32 s95, s45, s95\n"
        "  s_add_u32 s95, s95, 9437184\n"
        "  s_add_u32 s88, s88, 1\n"
        "  buffer_load_dwordx4 v149, s[4:7], s48 offen offset:1024 lds\n"
        "  v_and_b32 v218, 2, v216\n"
        "  v_add_u32 v218, v218, v216\n"
        "  v_lshl_add_u32 v218, v218, 12, s95\n"
        "  v_cndmask_b32_e64 v217, v184, v217, s[76:77]\n"
        "  v_cndmask_b32_e64 v185, v217, v218, s[78:79]\n"
        "  v_mul_u32_u24 v216, 896, v208\n"
        "  v_lshl_add_u32 v216, v213, 5, v216\n"
        "  v_lshl_add_u32 v172, v214, 4, v216\n"
        "  v_add_u32 v173, 6144, v172\n"
        "  v_add_u32 v174, 12288, v172\n"
        "  v_add_u32 v175, 18432, v172\n"
        "  v_add_u32 v176, 8704, v172\n"
        "  v_add_u32 v177, 14848, v172\n"
        "  s_mov_b32 exec_hi, 0\n"
        "  v_mov_b32 v176, 0x40000000\n"
        "  s_mov_b32 exec_hi, -1\n"
        "  s_mul_i32 s60, s40, 401408\n"
        "  s_add_u32 s60, s60, s63\n"
        "  s_mul_i32 s61, s40, 100352\n"
        "  s_add_u32 s61, s61, s63\n"
        "  s_mul_i32 s59, s40, 12288\n"
        "  s_lshl1_add_u32 s95, s96, s97\n"
        "  s_mul_i32 s62, s95, 294912\n"
        "  s_mul_i32 s95, s40, 73728\n"
        "  s_cmp_eq_u32 s88, 7\n"
        "  s_cselect_b32 s88, 0, s88\n"
        "  s_cselect_b32 s89, -6, 1\n"
        "  buffer_load_dwordx4 v150, s[4:7], s48 offen offset:2048 lds\n"
        "  s_add_u32 s62, s62, s95\n"
        "  v_and_b32 v205, 15, v204\n"
        "  v_bfe_u32 v219, v204, 2, 3\n"
        "  v_lshlrev_b32 v216, 3, v204\n"
        "  v_and_b32 v216, 24, v216\n"
        "  v_lshl_add_u32 v220, v219, 7, v216\n"
        "  v_lshrrev_b32 v217, 1, v219\n"
        "  v_lshl_add_u32 v220, v217, 5, v220\n"
        "  v_lshrrev_b32 v221, 5, v204\n"
        "  v_lshl_add_u32 v217, v221, 10, v220\n"
        "  v_add_u32 v178, s59, v217\n"
        "  v_xor_b32 v179, 32, v178\n"
        "  v_xor_b32 v180, 64, v178\n"
        "  v_xor_b32 v181, 96, v178\n"
        "  v_lshrrev_b32 v217, 1, v205\n"
        "  v_xor_b32 v217, v217, v206\n"
        "  v_lshlrev_b32 v217, 4, v217\n"
        "  v_lshl_add_u32 v160, v205, 7, v217\n"
        "  v_xor_b32 v216, v209, v208\n"
        "  v_mul_u32_u24 v217, 1792, v208\n"
        "  v_lshl_add_u32 v252, v216, 4, v217\n"
        "  v_add_u32 v253, 13312, v252\n"
        "  buffer_load_dwordx4 v151, s[4:7], s48 offen offset:3072 lds\n"
        "  s_mov_b32 s70, 0xf0000f\n"
        "  s_mov_b32 s71, 0xf0000f00\n"
        "  s_mov_b32 s72, 0xaaaaaaaa\n"
        "  s_mov_b32 s73, s72\n"
        "  s_mov_b32 s74, 0xcccccccc\n"
        "  s_mov_b32 s75, s74\n"
        "  v_mov_b32 v228, 0\n"
        "  v_mov_b32 v229, 0\n"
        "  v_mov_b32 v230, 0\n"
        "  v_mov_b32 v231, 0\n"
        "  s_lshl_b32 s95, s46, 13\n"
        "  v_add_u32 v160, s95, v160\n"
        "  v_xor_b32 v161, 64, v160\n"
        "  v_bfe_u32 v217, v205, 1, 2\n"
        "  v_xor_b32 v217, v217, v206\n"
        "  v_lshlrev_b32 v217, 4, v217\n"
        "  v_lshl_add_u32 v217, v205, 7, v217\n"
        "  v_add_u32 v162, 16384, v217\n"
        "  v_xor_b32 v163, 64, v162\n"
        "  v_mul_u32_u24 v217, 6144, v221\n"
        "  s_lshl_b32 s95, s47, 6\n"
        "  v_xor_b32 v216, s95, v220\n"
        "  s_add_u32 m0, s51, s57\n"
        "  s_nop 0  ; hazard\n"
        "  buffer_load_dwordx4 v153, s[8:11], s50 offen offset:0 lds\n"
        "  v_add_u32 v216, v216, v217\n"
        "  v_add_u32 v164, 18432, v216\n"
        "  v_xor_b32 v165, 32, v164\n"
        "  v_mov_b32 v171, 0x3f803f80\n"
        "  v_mov_b32 v224, v171\n"
        "  v_mov_b32 v225, v171\n"
        "  v_mov_b32 v226, v171\n"
        "  v_mov_b32 v227, v171\n"
        "  v_mul_u32_u24 v216, 49152, v206\n"
        "  v_bfe_u32 v217, v204, 3, 1\n"
        "  v_lshl_add_u32 v216, v217, 10, v216\n"
        "  v_lshl_add_u32 v216, v209, 4, v216\n"
        "  s_mov_b32 exec_hi, 0xffff\n"
        "  ds_write_b128 v216, v[224:227] offset:16384\n"
        "  s_mov_b32 exec_hi, -1\n"
        "  s_mov_b32 s66, 0xf000f\n"
        "  s_mov_b32 s67, s66\n"
        "  v_lshlrev_b32 v216, 2, v206\n"
        "  v_sub_u32 v216, v216, v205\n"
        "  v_mov_b32 v217, 0xf149f2ca\n"
        "  s_lshl_b32 s94, s46, 3\n"
        "  v_subrev_u32 v219, s94, v216\n"
        "  s_cmp_eq_u32 s46, 0\n"
        "  s_cselect_b32 s92, -1, 0\n"
        "  s_cselect_b32 s93, 0, -1\n"
        "  s_cmp_eq_u32 s43, 0\n"
        "  s_cselect_b64 s[92:93], 0, s[92:93]\n"
        "  s_lshl_b32 s90, s89, 7\n"
        "  s_add_u32 s48, s48, s90\n"
        "  buffer_load_dwordx4 v154, s[8:11], s50 offen offset:1024 lds\n"
        "  v_cmp_le_i32 vcc, 9, v216\n"
        "  v_add_u32 v218, 7, v216\n"
        "  s_mul_i32 s95, s44, 18\n"
        "  v_cndmask_b32_e64 v192, v217, 0, vcc\n"
        "  v_cmp_gt_u32 vcc, 8, v218\n"
        "  v_lshlrev_b32 v207, 2, v205\n"
        "  s_nop 0\n"
        "  v_cndmask_b32_e64 v196, v217, 0, vcc\n"
        "  v_cmp_le_i32 vcc, 1, v219\n"
        "  s_nop 0  ; hazard\n"
        "  s_and_b64 vcc, vcc, s[92:93]\n"
        "  v_cndmask_b32_e64 v200, v217, 0, vcc\n"
        "  v_cmp_le_i32 vcc, 8, v216\n"
        "  v_add_u32 v218, 8, v216\n"
        "  s_mul_i32 s95, s44, 18\n"
        "  v_cndmask_b32_e64 v193, v217, 0, vcc\n"
        "  v_cmp_gt_u32 vcc, 8, v218\n"
        "  v_lshlrev_b32 v207, 2, v205\n"
        "  s_nop 0\n"
        "  v_cndmask_b32_e64 v197, v217, 0, vcc\n"
        "  v_cmp_le_i32 vcc, 0, v219\n"
        "  s_nop 0  ; hazard\n"
        "  s_and_b64 vcc, vcc, s[92:93]\n"
        "  v_cndmask_b32_e64 v201, v217, 0, vcc\n"
        "  v_cmp_le_i32 vcc, 7, v216\n"
        "  v_add_u32 v218, 9, v216\n"
        "  s_mov_b32 s68, s50\n"
        "  s_mov_b32 s69, s51\n"
        "  buffer_load_dwordx4 v155, s[8:11], s50 offen offset:2048 lds\n"
        "  s_mul_i32 s95, s44, 18\n"
        "  v_cndmask_b32_e64 v194, v217, 0, vcc\n"
        "  v_cmp_gt_u32 vcc, 8, v218\n"
        "  v_lshlrev_b32 v207, 2, v205\n"
        "  s_nop 0\n"
        "  v_cndmask_b32_e64 v198, v217, 0, vcc\n"
        "  v_cmp_le_i32 vcc, -1, v219\n"
        "  s_nop 0  ; hazard\n"
        "  s_and_b64 vcc, vcc, s[92:93]\n"
        "  v_cndmask_b32_e64 v202, v217, 0, vcc\n"
        "  v_cmp_le_i32 vcc, 6, v216\n"
        "  v_add_u32 v218, 10, v216\n"
        "  s_mul_i32 s95, s44, 18\n"
        "  v_cndmask_b32_e64 v195, v217, 0, vcc\n"
        "  v_cmp_gt_u32 vcc, 8, v218\n"
        "  v_lshlrev_b32 v207, 2, v205\n"
        "  s_nop 0\n"
        "  v_cndmask_b32_e64 v199, v217, 0, vcc\n"
        "  v_cmp_le_i32 vcc, -2, v219\n"
        "  s_nop 0  ; hazard\n"
        "  s_and_b64 vcc, vcc, s[92:93]\n"
        "  v_cndmask_b32_e64 v203, v217, 0, vcc\n"
        "  v_cmp_ne_u32 vcc, 0, v196\n"
        "  v_cmp_ne_u32_e64 s[92:93], 0, v197\n"
        "  v_mov_b32 v217, 0xffff\n"
        "  v_cndmask_b32 v170, 0, v217, vcc\n"
        "  s_add_u32 m0, s51, s58\n"
        "  s_mul_i32 s90, s89, s85\n"
        "  s_add_u32 s50, s50, s90\n"
        "  s_mov_b64 exec, s[64:65]\n"
        "  buffer_load_dwordx4 v152, s[12:15], s49 offen offset:0 lds\n"
        "  s_mov_b64 exec, -1\n"
        "  s_mul_i32 s90, s89, s84\n"
        "  s_add_u32 s49, s49, s90\n"
        "  v_mov_b32 v217, 0xffff0000\n"
        "  v_cndmask_b32_e64 v218, 0, v217, s[92:93]\n"
        "  v_or_b32 v170, v170, v218\n"
        "  v_cmp_ne_u32 vcc, 0, v198\n"
        "  v_cmp_ne_u32_e64 s[92:93], 0, v199\n"
        "  v_mov_b32 v217, 0xffff\n"
        "  v_cndmask_b32 v254, 0, v217, vcc\n"
        "  v_mov_b32 v217, 0xffff0000\n"
        "  v_cndmask_b32_e64 v218, 0, v217, s[92:93]\n"
        "  v_or_b32 v254, v254, v218\n"
        "  s_lshl1_add_u32 s95, s45, s95\n"
        "  s_add_u32 s95, s95, s47\n"
        "  s_add_u32 s95, s95, 14\n"
        "  s_lshl_b32 s95, s95, 12\n"
        "  s_lshl1_add_u32 s94, s43, s46\n"
        "  s_mul_i32 s94, s94, 294912\n"
        "  s_add_u32 s82, s94, s95\n"
        "  v_mfma_f32_32x32x16_bf16 v[0:15], v[228:231], v[228:231], 0\n"
        "  v_mfma_f32_32x32x16_bf16 v[16:31], v[228:231], v[228:231], 0\n"
        "  v_mfma_f32_32x32x16_bf16 v[32:47], v[228:231], v[228:231], 0\n"
        "  v_mfma_f32_32x32x16_bf16 v[48:63], v[228:231], v[228:231], 0\n"
        "  v_mfma_f32_32x32x16_bf16 v[64:79], v[228:231], v[228:231], 0\n"
        "  v_mfma_f32_32x32x16_bf16 v[80:95], v[228:231], v[228:231], 0\n"
        "  v_mfma_f32_32x32x16_bf16 v[96:111], v[228:231], v[228:231], 0\n"
        "  v_mfma_f32_32x32x16_bf16 v[112:127], v[228:231], v[228:231], 0\n"
        "  v_mfma_f32_32x32x16_bf16 v[128:143], v[228:231], v[228:231], 0\n"
        "  s_mov_b32 s51, 98304\n"
        "  s_mov_b64 exec, s[80:81]\n"
        "  v_mov_b32 v188, 0\n"
        "  v_mov_b32 v189, 0\n"
        "  buffer_atomic_or_x2 v[188:189], v184, s[24:27], 0 offen sc0 sc1\n"
        "  s_mov_b64 exec, -1\n"
        "  s_waitcnt vmcnt(9) lgkmcnt(0)\n"
        "  s_barrier\n"
        "  s_cmp_lg_u32 s46, s47\n"
        "  s_cbranch_scc1 .Law1\n"
        "  ds_read_b128 a[0:3], v160 offset:0\n"
        "  ds_read_b128 a[4:7], v160 offset:2048\n"
        "  ds_read_b128 a[8:11], v160 offset:4096\n"
        "  ds_read_b128 a[12:15], v160 offset:6144\n"
        "  ds_read_b128 a[16:19], v162\n"
        "  ds_read_b64_tr_b16 a[24:25], v164 offset:1024\n"
        "  ds_read_b64_tr_b16 a[26:27], v164 offset:4096\n"
        "  ds_read_b64_tr_b16 a[28:29], v165 offset:1024\n"
        "  ds_read_b64_tr_b16 a[30:31], v165 offset:4096\n"
        "  ds_read_b64_tr_b16 a[32:33], v164 offset:2048\n"
        "  ds_read_b64_tr_b16 a[34:35], v164 offset:5120\n"
        "  ds_read_b64_tr_b16 a[36:37], v165 offset:2048\n"
        "  ds_read_b64_tr_b16 a[38:39], v165 offset:5120\n"
        "  ds_read_b64_tr_b16 a[20:21], v164 offset:0\n"
        "  ds_read_b64_tr_b16 a[22:23], v164 offset:3072\n"
        "  ds_read_b64_tr_b16 v[140:141], v165 offset:0\n"
        "  ds_read_b64_tr_b16 v[142:143], v165 offset:3072\n"
        "  s_mov_b32 s52, 49152\n"
        "  s_waitcnt lgkmcnt(15)  ; auto\n"
        "  v_mfma_f32_16x16x32_bf16 v[124:127], a[0:3], a[0:3], v[124:127]\n"
        "  s_add_u32 m0, s69, s87\n"
        "  ds_read_b128 a[44:47], v161 offset:0\n"
        "  ds_read_b128 a[48:51], v161 offset:2048\n"
        "  v_mfma_f32_16x16x32_bf16 v[128:131], a[4:7], a[4:7], v[128:131]\n"
        "  buffer_load_dwordx4 v156, s[8:11], s68 offen offset:0 lds\n"
        "  s_waitcnt lgkmcnt(14)  ; auto\n"
        "  v_mfma_f32_16x16x32_bf16 v[120:123], a[16:19], a[16:19], v[120:123]\n"
        "  ds_read_b128 a[52:55], v161 offset:4096\n"
        "  s_waitcnt lgkmcnt(13)  ; auto\n"
        "  v_mfma_f32_16x16x32_bf16 v[32:35], a[24:27], a[16:19], v[32:35]\n"
        "  buffer_load_dwordx4 v157, s[8:11], s68 offen offset:1024 lds\n"
        "  s_waitcnt lgkmcnt(11)  ; auto\n"
        "  v_mfma_f32_16x16x32_bf16 v[36:39], a[28:31], a[16:19], v[36:39]\n"
        "  ds_read_b128 a[56:59], v161 offset:6144\n"
        "  v_mfma_f32_16x16x32_bf16 v[40:43], a[24:27], a[0:3], v[40:43]\n"
        "  buffer_load_dwordx4 v158, s[8:11], s68 offen offset:2048 lds\n"
        "  v_mfma_f32_16x16x32_bf16 v[44:47], a[28:31], a[0:3], v[44:47]\n"
        "  s_add_u32 m0, s51, s56\n"
        "  ds_read_b128 a[60:63], v163\n"
        "  s_waitcnt lgkmcnt(11)  ; auto\n"
        "  v_mfma_f32_16x16x32_bf16 v[80:83], a[0:3], a[32:35], v[80:83]\n"
        "  buffer_load_dwordx4 v148, s[4:7], s48 offen offset:0 lds\n"
        "  s_waitcnt lgkmcnt(9)  ; auto\n"
        "  v_mfma_f32_16x16x32_bf16 v[84:87], a[0:3], a[36:39], v[84:87]\n"
        "  s_add_u32 s88, s88, 1\n"
        "  ds_read_b64_tr_b16 a[68:69], v164 offset:13312\n"
        "  ds_read_b64_tr_b16 a[70:71], v164 offset:16384\n"
        "  s_waitcnt lgkmcnt(9)  ; auto\n"
        "  v_mfma_f32_16x16x32_bf16 v[0:3], a[20:23], a[0:3], v[0:3]\n"
        "  buffer_load_dwordx4 v149, s[4:7], s48 offen offset:1024 lds\n"
        "  v_mfma_f32_16x16x32_bf16 v[48:51], a[24:27], a[4:7], v[48:51]\n"
        "  s_cmp_eq_u32 s88, 7\n"
        "  s_cselect_b32 s88, 0, s88\n"
        "  s_cselect_b32 s89, -6, 1\n"
        "  ds_read_b64_tr_b16 a[72:73], v165 offset:13312\n"
        "  v_mfma_f32_16x16x32_bf16 v[52:55], a[28:31], a[4:7], v[52:55]\n"
        "  buffer_load_dwordx4 v150, s[4:7], s48 offen offset:2048 lds\n"
        "  v_mfma_f32_16x16x32_bf16 v[88:91], a[4:7], a[32:35], v[88:91]\n"
        "  ds_read_b64_tr_b16 a[74:75], v165 offset:16384\n"
        "  v_mfma_f32_16x16x32_bf16 v[92:95], a[4:7], a[36:39], v[92:95]\n"
        "  ds_read_b64_tr_b16 a[76:77], v164 offset:14336\n"
        "  v_mfma_f32_16x16x32_bf16 v[8:11], a[20:23], a[4:7], v[8:11]\n"
        "  buffer_load_dwordx4 v151, s[4:7], s48 offen offset:3072 lds\n"
        "  s_waitcnt lgkmcnt(11)  ; auto\n"
        "  v_cndmask_b32_e64 v140, v140, v171, s[66:67]\n"
        "  v_mfma_f32_16x16x32_bf16 v[56:59], a[24:27], a[8:11], v[56:59]\n"
        "  s_add_u32 m0, s51, s57\n"
        "  v_cndmask_b32_e64 v141, v141, v171, s[66:67]\n"
        "  ds_read_b64_tr_b16 a[78:79], v164 offset:17408\n"
        "  v_mfma_f32_16x16x32_bf16 v[60:63], a[28:31], a[8:11], v[60:63]\n"
        "  buffer_load_dwordx4 v153, s[8:11], s50 offen offset:0 lds\n"
        "  s_waitcnt lgkmcnt(11)  ; auto\n"
        "  v_cndmask_b32_e64 v142, v142, v171, s[66:67]\n"
        "  v_mfma_f32_16x16x32_bf16 v[96:99], a[8:11], a[32:35], v[96:99]\n"
        "  s_lshl_b32 s90, s89, 7\n"
        "  s_add_u32 s48, s48, s90\n"
        "  ds_read_b64_tr_b16 a[80:81], v165 offset:14336\n"
        "  ds_read_b64_tr_b16 a[82:83], v165 offset:17408\n"
        "  v_mfma_f32_16x16x32_bf16 v[100:103], a[8:11], a[36:39], v[100:103]\n"
        "  buffer_load_dwordx4 v154, s[8:11], s50 offen offset:1024 lds\n"
        "  v_cndmask_b32_e64 v143, v143, v171, s[66:67]\n"
        "  v_mfma_f32_16x16x32_bf16 v[16:19], a[20:23], a[8:11], v[16:19]\n"
        "  s_mov_b32 s68, s50\n"
        "  s_mov_b32 s69, s51\n"
        "  ds_read_b64_tr_b16 a[64:65], v164 offset:12288\n"
        "  v_mfma_f32_16x16x32_bf16 v[64:67], a[24:27], a[12:15], v[64:67]\n"
        "  buffer_load_dwordx4 v155, s[8:11], s50 offen offset:2048 lds\n"
        "  v_mfma_f32_16x16x32_bf16 v[68:71], a[28:31], a[12:15], v[68:71]\n"
        "  s_add_u32 m0, s51, s58\n"
        "  s_mul_i32 s90, s89, s85\n"
        "  s_add_u32 s50, s50, s90\n"
        "  ds_read_b64_tr_b16 a[66:67], v164 offset:15360\n"
        "  v_mfma_f32_16x16x32_bf16 v[104:107], a[12:15], a[32:35], v[104:107]\n"
        "  s_mov_b64 exec, s[64:65]\n"
        "  buffer_load_dwordx4 v152, s[12:15], s49 offen offset:0 lds\n"
        "  s_mov_b64 exec, -1\n"
        "  v_mfma_f32_16x16x32_bf16 v[108:111], a[12:15], a[36:39], v[108:111]\n"
        "  s_mul_i32 s90, s89, s84\n"
        "  s_add_u32 s49, s49, s90\n"
        "  s_and_b32 s94, %[wg], 127\n"
        "  s_lshl2_add_u32 s94, s94, s40\n"
        "  v_lshlrev_b32 v215, 7, v204\n"
        "  s_mul_i32 s95, s94, 3200\n"
        "  ds_read_b64_tr_b16 v[144:145], v165 offset:12288\n"
        "  v_mfma_f32_16x16x32_bf16 v[24:27], a[20:23], a[12:15], v[24:27]\n"
        "  s_mov_b64 exec, 0x1ffffff\n"
        "  buffer_load_dword v187, v215, s[16:19], s95 offen\n"
        "  s_mov_b64 exec, -1\n"
        "  v_mfma_f32_16x16x32_bf16 v[4:7], v[140:143], a[0:3], v[4:7]\n"
        "  s_mul_i32 s95, s94, 896\n"
        "  ds_read_b64_tr_b16 v[146:147], v165 offset:15360\n"
        "  v_add_u32 v160, s52, v160\n"
        "  v_mfma_f32_16x16x32_bf16 v[12:15], v[140:143], a[4:7], v[12:15]\n"
        "  v_add_u32 v161, s52, v161\n"
        "  v_add_u32 v162, s52, v162\n"
        "  v_add_u32 v163, s52, v163\n"
        "  v_mfma_f32_16x16x32_bf16 v[20:23], v[140:143], a[8:11], v[20:23]\n"
        "  s_mov_b64 exec, 0x7f\n"
        "  buffer_load_dword v187, v215, s[20:23], s95 offen\n"
        "  s_mov_b64 exec, -1\n"
        "  v_mfma_f32_16x16x32_bf16 v[28:31], v[140:143], a[12:15], v[28:31]\n"
        "  v_add_u32 v164, s52, v164\n"
        "  v_add_u32 v165, s52, v165\n"
        "  s_mov_b32 s51, 0\n"
        "  s_mov_b32 s54, 49152\n"
        "  v_mfma_f32_16x16x32_bf16 v[116:119], v[140:143], a[16:19], v[116:119]\n"
        "  s_waitcnt vmcnt(10) lgkmcnt(0)\n"
        "  s_barrier\n"
        "  s_mov_b32 s53, 4\n"
        "  .p2align 3\n"
        ".La1_0:\n"
        "  v_mfma_f32_16x16x32_bf16 v[124:127], a[44:47], a[44:47], v[124:127]\n"
        "  s_add_u32 m0, s69, s87\n"
        "  ds_read_b128 a[0:3], v160 offset:0\n"
        "  v_mfma_f32_16x16x32_bf16 v[128:131], a[48:51], a[48:51], v[128:131]\n"
        "  buffer_load_dwordx4 v156, s[8:11], s68 offen offset:0 lds\n"
        "  v_mfma_f32_16x16x32_bf16 v[120:123], a[60:63], a[60:63], v[120:123]\n"
        "  ds_read_b128 a[4:7], v160 offset:2048\n"
        "  v_mfma_f32_16x16x32_bf16 v[32:35], a[68:71], a[60:63], v[32:35]\n"
        "  ds_read_b128 a[8:11], v160 offset:4096\n"
        "  v_mfma_f32_16x16x32_bf16 v[36:39], a[72:75], a[60:63], v[36:39]\n"
        "  ds_read_b128 a[12:15], v160 offset:6144\n"
        "  v_mfma_f32_16x16x32_bf16 v[40:43], a[68:71], a[44:47], v[40:43]\n"
        "  ds_read_b128 a[16:19], v162\n"
        "  v_mfma_f32_16x16x32_bf16 v[44:47], a[72:75], a[44:47], v[44:47]\n"
        "  buffer_load_dwordx4 v157, s[8:11], s68 offen offset:1024 lds\n"
        "  v_mfma_f32_16x16x32_bf16 v[80:83], a[44:47], a[76:79], v[80:83]\n"
        "  ds_read_b64_tr_b16 a[24:25], v164 offset:1024\n"
        "  v_mfma_f32_16x16x32_bf16 v[84:87], a[44:47], a[80:83], v[84:87]\n"
        "  ds_read_b64_tr_b16 a[26:27], v164 offset:4096\n"
        "  v_mfma_f32_16x16x32_bf16 v[0:3], a[64:67], a[44:47], v[0:3]\n"
        "  ds_read_b64_tr_b16 a[28:29], v165 offset:1024\n"
        "  v_mfma_f32_16x16x32_bf16 v[48:51], a[68:71], a[48:51], v[48:51]\n"
        "  ds_read_b64_tr_b16 a[30:31], v165 offset:4096\n"
        "  v_mfma_f32_16x16x32_bf16 v[52:55], a[72:75], a[48:51], v[52:55]\n"
        "  buffer_load_dwordx4 v158, s[8:11], s68 offen offset:2048 lds\n"
        "  v_mfma_f32_16x16x32_bf16 v[88:91], a[48:51], a[76:79], v[88:91]\n"
        "  s_add_u32 m0, s51, s56\n"
        "  ds_read_b64_tr_b16 a[32:33], v164 offset:2048\n"
        "  v_mfma_f32_16x16x32_bf16 v[92:95], a[48:51], a[80:83], v[92:95]\n"
        "  ds_read_b64_tr_b16 a[34:35], v164 offset:5120\n"
        "  v_mfma_f32_16x16x32_bf16 v[8:11], a[64:67], a[48:51], v[8:11]\n"
        "  v_cndmask_b32_e64 v144, v144, v171, s[66:67]\n"
        "  v_cndmask_b32_e64 v145, v145, v171, s[66:67]\n"
        "  ds_read_b64_tr_b16 a[36:37], v165 offset:2048\n"
        "  v_mfma_f32_16x16x32_bf16 v[56:59], a[68:71], a[52:55], v[56:59]\n"
        "  v_cndmask_b32_e64 v146, v146, v171, s[66:67]\n"
        "  v_cndmask_b32_e64 v147, v147, v171, s[66:67]\n"
        "  ds_read_b64_tr_b16 a[38:39], v165 offset:5120\n"
        "  v_mfma_f32_16x16x32_bf16 v[60:63], a[72:75], a[52:55], v[60:63]\n"
        "  buffer_load_dwordx4 v148, s[4:7], s48 offen offset:0 lds\n"
        "  v_mfma_f32_16x16x32_bf16 v[96:99], a[52:55], a[76:79], v[96:99]\n"
        "  s_add_u32 s88, s88, 1\n"
        "  ds_read_b64_tr_b16 a[20:21], v164 offset:0\n"
        "  v_mfma_f32_16x16x32_bf16 v[100:103], a[52:55], a[80:83], v[100:103]\n"
        "  ds_read_b64_tr_b16 a[22:23], v164 offset:3072\n"
        "  v_mfma_f32_16x16x32_bf16 v[16:19], a[64:67], a[52:55], v[16:19]\n"
        "  ds_read_b64_tr_b16 v[140:141], v165 offset:0\n"
        "  v_mfma_f32_16x16x32_bf16 v[64:67], a[68:71], a[56:59], v[64:67]\n"
        "  ds_read_b64_tr_b16 v[142:143], v165 offset:3072\n"
        "  v_mfma_f32_16x16x32_bf16 v[68:71], a[72:75], a[56:59], v[68:71]\n"
        "  ds_read_b64_tr_b16 a[68:69], v164 offset:13312\n"
        "  v_mfma_f32_16x16x32_bf16 v[104:107], a[56:59], a[76:79], v[104:107]\n"
        "  buffer_load_dwordx4 v149, s[4:7], s48 offen offset:1024 lds\n"
        "  v_mfma_f32_16x16x32_bf16 v[108:111], a[56:59], a[80:83], v[108:111]\n"
        "  s_cmp_eq_u32 s88, 7\n"
        "  s_cselect_b32 s88, 0, s88\n"
        "  s_cselect_b32 s89, -6, 1\n"
        "  ds_read_b64_tr_b16 a[70:71], v164 offset:16384\n"
        "  v_mfma_f32_16x16x32_bf16 v[24:27], a[64:67], a[56:59], v[24:27]\n"
        "  ds_read_b64_tr_b16 a[72:73], v165 offset:13312\n"
        "  v_mfma_f32_16x16x32_bf16 v[4:7], v[144:147], a[44:47], v[4:7]\n"
        "  ds_read_b64_tr_b16 a[74:75], v165 offset:16384\n"
        "  s_add_u32 s55, s54, 49152\n"
        "  v_mfma_f32_16x16x32_bf16 v[12:15], v[144:147], a[48:51], v[12:15]\n"
        "  ds_read_b64_tr_b16 a[76:77], v164 offset:14336\n"
        "  v_mfma_f32_16x16x32_bf16 v[20:23], v[144:147], a[52:55], v[20:23]\n"
        "  buffer_load_dwordx4 v150, s[4:7], s48 offen offset:2048 lds\n"
        "  s_cmp_eq_u32 s54, 98304\n"
        "  s_cselect_b32 s55, 0, s55\n"
        "  v_mfma_f32_16x16x32_bf16 v[28:31], v[144:147], a[56:59], v[28:31]\n"
        "  ds_read_b64_tr_b16 a[78:79], v164 offset:17408\n"
        "  s_sub_u32 s52, s55, s54\n"
        "  v_mfma_f32_16x16x32_bf16 v[116:119], v[144:147], a[60:63], v[116:119]\n"
        "  ds_read_b64_tr_b16 a[80:81], v165 offset:14336\n"
        "  s_waitcnt lgkmcnt(7)\n"
        "  v_mfma_f32_16x16x32_bf16 v[124:127], a[0:3], a[0:3], v[124:127]\n"
        "  ds_read_b64_tr_b16 a[82:83], v165 offset:17408\n"
        "  v_mfma_f32_16x16x32_bf16 v[128:131], a[4:7], a[4:7], v[128:131]\n"
        "  ds_read_b64_tr_b16 a[64:65], v164 offset:12288\n"
        "  v_mfma_f32_16x16x32_bf16 v[120:123], a[16:19], a[16:19], v[120:123]\n"
        "  buffer_load_dwordx4 v151, s[4:7], s48 offen offset:3072 lds\n"
        "  v_mfma_f32_16x16x32_bf16 v[32:35], a[24:27], a[16:19], v[32:35]\n"
        "  s_add_u32 m0, s51, s57\n"
        "  ds_read_b64_tr_b16 a[66:67], v164 offset:15360\n"
        "  v_mfma_f32_16x16x32_bf16 v[36:39], a[28:31], a[16:19], v[36:39]\n"
        "  ds_read_b128 a[44:47], v161 offset:0\n"
        "  v_mfma_f32_16x16x32_bf16 v[40:43], a[24:27], a[0:3], v[40:43]\n"
        "  ds_read_b128 a[48:51], v161 offset:2048\n"
        "  v_mfma_f32_16x16x32_bf16 v[44:47], a[28:31], a[0:3], v[44:47]\n"
        "  ds_read_b128 a[52:55], v161 offset:4096\n"
        "  v_mfma_f32_16x16x32_bf16 v[80:83], a[0:3], a[32:35], v[80:83]\n"
        "  ds_read_b128 a[56:59], v161 offset:6144\n"
        "  v_mfma_f32_16x16x32_bf16 v[84:87], a[0:3], a[36:39], v[84:87]\n"
        "  buffer_load_dwordx4 v153, s[8:11], s50 offen offset:0 lds\n"
        "  v_mfma_f32_16x16x32_bf16 v[0:3], a[20:23], a[0:3], v[0:3]\n"
        "  s_lshl_b32 s90, s89, 7\n"
        "  ds_read_b128 a[60:63], v163\n"
        "  v_mfma_f32_16x16x32_bf16 v[48:51], a[24:27], a[4:7], v[48:51]\n"
        "  ds_read_b64_tr_b16 v[144:145], v165 offset:12288\n"
        "  v_mfma_f32_16x16x32_bf16 v[52:55], a[28:31], a[4:7], v[52:55]\n"
        "  s_add_u32 s48, s48, s90\n"
        "  ds_read_b64_tr_b16 v[146:147], v165 offset:15360\n"
        "  v_add_u32 v160, s52, v160\n"
        "  v_mfma_f32_16x16x32_bf16 v[88:91], a[4:7], a[32:35], v[88:91]\n"
        "  v_add_u32 v161, s52, v161\n"
        "  v_add_u32 v162, s52, v162\n"
        "  v_add_u32 v163, s52, v163\n"
        "  v_mfma_f32_16x16x32_bf16 v[92:95], a[4:7], a[36:39], v[92:95]\n"
        "  buffer_load_dwordx4 v154, s[8:11], s50 offen offset:1024 lds\n"
        "  v_add_u32 v164, s52, v164\n"
        "  v_add_u32 v165, s52, v165\n"
        "  v_mfma_f32_16x16x32_bf16 v[8:11], a[20:23], a[4:7], v[8:11]\n"
        "  s_mov_b32 s68, s50\n"
        "  v_cndmask_b32_e64 v140, v140, v171, s[66:67]\n"
        "  v_mfma_f32_16x16x32_bf16 v[56:59], a[24:27], a[8:11], v[56:59]\n"
        "  v_cndmask_b32_e64 v141, v141, v171, s[66:67]\n"
        "  v_cndmask_b32_e64 v142, v142, v171, s[66:67]\n"
        "  v_mfma_f32_16x16x32_bf16 v[60:63], a[28:31], a[8:11], v[60:63]\n"
        "  s_mov_b32 s69, s51\n"
        "  v_cndmask_b32_e64 v143, v143, v171, s[66:67]\n"
        "  v_mfma_f32_16x16x32_bf16 v[96:99], a[8:11], a[32:35], v[96:99]\n"
        "  v_mfma_f32_16x16x32_bf16 v[100:103], a[8:11], a[36:39], v[100:103]\n"
        "  buffer_load_dwordx4 v155, s[8:11], s50 offen offset:2048 lds\n"
        "  v_mfma_f32_16x16x32_bf16 v[16:19], a[20:23], a[8:11], v[16:19]\n"
        "  s_add_u32 m0, s51, s58\n"
        "  v_mfma_f32_16x16x32_bf16 v[64:67], a[24:27], a[12:15], v[64:67]\n"
        "  s_mul_i32 s90, s89, s85\n"
        "  v_mfma_f32_16x16x32_bf16 v[68:71], a[28:31], a[12:15], v[68:71]\n"
        "  v_mfma_f32_16x16x32_bf16 v[104:107], a[12:15], a[32:35], v[104:107]\n"
        "  s_add_u32 s50, s50, s90\n"
        "  v_mfma_f32_16x16x32_bf16 v[108:111], a[12:15], a[36:39], v[108:111]\n"
        "  v_mfma_f32_16x16x32_bf16 v[24:27], a[20:23], a[12:15], v[24:27]\n"
        "  s_mov_b64 exec, s[64:65]\n"
        "  buffer_load_dwordx4 v152, s[12:15], s49 offen offset:0 lds\n"
        "  s_mov_b64 exec, -1\n"
        "  s_mul_i32 s90, s89, s84\n"
        "  v_mfma_f32_16x16x32_bf16 v[4:7], v[140:143], a[0:3], v[4:7]\n"
        "  s_add_u32 s49, s49, s90\n"
        "  v_mfma_f32_16x16x32_bf16 v[12:15], v[140:143], a[4:7], v[12:15]\n"
        "  v_mfma_f32_16x16x32_bf16 v[20:23], v[140:143], a[8:11], v[20:23]\n"
        "  s_mov_b32 s51, s54\n"
        "  s_mov_b32 s54, s55\n"
        "  v_mfma_f32_16x16x32_bf16 v[28:31], v[140:143], a[12:15], v[28:31]\n"
        "  s_sub_u32 s53, s53, 1\n"
        "  s_cmp_lg_u32 s53, 0\n"
        "  v_mfma_f32_16x16x32_bf16 v[116:119], v[140:143], a[16:19], v[116:119]\n"
        "  s_waitcnt vmcnt(8) lgkmcnt(0)\n"
        "  s_barrier\n"
        "  s_cbranch_scc1 .La1_0\n"
        "  v_mfma_f32_16x16x32_bf16 v[124:127], a[44:47], a[44:47], v[124:127]\n"
        "  s_lshr_b32 s94, s63, 7\n"
        "  s_add_u32 s89, s40, s94\n"
        "  s_and_b32 s89, s89, 3\n"
        "  ds_read_b128 a[0:3], v160 offset:0\n"
        "  v_mfma_f32_16x16x32_bf16 v[128:131], a[48:51], a[48:51], v[128:131]\n"
        "  s_mov_b32 s84, 0\n"
        "  s_mov_b32 s85, s84\n"
        "  s_lshl_b32 s88, s96, 7\n"
        "  ds_read_b128 a[4:7], v160 offset:2048\n"
        "  v_mfma_f32_16x16x32_bf16 v[120:123], a[60:63], a[60:63], v[120:123]\n"
        "  s_add_u32 s88, s88, s97\n"
        "  s_lshl_b32 s95, s89, 5\n"
        "  s_add_u32 s88, s88, s95\n"
        "  ds_read_b128 a[8:11], v160 offset:4096\n"
        "  v_mfma_f32_16x16x32_bf16 v[32:35], a[68:71], a[60:63], v[32:35]\n"
        "  s_add_u32 m0, s69, s87\n"
        "  ds_read_b128 a[12:15], v160 offset:6144\n"
        "  v_mfma_f32_16x16x32_bf16 v[36:39], a[72:75], a[60:63], v[36:39]\n"
        "  buffer_load_dwordx4 v156, s[8:11], s68 offen offset:0 lds\n"
        "  v_mfma_f32_16x16x32_bf16 v[40:43], a[68:71], a[44:47], v[40:43]\n"
        "  ds_read_b128 a[16:19], v162\n"
        "  v_mfma_f32_16x16x32_bf16 v[44:47], a[72:75], a[44:47], v[44:47]\n"
        "  ds_read_b64_tr_b16 a[24:25], v164 offset:1024\n"
        "  v_mfma_f32_16x16x32_bf16 v[80:83], a[44:47], a[76:79], v[80:83]\n"
        "  buffer_load_dwordx4 v157, s[8:11], s68 offen offset:1024 lds\n"
        "  v_mfma_f32_16x16x32_bf16 v[84:87], a[44:47], a[80:83], v[84:87]\n"
        "  ds_read_b64_tr_b16 a[26:27], v164 offset:4096\n"
        "  v_cndmask_b32_e64 v186, v189, v188, s[76:77]\n"
        "  v_mfma_f32_16x16x32_bf16 v[0:3], a[64:67], a[44:47], v[0:3]\n"
        "  ds_read_b64_tr_b16 a[28:29], v165 offset:1024\n"
        "  v_add_u32 v186, 1, v186\n"
        "  v_mfma_f32_16x16x32_bf16 v[48:51], a[68:71], a[48:51], v[48:51]\n"
        "  ds_read_b64_tr_b16 a[30:31], v165 offset:4096\n"
        "  v_mov_b32 v191, v186\n"
        "  v_mfma_f32_16x16x32_bf16 v[52:55], a[72:75], a[48:51], v[52:55]\n"
        "  buffer_load_dwordx4 v158, s[8:11], s68 offen offset:2048 lds\n"
        "  s_mov_b64 exec, 15\n"
        "  buffer_store_dword v186, v185, s[24:27], 0 offen sc1\n"
        "  s_mov_b64 exec, -1\n"
        "  v_mfma_f32_16x16x32_bf16 v[88:91], a[48:51], a[76:79], v[88:91]\n"
        "  s_add_u32 m0, s59, 49152\n"
        "  ds_read_b64_tr_b16 a[32:33], v164 offset:2048\n"
        "  v_mfma_f32_16x16x32_bf16 v[92:95], a[48:51], a[80:83], v[92:95]\n"
        "  ds_read_b64_tr_b16 a[34:35], v164 offset:5120\n"
        "  v_mfma_f32_16x16x32_bf16 v[8:11], a[64:67], a[48:51], v[8:11]\n"
        "  buffer_load_dwordx4 v172, s[16:19], s60 offen offset:0 lds\n"
        "  v_cndmask_b32_e64 v144, v144, v171, s[66:67]\n"
        "  v_mfma_f32_16x16x32_bf16 v[56:59], a[68:71], a[52:55], v[56:59]\n"
        "  v_cndmask_b32_e64 v145, v145, v171, s[66:67]\n"
        "  v_cndmask_b32_e64 v146, v146, v171, s[66:67]\n"
        "  ds_read_b64_tr_b16 a[36:37], v165 offset:2048\n"
        "  v_mfma_f32_16x16x32_bf16 v[60:63], a[72:75], a[52:55], v[60:63]\n"
        "  v_cndmask_b32_e64 v147, v147, v171, s[66:67]\n"
        "  ds_read_b64_tr_b16 a[38:39], v165 offset:5120\n"
        "  v_mfma_f32_16x16x32_bf16 v[96:99], a[52:55], a[76:79], v[96:99]\n"
        "  ds_read_b64_tr_b16 a[20:21], v164 offset:0\n"
        "  v_mfma_f32_16x16x32_bf16 v[100:103], a[52:55], a[80:83], v[100:103]\n"
        "  buffer_load_dwordx4 v173, s[16:19], s60 offen offset:1024 lds\n"
        "  v_mfma_f32_16x16x32_bf16 v[16:19], a[64:67], a[52:55], v[16:19]\n"
        "  ds_read_b64_tr_b16 a[22:23], v164 offset:3072\n"
        "  v_mfma_f32_16x16x32_bf16 v[64:67], a[68:71], a[56:59], v[64:67]\n"
        "  ds_read_b64_tr_b16 v[140:141], v165 offset:0\n"
        "  v_mfma_f32_16x16x32_bf16 v[68:71], a[72:75], a[56:59], v[68:71]\n"
        "  buffer_load_dwordx4 v174, s[16:19], s60 offen offset:2048 lds\n"
        "  v_mfma_f32_16x16x32_bf16 v[104:107], a[56:59], a[76:79], v[104:107]\n"
        "  ds_read_b64_tr_b16 v[142:143], v165 offset:3072\n"
        "  v_mfma_f32_16x16x32_bf16 v[108:111], a[56:59], a[80:83], v[108:111]\n"
        "  ds_read_b64_tr_b16 a[68:69], v164 offset:13312\n"
        "  v_mfma_f32_16x16x32_bf16 v[24:27], a[64:67], a[56:59], v[24:27]\n"
        "  ds_read_b64_tr_b16 a[70:71], v164 offset:16384\n"
        "  v_mfma_f32_16x16x32_bf16 v[4:7], v[144:147], a[44:47], v[4:7]\n"
        "  buffer_load_dwordx4 v175, s[16:19], s60 offen offset:3072 lds\n"
        "  s_add_u32 s55, s54, 49152\n"
        "  v_mfma_f32_16x16x32_bf16 v[12:15], v[144:147], a[48:51], v[12:15]\n"
        "  s_add_u32 s60, s60, 28672\n"
        "  ds_read_b64_tr_b16 a[72:73], v165 offset:13312\n"
        "  v_mfma_f32_16x16x32_bf16 v[20:23], v[144:147], a[52:55], v[20:23]\n"
        "  s_add_u32 m0, s59, 53248\n"
        "  ds_read_b64_tr_b16 a[74:75], v165 offset:16384\n"
        "  s_cmp_eq_u32 s54, 98304\n"
        "  s_cselect_b32 s55, 0, s55\n"
        "  v_mfma_f32_16x16x32_bf16 v[28:31], v[144:147], a[56:59], v[28:31]\n"
        "  ds_read_b64_tr_b16 a[76:77], v164 offset:14336\n"
        "  s_sub_u32 s52, s55, s54\n"
        "  v_mfma_f32_16x16x32_bf16 v[116:119], v[144:147], a[60:63], v[116:119]\n"
        "  buffer_load_dwordx4 v172, s[16:19], s60 offen offset:0 lds\n"
        "  s_waitcnt lgkmcnt(5)\n"
        "  v_mfma_f32_16x16x32_bf16 v[124:127], a[0:3], a[0:3], v[124:127]\n"
        "  ds_read_b64_tr_b16 a[78:79], v164 offset:17408\n"
        "  v_mfma_f32_16x16x32_bf16 v[128:131], a[4:7], a[4:7], v[128:131]\n"
        "  ds_read_b64_tr_b16 a[80:81], v165 offset:14336\n"
        "  v_mfma_f32_16x16x32_bf16 v[120:123], a[16:19], a[16:19], v[120:123]\n"
        "  buffer_load_dwordx4 v173, s[16:19], s60 offen offset:1024 lds\n"
        "  v_mfma_f32_16x16x32_bf16 v[32:35], a[24:27], a[16:19], v[32:35]\n"
        "  ds_read_b64_tr_b16 a[82:83], v165 offset:17408\n"
        "  v_mfma_f32_16x16x32_bf16 v[36:39], a[28:31], a[16:19], v[36:39]\n"
        "  ds_read_b64_tr_b16 a[64:65], v164 offset:12288\n"
        "  v_mfma_f32_16x16x32_bf16 v[40:43], a[24:27], a[0:3], v[40:43]\n"
        "  ds_read_b64_tr_b16 a[66:67], v164 offset:15360\n"
        "  v_mfma_f32_16x16x32_bf16 v[44:47], a[28:31], a[0:3], v[44:47]\n"
        "  buffer_load_dwordx4 v174, s[16:19], s60 offen offset:2048 lds\n"
        "  v_mfma_f32_16x16x32_bf16 v[80:83], a[0:3], a[32:35], v[80:83]\n"
        "  ds_read_b128 a[44:47], v161 offset:0\n"
        "  v_mfma_f32_16x16x32_bf16 v[84:87], a[0:3], a[36:39], v[84:87]\n"
        "  ds_read_b128 a[48:51], v161 offset:2048\n"
        "  v_mfma_f32_16x16x32_bf16 v[0:3], a[20:23], a[0:3], v[0:3]\n"
        "  buffer_load_dwordx4 v175, s[16:19], s60 offen offset:3072 lds\n"
        "  v_mfma_f32_16x16x32_bf16 v[48:51], a[24:27], a[4:7], v[48:51]\n"
        "  s_add_u32 s60, s60, 28672\n"
        "  ds_read_b128 a[52:55], v161 offset:4096\n"
        "  v_mfma_f32_16x16x32_bf16 v[52:55], a[28:31], a[4:7], v[52:55]\n"
        "  s_add_u32 m0, s59, 57344\n"
        "  ds_read_b128 a[56:59], v161 offset:6144\n"
        "  v_mfma_f32_16x16x32_bf16 v[88:91], a[4:7], a[32:35], v[88:91]\n"
        "  ds_read_b128 a[60:63], v163\n"
        "  v_mfma_f32_16x16x32_bf16 v[92:95], a[4:7], a[36:39], v[92:95]\n"
        "  buffer_load_dwordx4 v172, s[16:19], s60 offen offset:0 lds\n"
        "  v_mfma_f32_16x16x32_bf16 v[8:11], a[20:23], a[4:7], v[8:11]\n"
        "  ds_read_b64_tr_b16 v[144:145], v165 offset:12288\n"
        "  v_cndmask_b32_e64 v140, v140, v171, s[66:67]\n"
        "  v_mfma_f32_16x16x32_bf16 v[56:59], a[24:27], a[8:11], v[56:59]\n"
        "  ds_read_b64_tr_b16 v[146:147], v165 offset:15360\n"
        "  v_cndmask_b32_e64 v141, v141, v171, s[66:67]\n"
        "  v_add_u32 v160, s52, v160\n"
        "  v_mfma_f32_16x16x32_bf16 v[60:63], a[28:31], a[8:11], v[60:63]\n"
        "  buffer_load_dwordx4 v173, s[16:19], s60 offen offset:1024 lds\n"
        "  v_cndmask_b32_e64 v142, v142, v171, s[66:67]\n"
        "  v_add_u32 v161, s52, v161\n"
        "  v_mfma_f32_16x16x32_bf16 v[96:99], a[8:11], a[32:35], v[96:99]\n"
        "  v_cndmask_b32_e64 v143, v143, v171, s[66:67]\n"
        "  v_add_u32 v162, s52, v162\n"
        "  v_add_u32 v163, s52, v163\n"
        "  v_mfma_f32_16x16x32_bf16 v[100:103], a[8:11], a[36:39], v[100:103]\n"
        "  v_add_u32 v164, s52, v164\n"
        "  v_add_u32 v165, s52, v165\n"
        "  v_mfma_f32_16x16x32_bf16 v[16:19], a[20:23], a[8:11], v[16:19]\n"
        "  v_mfma_f32_16x16x32_bf16 v[64:67], a[24:27], a[12:15], v[64:67]\n"
        "  buffer_load_dwordx4 v174, s[16:19], s60 offen offset:2048 lds\n"
        "  v_mfma_f32_16x16x32_bf16 v[68:71], a[28:31], a[12:15], v[68:71]\n"
        "  v_mfma_f32_16x16x32_bf16 v[104:107], a[12:15], a[32:35], v[104:107]\n"
        "  v_mfma_f32_16x16x32_bf16 v[108:111], a[12:15], a[36:39], v[108:111]\n"
        "  v_mfma_f32_16x16x32_bf16 v[24:27], a[20:23], a[12:15], v[24:27]\n"
        "  buffer_load_dwordx4 v175, s[16:19], s60 offen offset:3072 lds\n"
        "  s_add_u32 s60, s60, 28672\n"
        "  v_mfma_f32_16x16x32_bf16 v[4:7], v[140:143], a[0:3], v[4:7]\n"
        "  v_mfma_f32_16x16x32_bf16 v[12:15], v[140:143], a[4:7], v[12:15]\n"
        "  v_mfma_f32_16x16x32_bf16 v[20:23], v[140:143], a[8:11], v[20:23]\n"
        "  s_mov_b32 s51, s54\n"
        "  s_mov_b32 s54, s55\n"
        "  v_mfma_f32_16x16x32_bf16 v[28:31], v[140:143], a[12:15], v[28:31]\n"
        "  v_mfma_f32_16x16x32_bf16 v[116:119], v[140:143], a[16:19], v[116:119]\n"
        "  s_waitcnt vmcnt(12) lgkmcnt(0)\n"
        "  s_barrier\n"
        "  v_mfma_f32_16x16x32_bf16 v[124:127], a[44:47], a[44:47], v[124:127]\n"
        "  s_add_u32 m0, s59, 98304\n"
        "  ds_read_b128 a[0:3], v160 offset:0\n"
        "  ds_read_b128 a[4:7], v160 offset:2048\n"
        "  v_mfma_f32_16x16x32_bf16 v[128:131], a[48:51], a[48:51], v[128:131]\n"
        "  buffer_load_dwordx4 v172, s[16:19], s60 offen offset:0 lds\n"
        "  v_mfma_f32_16x16x32_bf16 v[120:123], a[60:63], a[60:63], v[120:123]\n"
        "  ds_read_b128 a[8:11], v160 offset:4096\n"
        "  v_mfma_f32_16x16x32_bf16 v[32:35], a[68:71], a[60:63], v[32:35]\n"
        "  ds_read_b128 a[12:15], v160 offset:6144\n"
        "  v_mfma_f32_16x16x32_bf16 v[36:39], a[72:75], a[60:63], v[36:39]\n"
        "  buffer_load_dwordx4 v173, s[16:19], s60 offen offset:1024 lds\n"
        "  v_mfma_f32_16x16x32_bf16 v[40:43], a[68:71], a[44:47], v[40:43]\n"
        "  ds_read_b128 a[16:19], v162\n"
        "  v_mfma_f32_16x16x32_bf16 v[44:47], a[72:75], a[44:47], v[44:47]\n"
        "  ds_read_b64_tr_b16 a[24:25], v164 offset:1024\n"
        "  v_mfma_f32_16x16x32_bf16 v[80:83], a[44:47], a[76:79], v[80:83]\n"
        "  ds_read_b64_tr_b16 a[26:27], v164 offset:4096\n"
        "  v_mfma_f32_16x16x32_bf16 v[84:87], a[44:47], a[80:83], v[84:87]\n"
        "  buffer_load_dwordx4 v174, s[16:19], s60 offen offset:2048 lds\n"
        "  v_mfma_f32_16x16x32_bf16 v[0:3], a[64:67], a[44:47], v[0:3]\n"
        "  ds_read_b64_tr_b16 a[28:29], v165 offset:1024\n"
        "  v_mfma_f32_16x16x32_bf16 v[48:51], a[68:71], a[48:51], v[48:51]\n"
        "  ds_read_b64_tr_b16 a[30:31], v165 offset:4096\n"
        "  v_mfma_f32_16x16x32_bf16 v[52:55], a[72:75], a[48:51], v[52:55]\n"
        "  ds_read_b64_tr_b16 a[32:33], v164 offset:2048\n"
        "  v_mfma_f32_16x16x32_bf16 v[88:91], a[48:51], a[76:79], v[88:91]\n"
        "  buffer_load_dwordx4 v175, s[16:19], s60 offen offset:3072 lds\n"
        "  v_mfma_f32_16x16x32_bf16 v[92:95], a[48:51], a[80:83], v[92:95]\n"
        "  s_add_u32 s60, s60, 28672\n"
        "  ds_read_b64_tr_b16 a[34:35], v164 offset:5120\n"
        "  v_mfma_f32_16x16x32_bf16 v[8:11], a[64:67], a[48:51], v[8:11]\n"
        "  s_add_u32 m0, s59, 102400\n"
        "  v_cndmask_b32_e64 v144, v144, v171, s[66:67]\n"
        "  ds_read_b64_tr_b16 a[36:37], v165 offset:2048\n"
        "  ds_read_b64_tr_b16 a[38:39], v165 offset:5120\n"
        "  v_mfma_f32_16x16x32_bf16 v[56:59], a[68:71], a[52:55], v[56:59]\n"
        "  buffer_load_dwordx4 v172, s[16:19], s60 offen offset:0 lds\n"
        "  v_cndmask_b32_e64 v145, v145, v171, s[66:67]\n"
        "  v_mfma_f32_16x16x32_bf16 v[60:63], a[72:75], a[52:55], v[60:63]\n"
        "  v_cndmask_b32_e64 v146, v146, v171, s[66:67]\n"
        "  v_cndmask_b32_e64 v147, v147, v171, s[66:67]\n"
        "  ds_read_b64_tr_b16 a[20:21], v164 offset:0\n"
        "  v_mfma_f32_16x16x32_bf16 v[96:99], a[52:55], a[76:79], v[96:99]\n"
        "  ds_read_b64_tr_b16 a[22:23], v164 offset:3072\n"
        "  v_mfma_f32_16x16x32_bf16 v[100:103], a[52:55], a[80:83], v[100:103]\n"
        "  ds_read_b64_tr_b16 v[140:141], v165 offset:0\n"
        "  v_mfma_f32_16x16x32_bf16 v[16:19], a[64:67], a[52:55], v[16:19]\n"
        "  buffer_load_dwordx4 v173, s[16:19], s60 offen offset:1024 lds\n"
        "  v_mfma_f32_16x16x32_bf16 v[64:67], a[68:71], a[56:59], v[64:67]\n"
        "  ds_read_b64_tr_b16 v[142:143], v165 offset:3072\n"
        "  v_mfma_f32_16x16x32_bf16 v[68:71], a[72:75], a[56:59], v[68:71]\n"
        "  ds_read_b64_tr_b16 a[68:69], v164 offset:13312\n"
        "  v_mfma_f32_16x16x32_bf16 v[104:107], a[56:59], a[76:79], v[104:107]\n"
        "  ds_read_b64_tr_b16 a[70:71], v164 offset:16384\n"
        "  v_mfma_f32_16x16x32_bf16 v[108:111], a[56:59], a[80:83], v[108:111]\n"
        "  buffer_load_dwordx4 v174, s[16:19], s60 offen offset:2048 lds\n"
        "  v_mfma_f32_16x16x32_bf16 v[24:27], a[64:67], a[56:59], v[24:27]\n"
        "  ds_read_b64_tr_b16 a[72:73], v165 offset:13312\n"
        "  v_mfma_f32_16x16x32_bf16 v[4:7], v[144:147], a[44:47], v[4:7]\n"
        "  ds_read_b64_tr_b16 a[74:75], v165 offset:16384\n"
        "  s_add_u32 s55, s54, 49152\n"
        "  v_mfma_f32_16x16x32_bf16 v[12:15], v[144:147], a[48:51], v[12:15]\n"
        "  ds_read_b64_tr_b16 a[76:77], v164 offset:14336\n"
        "  v_mfma_f32_16x16x32_bf16 v[20:23], v[144:147], a[52:55], v[20:23]\n"
        "  buffer_load_dwordx4 v175, s[16:19], s60 offen offset:3072 lds\n"
        "  s_cmp_eq_u32 s54, 98304\n"
        "  s_cselect_b32 s55, 0, s55\n"
        "  v_mfma_f32_16x16x32_bf16 v[28:31], v[144:147], a[56:59], v[28:31]\n"
        "  s_add_u32 s60, s60, 28672\n"
        "  ds_read_b64_tr_b16 a[78:79], v164 offset:17408\n"
        "  ds_read_b64_tr_b16 a[80:81], v165 offset:14336\n"
        "  s_sub_u32 s52, s55, s54\n"
        "  v_mfma_f32_16x16x32_bf16 v[116:119], v[144:147], a[60:63], v[116:119]\n"
        "  s_add_u32 m0, s59, 106496\n"
        "  ds_read_b64_tr_b16 a[82:83], v165 offset:17408\n"
        "  s_waitcnt lgkmcnt(8)\n"
        "  v_mfma_f32_16x16x32_bf16 v[124:127], a[0:3], a[0:3], v[124:127]\n"
        "  buffer_load_dwordx4 v172, s[16:19], s60 offen offset:0 lds\n"
        "  v_cndmask_b32_e64 v140, v140, v171, s[66:67]\n"
        "  v_cndmask_b32_e64 v141, v141, v171, s[66:67]\n"
        "  v_cndmask_b32_e64 v142, v142, v171, s[66:67]\n"
        "  v_cndmask_b32_e64 v143, v143, v171, s[66:67]\n"
        "  v_mfma_f32_16x16x32_bf16 v[128:131], a[4:7], a[4:7], v[128:131]\n"
        "  ds_read_b64_tr_b16 a[64:65], v164 offset:12288\n"
        "  v_mfma_f32_16x16x32_bf16 v[120:123], a[16:19], a[16:19], v[120:123]\n"
        "  ds_read_b64_tr_b16 a[66:67], v164 offset:15360\n"
        "  v_mfma_f32_16x16x32_bf16 v[32:35], a[24:27], a[16:19], v[32:35]\n"
        "  ds_read_b128 a[44:47], v161 offset:0\n"
        "  v_mfma_f32_16x16x32_bf16 v[36:39], a[28:31], a[16:19], v[36:39]\n"
        "  buffer_load_dwordx4 v173, s[16:19], s60 offen offset:1024 lds\n"
        "  v_mfma_f32_16x16x32_bf16 v[4:7], v[140:143], a[0:3], v[4:7]\n"
        "  ds_read_b128 a[48:51], v161 offset:2048\n"
        "  v_mfma_f32_16x16x32_bf16 v[12:15], v[140:143], a[4:7], v[12:15]\n"
        "  ds_read_b128 a[52:55], v161 offset:4096\n"
        "  v_mfma_f32_16x16x32_bf16 v[20:23], v[140:143], a[8:11], v[20:23]\n"
        "  ds_read_b128 a[56:59], v161 offset:6144\n"
        "  v_mfma_f32_16x16x32_bf16 v[28:31], v[140:143], a[12:15], v[28:31]\n"
        "  buffer_load_dwordx4 v174, s[16:19], s60 offen offset:2048 lds\n"
        "  v_mfma_f32_16x16x32_bf16 v[116:119], v[140:143], a[16:19], v[116:119]\n"
        "  ds_read_b128 a[60:63], v163\n"
        "  v_mfma_f32_16x16x32_bf16 v[40:43], a[24:27], a[0:3], v[40:43]\n"
        "  ds_read_b64_tr_b16 v[144:145], v165 offset:12288\n"
        "  v_mfma_f32_16x16x32_bf16 v[44:47], a[28:31], a[0:3], v[44:47]\n"
        "  ds_read_b64_tr_b16 v[146:147], v165 offset:15360\n"
        "  v_mfma_f32_16x16x32_bf16 v[80:83], a[0:3], a[32:35], v[80:83]\n"
        "  buffer_load_dwordx4 v175, s[16:19], s60 offen offset:3072 lds\n"
        "  s_add_u32 s60, s60, 28672\n"
        "  v_mfma_f32_16x16x32_bf16 v[84:87], a[0:3], a[36:39], v[84:87]\n"
        "  v_mfma_f32_16x16x32_bf16 v[0:3], a[20:23], a[0:3], v[0:3]\n"
        "  v_mfma_f32_16x16x32_bf16 v[48:51], a[24:27], a[4:7], v[48:51]\n"
        "  s_mov_b32 s51, s54\n"
        "  s_mov_b32 s54, s55\n"
        "  v_mfma_f32_16x16x32_bf16 v[52:55], a[28:31], a[4:7], v[52:55]\n"
        "  v_mfma_f32_16x16x32_bf16 v[88:91], a[4:7], a[32:35], v[88:91]\n"
        "  s_waitcnt lgkmcnt(0)\n"
        "  s_barrier\n"
        "  v_mfma_f32_16x16x32_bf16 v[124:127], a[44:47], a[44:47], v[124:127]\n"
        "  v_cndmask_b32_e64 v144, v144, v171, s[66:67]\n"
        "  v_cndmask_b32_e64 v145, v145, v171, s[66:67]\n"
        "  v_mfma_f32_16x16x32_bf16 v[128:131], a[48:51], a[48:51], v[128:131]\n"
        "  v_cndmask_b32_e64 v146, v146, v171, s[66:67]\n"
        "  v_cndmask_b32_e64 v147, v147, v171, s[66:67]\n"
        "  v_mfma_f32_16x16x32_bf16 v[120:123], a[60:63], a[60:63], v[120:123]\n"
        "  s_lshl_b32 s54, s46, 8\n"
        "  s_add_u32 s54, s54, 147456\n"
        "  v_mfma_f32_16x16x32_bf16 v[32:35], a[68:71], a[60:63], v[32:35]\n"
        "  v_add_u32 v230, s54, v207\n"
        "  v_lshl_add_u32 v231, v206, 4, s54\n"
        "  v_mfma_f32_16x16x32_bf16 v[36:39], a[72:75], a[60:63], v[36:39]\n"
        "  v_add_u32 v232, 147968, v207\n"
        "  s_xor_b32 s54, s40, 2\n"
        "  v_mfma_f32_16x16x32_bf16 v[4:7], v[144:147], a[44:47], v[4:7]\n"
        "  s_mul_i32 s54, s54, 12288\n"
        "  v_add_u32 v233, s54, v182\n"
        "  v_mfma_f32_16x16x32_bf16 v[12:15], v[144:147], a[48:51], v[12:15]\n"
        "  s_mov_b64 exec, s[78:79]\n"
        "  v_mov_b32 v188, 0\n"
        "  v_mov_b32 v189, 0\n"
        "  buffer_atomic_or_x2 v[188:189], v184, s[24:27], 0 offen sc0 sc1\n"
        "  s_mov_b64 exec, -1\n"
        "  v_mfma_f32_16x16x32_bf16 v[20:23], v[144:147], a[52:55], v[20:23]\n"
        "  v_cndmask_b32_e64 v213, v124, v125, s[72:73]\n"
        "  v_cndmask_b32_e64 v214, v128, v129, s[72:73]\n"
        "  v_mfma_f32_16x16x32_bf16 v[28:31], v[144:147], a[56:59], v[28:31]\n"
        "  v_cndmask_b32_e64 v217, v120, v121, s[72:73]\n"
        "  v_cndmask_b32_e64 v218, v126, v127, s[72:73]\n"
        "  v_mfma_f32_16x16x32_bf16 v[116:119], v[144:147], a[60:63], v[116:119]\n"
        "  v_cndmask_b32_e64 v219, v130, v131, s[72:73]\n"
        "  v_cndmask_b32_e64 v222, v122, v123, s[72:73]\n"
        "  v_mfma_f32_16x16x32_bf16 v[92:95], a[4:7], a[36:39], v[92:95]\n"
        "  v_cndmask_b32_e64 v213, v213, v218, s[74:75]\n"
        "  v_cndmask_b32_e64 v214, v214, v219, s[74:75]\n"
        "  v_mfma_f32_16x16x32_bf16 v[8:11], a[20:23], a[4:7], v[8:11]\n"
        "  s_mul_i32 s95, s88, 896\n"
        "  s_add_u32 s95, s95, s63\n"
        "  s_lshl_b32 s94, s40, 11\n"
        "  s_add_u32 m0, s94, 155648\n"
        "  s_lshl_b32 s94, s40, 7\n"
        "  buffer_load_dwordx4 v252, s[4:7], s95 offen offset:0 lds\n"
        "  v_mfma_f32_16x16x32_bf16 v[56:59], a[24:27], a[8:11], v[56:59]\n"
        "  v_cndmask_b32_e64 v217, v217, v222, s[74:75]\n"
        "  v_mfma_f32_16x16x32_bf16 v[60:63], a[28:31], a[8:11], v[60:63]\n"
        "  ds_bpermute_b32 v208, v207, v4\n"
        "  ds_bpermute_b32 v209, v207, v12\n"
        "  v_mfma_f32_16x16x32_bf16 v[96:99], a[8:11], a[32:35], v[96:99]\n"
        "  ds_bpermute_b32 v212, v207, v116\n"
        "  ds_bpermute_b32 v210, v207, v20\n"
        "  v_mfma_f32_16x16x32_bf16 v[100:103], a[8:11], a[36:39], v[100:103]\n"
        "  ds_bpermute_b32 v211, v207, v28\n"
        "  ds_write_b128 v233, v[32:35] offset:0\n"
        "  v_mfma_f32_16x16x32_bf16 v[16:19], a[20:23], a[8:11], v[16:19]\n"
        "  ds_write_b128 v233, v[36:39] offset:1024\n"
        "  ds_write_b128 v233, v[116:119] offset:2048\n"
        "  v_mfma_f32_16x16x32_bf16 v[64:67], a[24:27], a[12:15], v[64:67]\n"
        "  s_waitcnt lgkmcnt(7)  ; auto\n"
        "  v_mul_f32 v218, 0xbb124925, v208\n"
        "  s_waitcnt lgkmcnt(6)  ; auto\n"
        "  v_mul_f32 v219, 0xbb124925, v209\n"
        "  v_mfma_f32_16x16x32_bf16 v[68:71], a[28:31], a[12:15], v[68:71]\n"
        "  s_waitcnt lgkmcnt(5)  ; auto\n"
        "  v_mul_f32 v222, 0xbb124925, v212\n"
        "  v_mul_f32 v223, 0x3b124925, v213\n"
        "  v_mfma_f32_16x16x32_bf16 v[104:107], a[12:15], a[32:35], v[104:107]\n"
        "  v_mul_f32 v224, 0x3b124925, v214\n"
        "  v_mul_f32 v227, 0x3b124925, v217\n"
        "  v_mfma_f32_16x16x32_bf16 v[108:111], a[12:15], a[36:39], v[108:111]\n"
        "  v_fma_f32 v223, -v218, v218, v223\n"
        "  v_fma_f32 v224, -v219, v219, v224\n"
        "  v_mfma_f32_16x16x32_bf16 v[24:27], a[20:23], a[12:15], v[24:27]\n"
        "  buffer_load_dwordx4 v253, s[4:7], s95 offen offset:1024 lds\n"
        "  v_fma_f32 v227, -v222, v222, v227\n"
        "  v_mfma_f32_16x16x32_bf16 v[40:43], a[68:71], a[44:47], v[40:43]\n"
        "  v_max_f32 v223, 0, v223\n"
        "  v_max_f32 v224, 0, v224\n"
        "  v_mfma_f32_16x16x32_bf16 v[44:47], a[72:75], a[44:47], v[44:47]\n"
        "  v_max_f32 v227, 0, v227\n"
        "  v_add_f32 v223, 0x3727c5ac, v223\n"
        "  v_mfma_f32_16x16x32_bf16 v[80:83], a[44:47], a[76:79], v[80:83]\n"
        "  v_add_f32 v224, 0x3727c5ac, v224\n"
        "  v_add_f32 v227, 0x3727c5ac, v227\n"
        "  v_mfma_f32_16x16x32_bf16 v[84:87], a[44:47], a[80:83], v[84:87]\n"
        "  v_rsq_f32 v223, v223\n"
        "  v_rsq_f32 v224, v224\n"
        "  v_mfma_f32_16x16x32_bf16 v[0:3], a[64:67], a[44:47], v[0:3]\n"
        "  v_rsq_f32 v227, v227\n"
        "  v_mul_f32 v236, v218, v223\n"
        "  v_mfma_f32_16x16x32_bf16 v[48:51], a[68:71], a[48:51], v[48:51]\n"
        "  v_mul_f32 v237, v219, v224\n"
        "  v_mul_f32 v240, v222, v227\n"
        "  v_mfma_f32_16x16x32_bf16 v[52:55], a[72:75], a[48:51], v[52:55]\n"
        "  s_mov_b64 exec, s[70:71]\n"
        "  ds_write2_b32 v230, v223, v236 offset0:0 offset1:160\n"
        "  ds_write2_b32 v230, v224, v237 offset0:16 offset1:176\n"
        "  ds_write2_b32 v232, v227, v240 offset0:0 offset1:160\n"
        "  s_mov_b64 exec, -1\n"
        "  v_mfma_f32_16x16x32_bf16 v[88:91], a[48:51], a[76:79], v[88:91]\n"
        "  v_mov_b32_dpp v156, v32 row_newbcast:0 row_mask:0xf bank_mask:0xf\n"
        "  v_mov_b32_dpp v157, v33 row_newbcast:0 row_mask:0xf bank_mask:0xf\n"
        "  v_mfma_f32_16x16x32_bf16 v[92:95], a[48:51], a[80:83], v[92:95]\n"
        "  s_add_u32 m0, s94, 149504\n"
        "  s_mov_b64 exec, 0xff\n"
        "  buffer_load_dwordx4 v182, s[36:39], s63 offen offset:0 lds\n"
        "  s_mov_b64 exec, -1\n"
        "  v_mfma_f32_16x16x32_bf16 v[8:11], a[64:67], a[48:51], v[8:11]\n"
        "  v_mov_b32_dpp v158, v34 row_newbcast:0 row_mask:0xf bank_mask:0xf\n"
        "  v_mov_b32_dpp v159, v35 row_newbcast:0 row_mask:0xf bank_mask:0xf\n"
        "  v_mfma_f32_16x16x32_bf16 v[56:59], a[68:71], a[52:55], v[56:59]\n"
        "  v_mov_b32_dpp v160, v36 row_newbcast:0 row_mask:0xf bank_mask:0xf\n"
        "  v_mov_b32_dpp v161, v37 row_newbcast:0 row_mask:0xf bank_mask:0xf\n"
        "  v_mfma_f32_16x16x32_bf16 v[60:63], a[72:75], a[52:55], v[60:63]\n"
        "  v_mov_b32_dpp v162, v38 row_newbcast:0 row_mask:0xf bank_mask:0xf\n"
        "  v_mov_b32_dpp v163, v39 row_newbcast:0 row_mask:0xf bank_mask:0xf\n"
        "  v_mfma_f32_16x16x32_bf16 v[96:99], a[52:55], a[76:79], v[96:99]\n"
        "  v_mov_b32_dpp v152, v116 row_newbcast:0 row_mask:0xf bank_mask:0xf\n"
        "  v_mov_b32_dpp v153, v117 row_newbcast:0 row_mask:0xf bank_mask:0xf\n"
        "  v_mfma_f32_16x16x32_bf16 v[100:103], a[52:55], a[80:83], v[100:103]\n"
        "  v_mov_b32_dpp v154, v118 row_newbcast:0 row_mask:0xf bank_mask:0xf\n"
        "  v_mov_b32_dpp v155, v119 row_newbcast:0 row_mask:0xf bank_mask:0xf\n"
        "  v_mfma_f32_16x16x32_bf16 v[16:19], a[64:67], a[52:55], v[16:19]\n"
        "  s_waitcnt lgkmcnt(7)  ; auto\n"
        "  v_mul_f32 v220, 0xbb124925, v210\n"
        "  s_waitcnt lgkmcnt(6)  ; auto\n"
        "  v_mul_f32 v221, 0xbb124925, v211\n"
        "  v_mfma_f32_16x16x32_bf16 v[64:67], a[68:71], a[56:59], v[64:67]\n"
        "  v_add_u32 v233, s59, v182\n"
        "  v_lshl_add_u32 v234, v206, 8, s59\n"
        "  v_mfma_f32_16x16x32_bf16 v[68:71], a[72:75], a[56:59], v[68:71]\n"
        "  v_lshl_add_u32 v235, v205, 4, s59\n"
        "  v_mfma_f32_16x16x32_bf16 v[104:107], a[56:59], a[76:79], v[104:107]\n"
        "  v_mfma_f32_16x16x32_bf16 v[108:111], a[56:59], a[80:83], v[108:111]\n"
        "  v_mfma_f32_16x16x32_bf16 v[24:27], a[64:67], a[56:59], v[24:27]\n"
        "  s_waitcnt lgkmcnt(0)\n"
        "  s_barrier\n"
        "  ds_read_b128 v[148:151], v234 offset:2048\n"
        "  ds_read_b128 v[72:75], v233 offset:0\n"
        "  ds_read_b128 v[76:79], v233 offset:1024\n"
        "  ds_read_b32 v164, v235 offset:0\n"
        "  ds_read_b32 v165, v235 offset:1024\n"
        "  s_branch .Lae\n"
        ".Law1:\n"
        "  ds_read_b128 a[0:3], v160 offset:0\n"
        "  ds_read_b128 a[4:7], v160 offset:2048\n"
        "  ds_read_b128 a[8:11], v160 offset:4096\n"
        "  ds_read_b128 a[12:15], v160 offset:6144\n"
        "  ds_read_b128 a[16:19], v162\n"
        "  s_waitcnt lgkmcnt(6)  ; auto\n"
        "  ds_read_b64_tr_b16 a[24:25], v164 offset:1024\n"
        "  ds_read_b64_tr_b16 a[26:27], v164 offset:4096\n"
        "  s_waitcnt lgkmcnt(7)  ; auto\n"
        "  ds_read_b64_tr_b16 a[28:29], v165 offset:1024\n"
        "  ds_read_b64_tr_b16 a[30:31], v165 offset:4096\n"
        "  ds_read_b64_tr_b16 a[32:33], v164 offset:2048\n"
        "  ds_read_b64_tr_b16 a[34:35], v164 offset:5120\n"
        "  ds_read_b64_tr_b16 a[36:37], v165 offset:2048\n"
        "  ds_read_b64_tr_b16 a[38:39], v165 offset:5120\n"
        "  ds_read_b64_tr_b16 a[20:21], v164 offset:0\n"
        "  ds_read_b64_tr_b16 a[22:23], v164 offset:3072\n"
        "  ds_read_b64_tr_b16 v[140:141], v165 offset:0\n"
        "  ds_read_b64_tr_b16 v[142:143], v165 offset:3072\n"
        "  s_mov_b32 s52, 49152\n"
        "  s_waitcnt lgkmcnt(14)  ; auto\n"
        "  v_mfma_f32_16x16x32_bf16 v[132:135], a[8:11], a[8:11], v[132:135]\n"
        "  s_add_u32 m0, s69, s87\n"
        "  ds_read_b128 a[44:47], v161 offset:0\n"
        "  ds_read_b128 a[48:51], v161 offset:2048\n"
        "  s_waitcnt lgkmcnt(15)  ; auto\n"
        "  v_mfma_f32_16x16x32_bf16 v[136:139], a[12:15], a[12:15], v[136:139]\n"
        "  buffer_load_dwordx4 v156, s[8:11], s68 offen offset:0 lds\n"
        "  s_waitcnt lgkmcnt(8)  ; auto\n"
        "  v_mfma_f32_16x16x32_bf16 v[72:75], a[16:19], a[32:35], v[72:75]\n"
        "  ds_read_b128 a[52:55], v161 offset:4096\n"
        "  s_waitcnt lgkmcnt(7)  ; auto\n"
        "  v_mfma_f32_16x16x32_bf16 v[76:79], a[16:19], a[36:39], v[76:79]\n"
        "  buffer_load_dwordx4 v157, s[8:11], s68 offen offset:1024 lds\n"
        "  s_waitcnt lgkmcnt(5)  ; auto\n"
        "  v_mfma_f32_16x16x32_bf16 v[112:115], a[20:23], a[16:19], v[112:115]\n"
        "  ds_read_b128 a[56:59], v161 offset:6144\n"
        "  v_mfma_f32_16x16x32_bf16 v[40:43], a[24:27], a[0:3], v[40:43]\n"
        "  buffer_load_dwordx4 v158, s[8:11], s68 offen offset:2048 lds\n"
        "  v_mfma_f32_16x16x32_bf16 v[44:47], a[28:31], a[0:3], v[44:47]\n"
        "  s_add_u32 m0, s51, s56\n"
        "  ds_read_b128 a[60:63], v163\n"
        "  ds_read_b64_tr_b16 a[68:69], v164 offset:13312\n"
        "  v_mfma_f32_16x16x32_bf16 v[80:83], a[0:3], a[32:35], v[80:83]\n"
        "  buffer_load_dwordx4 v148, s[4:7], s48 offen offset:0 lds\n"
        "  v_mfma_f32_16x16x32_bf16 v[84:87], a[0:3], a[36:39], v[84:87]\n"
        "  s_add_u32 s88, s88, 1\n"
        "  ds_read_b64_tr_b16 a[70:71], v164 offset:16384\n"
        "  v_mfma_f32_16x16x32_bf16 v[0:3], a[20:23], a[0:3], v[0:3]\n"
        "  buffer_load_dwordx4 v149, s[4:7], s48 offen offset:1024 lds\n"
        "  v_mfma_f32_16x16x32_bf16 v[48:51], a[24:27], a[4:7], v[48:51]\n"
        "  s_cmp_eq_u32 s88, 7\n"
        "  s_cselect_b32 s88, 0, s88\n"
        "  s_cselect_b32 s89, -6, 1\n"
        "  ds_read_b64_tr_b16 a[72:73], v165 offset:13312\n"
        "  v_mfma_f32_16x16x32_bf16 v[52:55], a[28:31], a[4:7], v[52:55]\n"
        "  buffer_load_dwordx4 v150, s[4:7], s48 offen offset:2048 lds\n"
        "  v_mfma_f32_16x16x32_bf16 v[88:91], a[4:7], a[32:35], v[88:91]\n"
        "  ds_read_b64_tr_b16 a[74:75], v165 offset:16384\n"
        "  ds_read_b64_tr_b16 a[76:77], v164 offset:14336\n"
        "  v_mfma_f32_16x16x32_bf16 v[92:95], a[4:7], a[36:39], v[92:95]\n"
        "  buffer_load_dwordx4 v151, s[4:7], s48 offen offset:3072 lds\n"
        "  v_mfma_f32_16x16x32_bf16 v[8:11], a[20:23], a[4:7], v[8:11]\n"
        "  s_add_u32 m0, s51, s57\n"
        "  s_waitcnt lgkmcnt(11)  ; auto\n"
        "  v_cndmask_b32_e64 v140, v140, v171, s[66:67]\n"
        "  ds_read_b64_tr_b16 a[78:79], v164 offset:17408\n"
        "  v_mfma_f32_16x16x32_bf16 v[56:59], a[24:27], a[8:11], v[56:59]\n"
        "  buffer_load_dwordx4 v153, s[8:11], s50 offen offset:0 lds\n"
        "  v_cndmask_b32_e64 v141, v141, v171, s[66:67]\n"
        "  v_mfma_f32_16x16x32_bf16 v[60:63], a[28:31], a[8:11], v[60:63]\n"
        "  s_lshl_b32 s90, s89, 7\n"
        "  s_add_u32 s48, s48, s90\n"
        "  ds_read_b64_tr_b16 a[80:81], v165 offset:14336\n"
        "  v_mfma_f32_16x16x32_bf16 v[96:99], a[8:11], a[32:35], v[96:99]\n"
        "  buffer_load_dwordx4 v154, s[8:11], s50 offen offset:1024 lds\n"
        "  s_waitcnt lgkmcnt(12)  ; auto\n"
        "  v_cndmask_b32_e64 v142, v142, v171, s[66:67]\n"
        "  v_mfma_f32_16x16x32_bf16 v[100:103], a[8:11], a[36:39], v[100:103]\n"
        "  s_mov_b32 s68, s50\n"
        "  s_mov_b32 s69, s51\n"
        "  ds_read_b64_tr_b16 a[82:83], v165 offset:17408\n"
        "  ds_read_b64_tr_b16 a[64:65], v164 offset:12288\n"
        "  v_mfma_f32_16x16x32_bf16 v[16:19], a[20:23], a[8:11], v[16:19]\n"
        "  buffer_load_dwordx4 v155, s[8:11], s50 offen offset:2048 lds\n"
        "  v_cndmask_b32_e64 v143, v143, v171, s[66:67]\n"
        "  v_mfma_f32_16x16x32_bf16 v[64:67], a[24:27], a[12:15], v[64:67]\n"
        "  s_add_u32 m0, s51, s58\n"
        "  s_mul_i32 s90, s89, s85\n"
        "  s_add_u32 s50, s50, s90\n"
        "  ds_read_b64_tr_b16 a[66:67], v164 offset:15360\n"
        "  v_mfma_f32_16x16x32_bf16 v[68:71], a[28:31], a[12:15], v[68:71]\n"
        "  s_mov_b64 exec, s[64:65]\n"
        "  buffer_load_dwordx4 v152, s[12:15], s49 offen offset:0 lds\n"
        "  s_mov_b64 exec, -1\n"
        "  v_mfma_f32_16x16x32_bf16 v[104:107], a[12:15], a[32:35], v[104:107]\n"
        "  s_mul_i32 s90, s89, s84\n"
        "  s_add_u32 s49, s49, s90\n"
        "  s_and_b32 s94, %[wg], 127\n"
        "  s_lshl2_add_u32 s94, s94, s40\n"
        "  v_lshlrev_b32 v215, 7, v204\n"
        "  s_mul_i32 s95, s94, 3200\n"
        "  ds_read_b64_tr_b16 v[144:145], v165 offset:12288\n"
        "  v_mfma_f32_16x16x32_bf16 v[108:111], a[12:15], a[36:39], v[108:111]\n"
        "  s_mov_b64 exec, 0x1ffffff\n"
        "  buffer_load_dword v187, v215, s[16:19], s95 offen\n"
        "  s_mov_b64 exec, -1\n"
        "  v_mfma_f32_16x16x32_bf16 v[24:27], a[20:23], a[12:15], v[24:27]\n"
        "  s_mul_i32 s95, s94, 896\n"
        "  ds_read_b64_tr_b16 v[146:147], v165 offset:15360\n"
        "  v_add_u32 v160, s52, v160\n"
        "  v_mfma_f32_16x16x32_bf16 v[4:7], v[140:143], a[0:3], v[4:7]\n"
        "  v_add_u32 v161, s52, v161\n"
        "  v_add_u32 v162, s52, v162\n"
        "  v_add_u32 v163, s52, v163\n"
        "  v_mfma_f32_16x16x32_bf16 v[12:15], v[140:143], a[4:7], v[12:15]\n"
        "  s_mov_b64 exec, 0x7f\n"
        "  buffer_load_dword v187, v215, s[20:23], s95 offen\n"
        "  s_mov_b64 exec, -1\n"
        "  v_mfma_f32_16x16x32_bf16 v[20:23], v[140:143], a[8:11], v[20:23]\n"
        "  v_add_u32 v164, s52, v164\n"
        "  v_add_u32 v165, s52, v165\n"
        "  s_mov_b32 s51, 0\n"
        "  s_mov_b32 s54, 49152\n"
        "  v_mfma_f32_16x16x32_bf16 v[28:31], v[140:143], a[12:15], v[28:31]\n"
        "  s_waitcnt vmcnt(10) lgkmcnt(0)\n"
        "  s_barrier\n"
        "  s_mov_b32 s53, 4\n"
        "  .p2align 3\n"
        ".La1_1:\n"
        "  v_mfma_f32_16x16x32_bf16 v[132:135], a[52:55], a[52:55], v[132:135]\n"
        "  s_add_u32 m0, s69, s87\n"
        "  ds_read_b128 a[0:3], v160 offset:0\n"
        "  v_mfma_f32_16x16x32_bf16 v[136:139], a[56:59], a[56:59], v[136:139]\n"
        "  buffer_load_dwordx4 v156, s[8:11], s68 offen offset:0 lds\n"
        "  v_mfma_f32_16x16x32_bf16 v[72:75], a[60:63], a[76:79], v[72:75]\n"
        "  ds_read_b128 a[4:7], v160 offset:2048\n"
        "  v_mfma_f32_16x16x32_bf16 v[76:79], a[60:63], a[80:83], v[76:79]\n"
        "  ds_read_b128 a[8:11], v160 offset:4096\n"
        "  v_mfma_f32_16x16x32_bf16 v[112:115], a[64:67], a[60:63], v[112:115]\n"
        "  ds_read_b128 a[12:15], v160 offset:6144\n"
        "  v_mfma_f32_16x16x32_bf16 v[40:43], a[68:71], a[44:47], v[40:43]\n"
        "  ds_read_b128 a[16:19], v162\n"
        "  v_mfma_f32_16x16x32_bf16 v[44:47], a[72:75], a[44:47], v[44:47]\n"
        "  buffer_load_dwordx4 v157, s[8:11], s68 offen offset:1024 lds\n"
        "  v_mfma_f32_16x16x32_bf16 v[80:83], a[44:47], a[76:79], v[80:83]\n"
        "  ds_read_b64_tr_b16 a[24:25], v164 offset:1024\n"
        "  v_mfma_f32_16x16x32_bf16 v[84:87], a[44:47], a[80:83], v[84:87]\n"
        "  ds_read_b64_tr_b16 a[26:27], v164 offset:4096\n"
        "  v_mfma_f32_16x16x32_bf16 v[0:3], a[64:67], a[44:47], v[0:3]\n"
        "  ds_read_b64_tr_b16 a[28:29], v165 offset:1024\n"
        "  v_mfma_f32_16x16x32_bf16 v[48:51], a[68:71], a[48:51], v[48:51]\n"
        "  ds_read_b64_tr_b16 a[30:31], v165 offset:4096\n"
        "  v_mfma_f32_16x16x32_bf16 v[52:55], a[72:75], a[48:51], v[52:55]\n"
        "  buffer_load_dwordx4 v158, s[8:11], s68 offen offset:2048 lds\n"
        "  v_mfma_f32_16x16x32_bf16 v[88:91], a[48:51], a[76:79], v[88:91]\n"
        "  s_add_u32 m0, s51, s56\n"
        "  ds_read_b64_tr_b16 a[32:33], v164 offset:2048\n"
        "  v_mfma_f32_16x16x32_bf16 v[92:95], a[48:51], a[80:83], v[92:95]\n"
        "  ds_read_b64_tr_b16 a[34:35], v164 offset:5120\n"
        "  v_mfma_f32_16x16x32_bf16 v[8:11], a[64:67], a[48:51], v[8:11]\n"
        "  v_cndmask_b32_e64 v144, v144, v171, s[66:67]\n"
        "  v_cndmask_b32_e64 v145, v145, v171, s[66:67]\n"
        "  ds_read_b64_tr_b16 a[36:37], v165 offset:2048\n"
        "  v_mfma_f32_16x16x32_bf16 v[56:59], a[68:71], a[52:55], v[56:59]\n"
        "  v_cndmask_b32_e64 v146, v146, v171, s[66:67]\n"
        "  v_cndmask_b32_e64 v147, v147, v171, s[66:67]\n"
        "  ds_read_b64_tr_b16 a[38:39], v165 offset:5120\n"
        "  v_mfma_f32_16x16x32_bf16 v[60:63], a[72:75], a[52:55], v[60:63]\n"
        "  buffer_load_dwordx4 v148, s[4:7], s48 offen offset:0 lds\n"
        "  v_mfma_f32_16x16x32_bf16 v[96:99], a[52:55], a[76:79], v[96:99]\n"
        "  s_add_u32 s88, s88, 1\n"
        "  ds_read_b64_tr_b16 a[20:21], v164 offset:0\n"
        "  v_mfma_f32_16x16x32_bf16 v[100:103], a[52:55], a[80:83], v[100:103]\n"
        "  ds_read_b64_tr_b16 a[22:23], v164 offset:3072\n"
        "  v_mfma_f32_16x16x32_bf16 v[16:19], a[64:67], a[52:55], v[16:19]\n"
        "  ds_read_b64_tr_b16 v[140:141], v165 offset:0\n"
        "  v_mfma_f32_16x16x32_bf16 v[64:67], a[68:71], a[56:59], v[64:67]\n"
        "  ds_read_b64_tr_b16 v[142:143], v165 offset:3072\n"
        "  v_mfma_f32_16x16x32_bf16 v[68:71], a[72:75], a[56:59], v[68:71]\n"
        "  buffer_load_dwordx4 v149, s[4:7], s48 offen offset:1024 lds\n"
        "  v_mfma_f32_16x16x32_bf16 v[104:107], a[56:59], a[76:79], v[104:107]\n"
        "  s_cmp_eq_u32 s88, 7\n"
        "  s_cselect_b32 s88, 0, s88\n"
        "  s_cselect_b32 s89, -6, 1\n"
        "  ds_read_b128 a[60:63], v163\n"
        "  v_mfma_f32_16x16x32_bf16 v[108:111], a[56:59], a[80:83], v[108:111]\n"
        "  ds_read_b64_tr_b16 a[68:69], v164 offset:13312\n"
        "  v_mfma_f32_16x16x32_bf16 v[24:27], a[64:67], a[56:59], v[24:27]\n"
        "  ds_read_b64_tr_b16 a[70:71], v164 offset:16384\n"
        "  s_add_u32 s55, s54, 49152\n"
        "  v_mfma_f32_16x16x32_bf16 v[4:7], v[144:147], a[44:47], v[4:7]\n"
        "  ds_read_b64_tr_b16 a[72:73], v165 offset:13312\n"
        "  v_mfma_f32_16x16x32_bf16 v[12:15], v[144:147], a[48:51], v[12:15]\n"
        "  buffer_load_dwordx4 v150, s[4:7], s48 offen offset:2048 lds\n"
        "  s_cmp_eq_u32 s54, 98304\n"
        "  s_cselect_b32 s55, 0, s55\n"
        "  v_mfma_f32_16x16x32_bf16 v[20:23], v[144:147], a[52:55], v[20:23]\n"
        "  ds_read_b64_tr_b16 a[74:75], v165 offset:16384\n"
        "  s_sub_u32 s52, s55, s54\n"
        "  v_mfma_f32_16x16x32_bf16 v[28:31], v[144:147], a[56:59], v[28:31]\n"
        "  ds_read_b64_tr_b16 a[76:77], v164 offset:14336\n"
        "  s_waitcnt lgkmcnt(6)\n"
        "  v_mfma_f32_16x16x32_bf16 v[132:135], a[8:11], a[8:11], v[132:135]\n"
        "  ds_read_b64_tr_b16 a[78:79], v164 offset:17408\n"
        "  v_mfma_f32_16x16x32_bf16 v[136:139], a[12:15], a[12:15], v[136:139]\n"
        "  ds_read_b64_tr_b16 a[80:81], v165 offset:14336\n"
        "  v_mfma_f32_16x16x32_bf16 v[72:75], a[16:19], a[32:35], v[72:75]\n"
        "  buffer_load_dwordx4 v151, s[4:7], s48 offen offset:3072 lds\n"
        "  v_mfma_f32_16x16x32_bf16 v[76:79], a[16:19], a[36:39], v[76:79]\n"
        "  s_add_u32 m0, s51, s57\n"
        "  ds_read_b64_tr_b16 a[82:83], v165 offset:17408\n"
        "  v_mfma_f32_16x16x32_bf16 v[112:115], a[20:23], a[16:19], v[112:115]\n"
        "  ds_read_b64_tr_b16 a[64:65], v164 offset:12288\n"
        "  v_mfma_f32_16x16x32_bf16 v[40:43], a[24:27], a[0:3], v[40:43]\n"
        "  ds_read_b64_tr_b16 a[66:67], v164 offset:15360\n"
        "  v_mfma_f32_16x16x32_bf16 v[44:47], a[28:31], a[0:3], v[44:47]\n"
        "  ds_read_b128 a[44:47], v161 offset:0\n"
        "  v_mfma_f32_16x16x32_bf16 v[80:83], a[0:3], a[32:35], v[80:83]\n"
        "  buffer_load_dwordx4 v153, s[8:11], s50 offen offset:0 lds\n"
        "  v_mfma_f32_16x16x32_bf16 v[84:87], a[0:3], a[36:39], v[84:87]\n"
        "  s_lshl_b32 s90, s89, 7\n"
        "  ds_read_b128 a[48:51], v161 offset:2048\n"
        "  v_mfma_f32_16x16x32_bf16 v[0:3], a[20:23], a[0:3], v[0:3]\n"
        "  ds_read_b128 a[52:55], v161 offset:4096\n"
        "  v_mfma_f32_16x16x32_bf16 v[48:51], a[24:27], a[4:7], v[48:51]\n"
        "  s_add_u32 s48, s48, s90\n"
        "  ds_read_b128 a[56:59], v161 offset:6144\n"
        "  v_mfma_f32_16x16x32_bf16 v[52:55], a[28:31], a[4:7], v[52:55]\n"
        "  ds_read_b64_tr_b16 v[144:145], v165 offset:12288\n"
        "  v_mfma_f32_16x16x32_bf16 v[88:91], a[4:7], a[32:35], v[88:91]\n"
        "  buffer_load_dwordx4 v154, s[8:11], s50 offen offset:1024 lds\n"
        "  v_mfma_f32_16x16x32_bf16 v[92:95], a[4:7], a[36:39], v[92:95]\n"
        "  s_mov_b32 s68, s50\n"
        "  ds_read_b64_tr_b16 v[146:147], v165 offset:15360\n"
        "  v_add_u32 v160, s52, v160\n"
        "  v_mfma_f32_16x16x32_bf16 v[8:11], a[20:23], a[4:7], v[8:11]\n"
        "  v_cndmask_b32_e64 v140, v140, v171, s[66:67]\n"
        "  v_cndmask_b32_e64 v141, v141, v171, s[66:67]\n"
        "  v_add_u32 v161, s52, v161\n"
        "  v_mfma_f32_16x16x32_bf16 v[56:59], a[24:27], a[8:11], v[56:59]\n"
        "  s_mov_b32 s69, s51\n"
        "  v_cndmask_b32_e64 v142, v142, v171, s[66:67]\n"
        "  v_add_u32 v162, s52, v162\n"
        "  v_mfma_f32_16x16x32_bf16 v[60:63], a[28:31], a[8:11], v[60:63]\n"
        "  v_cndmask_b32_e64 v143, v143, v171, s[66:67]\n"
        "  v_add_u32 v163, s52, v163\n"
        "  v_add_u32 v164, s52, v164\n"
        "  v_mfma_f32_16x16x32_bf16 v[96:99], a[8:11], a[32:35], v[96:99]\n"
        "  buffer_load_dwordx4 v155, s[8:11], s50 offen offset:2048 lds\n"
        "  v_add_u32 v165, s52, v165\n"
        "  v_mfma_f32_16x16x32_bf16 v[100:103], a[8:11], a[36:39], v[100:103]\n"
        "  s_add_u32 m0, s51, s58\n"
        "  v_mfma_f32_16x16x32_bf16 v[16:19], a[20:23], a[8:11], v[16:19]\n"
        "  s_mul_i32 s90, s89, s85\n"
        "  v_mfma_f32_16x16x32_bf16 v[64:67], a[24:27], a[12:15], v[64:67]\n"
        "  v_mfma_f32_16x16x32_bf16 v[68:71], a[28:31], a[12:15], v[68:71]\n"
        "  s_add_u32 s50, s50, s90\n"
        "  v_mfma_f32_16x16x32_bf16 v[104:107], a[12:15], a[32:35], v[104:107]\n"
        "  v_mfma_f32_16x16x32_bf16 v[108:111], a[12:15], a[36:39], v[108:111]\n"
        "  s_mov_b64 exec, s[64:65]\n"
        "  buffer_load_dwordx4 v152, s[12:15], s49 offen offset:0 lds\n"
        "  s_mov_b64 exec, -1\n"
        "  s_mul_i32 s90, s89, s84\n"
        "  v_mfma_f32_16x16x32_bf16 v[24:27], a[20:23], a[12:15], v[24:27]\n"
        "  s_add_u32 s49, s49, s90\n"
        "  v_mfma_f32_16x16x32_bf16 v[4:7], v[140:143], a[0:3], v[4:7]\n"
        "  v_mfma_f32_16x16x32_bf16 v[12:15], v[140:143], a[4:7], v[12:15]\n"
        "  s_mov_b32 s51, s54\n"
        "  s_mov_b32 s54, s55\n"
        "  v_mfma_f32_16x16x32_bf16 v[20:23], v[140:143], a[8:11], v[20:23]\n"
        "  s_sub_u32 s53, s53, 1\n"
        "  s_cmp_lg_u32 s53, 0\n"
        "  v_mfma_f32_16x16x32_bf16 v[28:31], v[140:143], a[12:15], v[28:31]\n"
        "  s_waitcnt vmcnt(8) lgkmcnt(0)\n"
        "  s_barrier\n"
        "  s_cbranch_scc1 .La1_1\n"
        "  v_mfma_f32_16x16x32_bf16 v[132:135], a[52:55], a[52:55], v[132:135]\n"
        "  s_lshr_b32 s94, s63, 7\n"
        "  s_add_u32 s89, s40, s94\n"
        "  s_and_b32 s89, s89, 3\n"
        "  ds_read_b128 a[0:3], v160 offset:0\n"
        "  v_mfma_f32_16x16x32_bf16 v[136:139], a[56:59], a[56:59], v[136:139]\n"
        "  s_mov_b32 s84, 0\n"
        "  s_mov_b32 s85, s84\n"
        "  s_lshl_b32 s88, s96, 7\n"
        "  ds_read_b128 a[4:7], v160 offset:2048\n"
        "  v_mfma_f32_16x16x32_bf16 v[72:75], a[60:63], a[76:79], v[72:75]\n"
        "  s_add_u32 s88, s88, s97\n"
        "  s_lshl_b32 s95, s89, 5\n"
        "  s_add_u32 s88, s88, s95\n"
        "  ds_read_b128 a[8:11], v160 offset:4096\n"
        "  v_mfma_f32_16x16x32_bf16 v[76:79], a[60:63], a[80:83], v[76:79]\n"
        "  s_add_u32 m0, s69, s87\n"
        "  ds_read_b128 a[12:15], v160 offset:6144\n"
        "  v_mfma_f32_16x16x32_bf16 v[112:115], a[64:67], a[60:63], v[112:115]\n"
        "  buffer_load_dwordx4 v156, s[8:11], s68 offen offset:0 lds\n"
        "  v_mfma_f32_16x16x32_bf16 v[40:43], a[68:71], a[44:47], v[40:43]\n"
        "  ds_read_b128 a[16:19], v162\n"
        "  v_mfma_f32_16x16x32_bf16 v[44:47], a[72:75], a[44:47], v[44:47]\n"
        "  ds_read_b64_tr_b16 a[24:25], v164 offset:1024\n"
        "  v_mfma_f32_16x16x32_bf16 v[80:83], a[44:47], a[76:79], v[80:83]\n"
        "  buffer_load_dwordx4 v157, s[8:11], s68 offen offset:1024 lds\n"
        "  v_mfma_f32_16x16x32_bf16 v[84:87], a[44:47], a[80:83], v[84:87]\n"
        "  ds_read_b64_tr_b16 a[26:27], v164 offset:4096\n"
        "  v_cndmask_b32_e64 v186, v189, v188, s[76:77]\n"
        "  v_mfma_f32_16x16x32_bf16 v[0:3], a[64:67], a[44:47], v[0:3]\n"
        "  ds_read_b64_tr_b16 a[28:29], v165 offset:1024\n"
        "  v_add_u32 v186, 1, v186\n"
        "  v_mfma_f32_16x16x32_bf16 v[48:51], a[68:71], a[48:51], v[48:51]\n"
        "  buffer_load_dwordx4 v158, s[8:11], s68 offen offset:2048 lds\n"
        "  v_mov_b32 v191, v186\n"
        "  v_mfma_f32_16x16x32_bf16 v[52:55], a[72:75], a[48:51], v[52:55]\n"
        "  s_add_u32 m0, s59, 49152\n"
        "  ds_read_b64_tr_b16 a[30:31], v165 offset:4096\n"
        "  v_mfma_f32_16x16x32_bf16 v[88:91], a[48:51], a[76:79], v[88:91]\n"
        "  ds_read_b64_tr_b16 a[32:33], v164 offset:2048\n"
        "  s_mov_b64 exec, 15\n"
        "  buffer_store_dword v186, v185, s[24:27], 0 offen sc1\n"
        "  s_mov_b64 exec, -1\n"
        "  v_mfma_f32_16x16x32_bf16 v[92:95], a[48:51], a[80:83], v[92:95]\n"
        "  ds_read_b64_tr_b16 a[34:35], v164 offset:5120\n"
        "  v_mfma_f32_16x16x32_bf16 v[8:11], a[64:67], a[48:51], v[8:11]\n"
        "  buffer_load_dwordx4 v172, s[16:19], s60 offen offset:0 lds\n"
        "  v_cndmask_b32_e64 v144, v144, v171, s[66:67]\n"
        "  v_mfma_f32_16x16x32_bf16 v[56:59], a[68:71], a[52:55], v[56:59]\n"
        "  v_cndmask_b32_e64 v145, v145, v171, s[66:67]\n"
        "  v_cndmask_b32_e64 v146, v146, v171, s[66:67]\n"
        "  ds_read_b64_tr_b16 a[36:37], v165 offset:2048\n"
        "  v_mfma_f32_16x16x32_bf16 v[60:63], a[72:75], a[52:55], v[60:63]\n"
        "  v_cndmask_b32_e64 v147, v147, v171, s[66:67]\n"
        "  ds_read_b64_tr_b16 a[38:39], v165 offset:5120\n"
        "  v_mfma_f32_16x16x32_bf16 v[96:99], a[52:55], a[76:79], v[96:99]\n"
        "  buffer_load_dwordx4 v173, s[16:19], s60 offen offset:1024 lds\n"
        "  v_mfma_f32_16x16x32_bf16 v[100:103], a[52:55], a[80:83], v[100:103]\n"
        "  ds_read_b64_tr_b16 a[20:21], v164 offset:0\n"
        "  v_mfma_f32_16x16x32_bf16 v[16:19], a[64:67], a[52:55], v[16:19]\n"
        "  ds_read_b64_tr_b16 a[22:23], v164 offset:3072\n"
        "  v_mfma_f32_16x16x32_bf16 v[64:67], a[68:71], a[56:59], v[64:67]\n"
        "  ds_read_b64_tr_b16 v[140:141], v165 offset:0\n"
        "  v_mfma_f32_16x16x32_bf16 v[68:71], a[72:75], a[56:59], v[68:71]\n"
        "  buffer_load_dwordx4 v174, s[16:19], s60 offen offset:2048 lds\n"
        "  v_mfma_f32_16x16x32_bf16 v[104:107], a[56:59], a[76:79], v[104:107]\n"
        "  ds_read_b64_tr_b16 v[142:143], v165 offset:3072\n"
        "  v_mfma_f32_16x16x32_bf16 v[108:111], a[56:59], a[80:83], v[108:111]\n"
        "  ds_read_b128 a[60:63], v163\n"
        "  v_mfma_f32_16x16x32_bf16 v[24:27], a[64:67], a[56:59], v[24:27]\n"
        "  buffer_load_dwordx4 v175, s[16:19], s60 offen offset:3072 lds\n"
        "  s_add_u32 s55, s54, 49152\n"
        "  v_mfma_f32_16x16x32_bf16 v[4:7], v[144:147], a[44:47], v[4:7]\n"
        "  s_add_u32 s60, s60, 28672\n"
        "  ds_read_b64_tr_b16 a[68:69], v164 offset:13312\n"
        "  v_mfma_f32_16x16x32_bf16 v[12:15], v[144:147], a[48:51], v[12:15]\n"
        "  s_add_u32 m0, s59, 53248\n"
        "  ds_read_b64_tr_b16 a[70:71], v164 offset:16384\n"
        "  s_cmp_eq_u32 s54, 98304\n"
        "  s_cselect_b32 s55, 0, s55\n"
        "  v_mfma_f32_16x16x32_bf16 v[20:23], v[144:147], a[52:55], v[20:23]\n"
        "  ds_read_b64_tr_b16 a[72:73], v165 offset:13312\n"
        "  s_sub_u32 s52, s55, s54\n"
        "  v_mfma_f32_16x16x32_bf16 v[28:31], v[144:147], a[56:59], v[28:31]\n"
        "  buffer_load_dwordx4 v172, s[16:19], s60 offen offset:0 lds\n"
        "  s_waitcnt lgkmcnt(4)\n"
        "  v_mfma_f32_16x16x32_bf16 v[132:135], a[8:11], a[8:11], v[132:135]\n"
        "  ds_read_b64_tr_b16 a[74:75], v165 offset:16384\n"
        "  v_mfma_f32_16x16x32_bf16 v[136:139], a[12:15], a[12:15], v[136:139]\n"
        "  ds_read_b64_tr_b16 a[76:77], v164 offset:14336\n"
        "  v_mfma_f32_16x16x32_bf16 v[72:75], a[16:19], a[32:35], v[72:75]\n"
        "  buffer_load_dwordx4 v173, s[16:19], s60 offen offset:1024 lds\n"
        "  v_mfma_f32_16x16x32_bf16 v[76:79], a[16:19], a[36:39], v[76:79]\n"
        "  ds_read_b64_tr_b16 a[78:79], v164 offset:17408\n"
        "  v_mfma_f32_16x16x32_bf16 v[112:115], a[20:23], a[16:19], v[112:115]\n"
        "  ds_read_b64_tr_b16 a[80:81], v165 offset:14336\n"
        "  v_mfma_f32_16x16x32_bf16 v[40:43], a[24:27], a[0:3], v[40:43]\n"
        "  buffer_load_dwordx4 v174, s[16:19], s60 offen offset:2048 lds\n"
        "  v_mfma_f32_16x16x32_bf16 v[44:47], a[28:31], a[0:3], v[44:47]\n"
        "  ds_read_b64_tr_b16 a[82:83], v165 offset:17408\n"
        "  v_mfma_f32_16x16x32_bf16 v[80:83], a[0:3], a[32:35], v[80:83]\n"
        "  ds_read_b64_tr_b16 a[64:65], v164 offset:12288\n"
        "  v_mfma_f32_16x16x32_bf16 v[84:87], a[0:3], a[36:39], v[84:87]\n"
        "  ds_read_b64_tr_b16 a[66:67], v164 offset:15360\n"
        "  v_mfma_f32_16x16x32_bf16 v[0:3], a[20:23], a[0:3], v[0:3]\n"
        "  buffer_load_dwordx4 v175, s[16:19], s60 offen offset:3072 lds\n"
        "  v_mfma_f32_16x16x32_bf16 v[48:51], a[24:27], a[4:7], v[48:51]\n"
        "  s_add_u32 s60, s60, 28672\n"
        "  ds_read_b128 a[44:47], v161 offset:0\n"
        "  v_mfma_f32_16x16x32_bf16 v[52:55], a[28:31], a[4:7], v[52:55]\n"
        "  s_add_u32 m0, s59, 57344\n"
        "  ds_read_b128 a[48:51], v161 offset:2048\n"
        "  v_mfma_f32_16x16x32_bf16 v[88:91], a[4:7], a[32:35], v[88:91]\n"
        "  buffer_load_dwordx4 v172, s[16:19], s60 offen offset:0 lds\n"
        "  v_mfma_f32_16x16x32_bf16 v[92:95], a[4:7], a[36:39], v[92:95]\n"
        "  ds_read_b128 a[52:55], v161 offset:4096\n"
        "  v_mfma_f32_16x16x32_bf16 v[8:11], a[20:23], a[4:7], v[8:11]\n"
        "  ds_read_b128 a[56:59], v161 offset:6144\n"
        "  v_cndmask_b32_e64 v140, v140, v171, s[66:67]\n"
        "  v_mfma_f32_16x16x32_bf16 v[56:59], a[24:27], a[8:11], v[56:59]\n"
        "  ds_read_b64_tr_b16 v[144:145], v165 offset:12288\n"
        "  v_cndmask_b32_e64 v141, v141, v171, s[66:67]\n"
        "  v_mfma_f32_16x16x32_bf16 v[60:63], a[28:31], a[8:11], v[60:63]\n"
        "  buffer_load_dwordx4 v173, s[16:19], s60 offen offset:1024 lds\n"
        "  v_cndmask_b32_e64 v142, v142, v171, s[66:67]\n"
        "  v_mfma_f32_16x16x32_bf16 v[96:99], a[8:11], a[32:35], v[96:99]\n"
        "  ds_read_b64_tr_b16 v[146:147], v165 offset:15360\n"
        "  v_cndmask_b32_e64 v143, v143, v171, s[66:67]\n"
        "  v_add_u32 v160, s52, v160\n"
        "  v_mfma_f32_16x16x32_bf16 v[100:103], a[8:11], a[36:39], v[100:103]\n"
        "  v_add_u32 v161, s52, v161\n"
        "  v_add_u32 v162, s52, v162\n"
        "  v_add_u32 v163, s52, v163\n"
        "  v_mfma_f32_16x16x32_bf16 v[16:19], a[20:23], a[8:11], v[16:19]\n"
        "  buffer_load_dwordx4 v174, s[16:19], s60 offen offset:2048 lds\n"
        "  v_add_u32 v164, s52, v164\n"
        "  v_add_u32 v165, s52, v165\n"
        "  v_mfma_f32_16x16x32_bf16 v[64:67], a[24:27], a[12:15], v[64:67]\n"
        "  v_mfma_f32_16x16x32_bf16 v[68:71], a[28:31], a[12:15], v[68:71]\n"
        "  v_mfma_f32_16x16x32_bf16 v[104:107], a[12:15], a[32:35], v[104:107]\n"
        "  v_mfma_f32_16x16x32_bf16 v[108:111], a[12:15], a[36:39], v[108:111]\n"
        "  buffer_load_dwordx4 v175, s[16:19], s60 offen offset:3072 lds\n"
        "  s_add_u32 s60, s60, 28672\n"
        "  v_mfma_f32_16x16x32_bf16 v[24:27], a[20:23], a[12:15], v[24:27]\n"
        "  v_mfma_f32_16x16x32_bf16 v[4:7], v[140:143], a[0:3], v[4:7]\n"
        "  v_mfma_f32_16x16x32_bf16 v[12:15], v[140:143], a[4:7], v[12:15]\n"
        "  s_mov_b32 s51, s54\n"
        "  s_mov_b32 s54, s55\n"
        "  v_mfma_f32_16x16x32_bf16 v[20:23], v[140:143], a[8:11], v[20:23]\n"
        "  v_mfma_f32_16x16x32_bf16 v[28:31], v[140:143], a[12:15], v[28:31]\n"
        "  s_waitcnt vmcnt(12) lgkmcnt(0)\n"
        "  s_barrier\n"
        "  v_mfma_f32_16x16x32_bf16 v[132:135], a[52:55], a[52:55], v[132:135]\n"
        "  s_add_u32 m0, s59, 98304\n"
        "  ds_read_b128 a[0:3], v160 offset:0\n"
        "  ds_read_b128 a[4:7], v160 offset:2048\n"
        "  v_mfma_f32_16x16x32_bf16 v[136:139], a[56:59], a[56:59], v[136:139]\n"
        "  buffer_load_dwordx4 v172, s[16:19], s60 offen offset:0 lds\n"
        "  v_mfma_f32_16x16x32_bf16 v[72:75], a[60:63], a[76:79], v[72:75]\n"
        "  ds_read_b128 a[8:11], v160 offset:4096\n"
        "  v_mfma_f32_16x16x32_bf16 v[76:79], a[60:63], a[80:83], v[76:79]\n"
        "  ds_read_b128 a[12:15], v160 offset:6144\n"
        "  v_mfma_f32_16x16x32_bf16 v[112:115], a[64:67], a[60:63], v[112:115]\n"
        "  buffer_load_dwordx4 v173, s[16:19], s60 offen offset:1024 lds\n"
        "  v_mfma_f32_16x16x32_bf16 v[40:43], a[68:71], a[44:47], v[40:43]\n"
        "  ds_read_b128 a[16:19], v162\n"
        "  v_mfma_f32_16x16x32_bf16 v[44:47], a[72:75], a[44:47], v[44:47]\n"
        "  ds_read_b64_tr_b16 a[24:25], v164 offset:1024\n"
        "  v_mfma_f32_16x16x32_bf16 v[80:83], a[44:47], a[76:79], v[80:83]\n"
        "  ds_read_b64_tr_b16 a[26:27], v164 offset:4096\n"
        "  ds_read_b64_tr_b16 a[28:29], v165 offset:1024\n"
        "  v_mfma_f32_16x16x32_bf16 v[84:87], a[44:47], a[80:83], v[84:87]\n"
        "  buffer_load_dwordx4 v174, s[16:19], s60 offen offset:2048 lds\n"
        "  v_mfma_f32_16x16x32_bf16 v[0:3], a[64:67], a[44:47], v[0:3]\n"
        "  ds_read_b64_tr_b16 a[30:31], v165 offset:4096\n"
        "  v_mfma_f32_16x16x32_bf16 v[48:51], a[68:71], a[48:51], v[48:51]\n"
        "  ds_read_b64_tr_b16 a[32:33], v164 offset:2048\n"
        "  v_mfma_f32_16x16x32_bf16 v[52:55], a[72:75], a[48:51], v[52:55]\n"
        "  buffer_load_dwordx4 v175, s[16:19], s60 offen offset:3072 lds\n"
        "  v_mfma_f32_16x16x32_bf16 v[88:91], a[48:51], a[76:79], v[88:91]\n"
        "  s_add_u32 s60, s60, 28672\n"
        "  ds_read_b64_tr_b16 a[34:35], v164 offset:5120\n"
        "  v_mfma_f32_16x16x32_bf16 v[92:95], a[48:51], a[80:83], v[92:95]\n"
        "  s_add_u32 m0, s59, 102400\n"
        "  ds_read_b64_tr_b16 a[36:37], v165 offset:2048\n"
        "  v_mfma_f32_16x16x32_bf16 v[8:11], a[64:67], a[48:51], v[8:11]\n"
        "  v_cndmask_b32_e64 v144, v144, v171, s[66:67]\n"
        "  v_cndmask_b32_e64 v145, v145, v171, s[66:67]\n"
        "  ds_read_b64_tr_b16 a[38:39], v165 offset:5120\n"
        "  v_mfma_f32_16x16x32_bf16 v[56:59], a[68:71], a[52:55], v[56:59]\n"
        "  buffer_load_dwordx4 v172, s[16:19], s60 offen offset:0 lds\n"
        "  v_cndmask_b32_e64 v146, v146, v171, s[66:67]\n"
        "  v_mfma_f32_16x16x32_bf16 v[60:63], a[72:75], a[52:55], v[60:63]\n"
        "  v_cndmask_b32_e64 v147, v147, v171, s[66:67]\n"
        "  ds_read_b64_tr_b16 a[20:21], v164 offset:0\n"
        "  ds_read_b64_tr_b16 a[22:23], v164 offset:3072\n"
        "  v_mfma_f32_16x16x32_bf16 v[96:99], a[52:55], a[76:79], v[96:99]\n"
        "  ds_read_b64_tr_b16 v[140:141], v165 offset:0\n"
        "  v_mfma_f32_16x16x32_bf16 v[100:103], a[52:55], a[80:83], v[100:103]\n"
        "  buffer_load_dwordx4 v173, s[16:19], s60 offen offset:1024 lds\n"
        "  v_mfma_f32_16x16x32_bf16 v[16:19], a[64:67], a[52:55], v[16:19]\n"
        "  ds_read_b64_tr_b16 v[142:143], v165 offset:3072\n"
        "  v_mfma_f32_16x16x32_bf16 v[64:67], a[68:71], a[56:59], v[64:67]\n"
        "  ds_read_b128 a[60:63], v163\n"
        "  v_mfma_f32_16x16x32_bf16 v[68:71], a[72:75], a[56:59], v[68:71]\n"
        "  ds_read_b64_tr_b16 a[68:69], v164 offset:13312\n"
        "  v_mfma_f32_16x16x32_bf16 v[104:107], a[56:59], a[76:79], v[104:107]\n"
        "  buffer_load_dwordx4 v174, s[16:19], s60 offen offset:2048 lds\n"
        "  v_mfma_f32_16x16x32_bf16 v[108:111], a[56:59], a[80:83], v[108:111]\n"
        "  ds_read_b64_tr_b16 a[70:71], v164 offset:16384\n"
        "  v_mfma_f32_16x16x32_bf16 v[24:27], a[64:67], a[56:59], v[24:27]\n"
        "  ds_read_b64_tr_b16 a[72:73], v165 offset:13312\n"
        "  ds_read_b64_tr_b16 a[74:75], v165 offset:16384\n"
        "  s_add_u32 s55, s54, 49152\n"
        "  v_mfma_f32_16x16x32_bf16 v[4:7], v[144:147], a[44:47], v[4:7]\n"
        "  buffer_load_dwordx4 v175, s[16:19], s60 offen offset:3072 lds\n"
        "  v_mfma_f32_16x16x32_bf16 v[12:15], v[144:147], a[48:51], v[12:15]\n"
        "  s_add_u32 s60, s60, 28672\n"
        "  ds_read_b64_tr_b16 a[76:77], v164 offset:14336\n"
        "  s_cmp_eq_u32 s54, 98304\n"
        "  s_cselect_b32 s55, 0, s55\n"
        "  v_mfma_f32_16x16x32_bf16 v[20:23], v[144:147], a[52:55], v[20:23]\n"
        "  s_add_u32 m0, s59, 106496\n"
        "  ds_read_b64_tr_b16 a[78:79], v164 offset:17408\n"
        "  s_sub_u32 s52, s55, s54\n"
        "  v_mfma_f32_16x16x32_bf16 v[28:31], v[144:147], a[56:59], v[28:31]\n"
        "  ds_read_b64_tr_b16 a[80:81], v165 offset:14336\n"
        "  s_waitcnt lgkmcnt(8)\n"
        "  v_mfma_f32_16x16x32_bf16 v[132:135], a[8:11], a[8:11], v[132:135]\n"
        "  buffer_load_dwordx4 v172, s[16:19], s60 offen offset:0 lds\n"
        "  v_cndmask_b32_e64 v140, v140, v171, s[66:67]\n"
        "  v_cndmask_b32_e64 v141, v141, v171, s[66:67]\n"
        "  v_cndmask_b32_e64 v142, v142, v171, s[66:67]\n"
        "  v_cndmask_b32_e64 v143, v143, v171, s[66:67]\n"
        "  v_mfma_f32_16x16x32_bf16 v[136:139], a[12:15], a[12:15], v[136:139]\n"
        "  ds_read_b64_tr_b16 a[82:83], v165 offset:17408\n"
        "  v_mfma_f32_16x16x32_bf16 v[72:75], a[16:19], a[32:35], v[72:75]\n"
        "  ds_read_b64_tr_b16 a[64:65], v164 offset:12288\n"
        "  v_mfma_f32_16x16x32_bf16 v[76:79], a[16:19], a[36:39], v[76:79]\n"
        "  buffer_load_dwordx4 v173, s[16:19], s60 offen offset:1024 lds\n"
        "  v_mfma_f32_16x16x32_bf16 v[112:115], a[20:23], a[16:19], v[112:115]\n"
        "  ds_read_b64_tr_b16 a[66:67], v164 offset:15360\n"
        "  ds_read_b128 a[44:47], v161 offset:0\n"
        "  v_mfma_f32_16x16x32_bf16 v[4:7], v[140:143], a[0:3], v[4:7]\n"
        "  ds_read_b128 a[48:51], v161 offset:2048\n"
        "  v_mfma_f32_16x16x32_bf16 v[12:15], v[140:143], a[4:7], v[12:15]\n"
        "  ds_read_b128 a[52:55], v161 offset:4096\n"
        "  v_mfma_f32_16x16x32_bf16 v[20:23], v[140:143], a[8:11], v[20:23]\n"
        "  buffer_load_dwordx4 v174, s[16:19], s60 offen offset:2048 lds\n"
        "  v_mfma_f32_16x16x32_bf16 v[28:31], v[140:143], a[12:15], v[28:31]\n"
        "  ds_read_b128 a[56:59], v161 offset:6144\n"
        "  v_mfma_f32_16x16x32_bf16 v[40:43], a[24:27], a[0:3], v[40:43]\n"
        "  ds_read_b64_tr_b16 v[144:145], v165 offset:12288\n"
        "  v_mfma_f32_16x16x32_bf16 v[44:47], a[28:31], a[0:3], v[44:47]\n"
        "  ds_read_b64_tr_b16 v[146:147], v165 offset:15360\n"
        "  v_mfma_f32_16x16x32_bf16 v[80:83], a[0:3], a[32:35], v[80:83]\n"
        "  buffer_load_dwordx4 v175, s[16:19], s60 offen offset:3072 lds\n"
        "  s_add_u32 s60, s60, 28672\n"
        "  v_mfma_f32_16x16x32_bf16 v[84:87], a[0:3], a[36:39], v[84:87]\n"
        "  v_mfma_f32_16x16x32_bf16 v[0:3], a[20:23], a[0:3], v[0:3]\n"
        "  v_mfma_f32_16x16x32_bf16 v[48:51], a[24:27], a[4:7], v[48:51]\n"
        "  s_mov_b32 s51, s54\n"
        "  s_mov_b32 s54, s55\n"
        "  v_mfma_f32_16x16x32_bf16 v[52:55], a[28:31], a[4:7], v[52:55]\n"
        "  v_mfma_f32_16x16x32_bf16 v[88:91], a[4:7], a[32:35], v[88:91]\n"
        "  s_waitcnt lgkmcnt(0)\n"
        "  s_barrier\n"
        "  v_mfma_f32_16x16x32_bf16 v[132:135], a[52:55], a[52:55], v[132:135]\n"
        "  v_cndmask_b32_e64 v144, v144, v171, s[66:67]\n"
        "  v_cndmask_b32_e64 v145, v145, v171, s[66:67]\n"
        "  v_mfma_f32_16x16x32_bf16 v[136:139], a[56:59], a[56:59], v[136:139]\n"
        "  v_cndmask_b32_e64 v146, v146, v171, s[66:67]\n"
        "  v_cndmask_b32_e64 v147, v147, v171, s[66:67]\n"
        "  v_mfma_f32_16x16x32_bf16 v[72:75], a[60:63], a[76:79], v[72:75]\n"
        "  s_lshl_b32 s54, s46, 8\n"
        "  s_add_u32 s54, s54, 147456\n"
        "  v_mfma_f32_16x16x32_bf16 v[76:79], a[60:63], a[80:83], v[76:79]\n"
        "  v_add_u32 v230, s54, v207\n"
        "  v_lshl_add_u32 v231, v206, 4, s54\n"
        "  v_mfma_f32_16x16x32_bf16 v[112:115], a[64:67], a[60:63], v[112:115]\n"
        "  v_add_u32 v232, 147968, v207\n"
        "  s_xor_b32 s54, s40, 2\n"
        "  v_mfma_f32_16x16x32_bf16 v[4:7], v[144:147], a[44:47], v[4:7]\n"
        "  s_mul_i32 s54, s54, 12288\n"
        "  v_add_u32 v233, s54, v182\n"
        "  v_mfma_f32_16x16x32_bf16 v[12:15], v[144:147], a[48:51], v[12:15]\n"
        "  s_mov_b64 exec, s[78:79]\n"
        "  v_mov_b32 v188, 0\n"
        "  v_mov_b32 v189, 0\n"
        "  buffer_atomic_or_x2 v[188:189], v184, s[24:27], 0 offen sc0 sc1\n"
        "  s_mov_b64 exec, -1\n"
        "  v_mfma_f32_16x16x32_bf16 v[20:23], v[144:147], a[52:55], v[20:23]\n"
        "  v_cndmask_b32_e64 v215, v132, v133, s[72:73]\n"
        "  v_cndmask_b32_e64 v216, v136, v137, s[72:73]\n"
        "  v_mfma_f32_16x16x32_bf16 v[28:31], v[144:147], a[56:59], v[28:31]\n"
        "  v_cndmask_b32_e64 v220, v134, v135, s[72:73]\n"
        "  v_cndmask_b32_e64 v221, v138, v139, s[72:73]\n"
        "  v_mfma_f32_16x16x32_bf16 v[92:95], a[4:7], a[36:39], v[92:95]\n"
        "  v_cndmask_b32_e64 v215, v215, v220, s[74:75]\n"
        "  v_cndmask_b32_e64 v216, v216, v221, s[74:75]\n"
        "  v_mfma_f32_16x16x32_bf16 v[8:11], a[20:23], a[4:7], v[8:11]\n"
        "  v_mfma_f32_16x16x32_bf16 v[56:59], a[24:27], a[8:11], v[56:59]\n"
        "  s_mul_i32 s95, s88, 896\n"
        "  s_add_u32 s95, s95, s63\n"
        "  s_lshl_b32 s94, s40, 11\n"
        "  s_add_u32 m0, s94, 155648\n"
        "  s_lshl_b32 s94, s40, 7\n"
        "  buffer_load_dwordx4 v252, s[4:7], s95 offen offset:0 lds\n"
        "  v_mfma_f32_16x16x32_bf16 v[60:63], a[28:31], a[8:11], v[60:63]\n"
        "  v_mfma_f32_16x16x32_bf16 v[96:99], a[8:11], a[32:35], v[96:99]\n"
        "  ds_bpermute_b32 v210, v207, v20\n"
        "  ds_bpermute_b32 v211, v207, v28\n"
        "  v_mfma_f32_16x16x32_bf16 v[100:103], a[8:11], a[36:39], v[100:103]\n"
        "  ds_bpermute_b32 v208, v207, v4\n"
        "  ds_bpermute_b32 v209, v207, v12\n"
        "  v_mfma_f32_16x16x32_bf16 v[16:19], a[20:23], a[8:11], v[16:19]\n"
        "  ds_write_b128 v233, v[72:75] offset:0\n"
        "  ds_write_b128 v233, v[76:79] offset:1024\n"
        "  v_mfma_f32_16x16x32_bf16 v[64:67], a[24:27], a[12:15], v[64:67]\n"
        "  ds_write_b128 v233, v[112:115] offset:2048\n"
        "  s_waitcnt lgkmcnt(6)  ; auto\n"
        "  v_mul_f32 v220, 0xbb124925, v210\n"
        "  v_mfma_f32_16x16x32_bf16 v[68:71], a[28:31], a[12:15], v[68:71]\n"
        "  s_waitcnt lgkmcnt(5)  ; auto\n"
        "  v_mul_f32 v221, 0xbb124925, v211\n"
        "  v_mul_f32 v225, 0x3b124925, v215\n"
        "  v_mfma_f32_16x16x32_bf16 v[104:107], a[12:15], a[32:35], v[104:107]\n"
        "  v_mul_f32 v226, 0x3b124925, v216\n"
        "  v_fma_f32 v225, -v220, v220, v225\n"
        "  v_mfma_f32_16x16x32_bf16 v[108:111], a[12:15], a[36:39], v[108:111]\n"
        "  v_fma_f32 v226, -v221, v221, v226\n"
        "  v_max_f32 v225, 0, v225\n"
        "  v_mfma_f32_16x16x32_bf16 v[24:27], a[20:23], a[12:15], v[24:27]\n"
        "  buffer_load_dwordx4 v253, s[4:7], s95 offen offset:1024 lds\n"
        "  v_max_f32 v226, 0, v226\n"
        "  v_mfma_f32_16x16x32_bf16 v[40:43], a[68:71], a[44:47], v[40:43]\n"
        "  v_add_f32 v225, 0x3727c5ac, v225\n"
        "  v_add_f32 v226, 0x3727c5ac, v226\n"
        "  v_mfma_f32_16x16x32_bf16 v[44:47], a[72:75], a[44:47], v[44:47]\n"
        "  v_rsq_f32 v225, v225\n"
        "  v_rsq_f32 v226, v226\n"
        "  v_mfma_f32_16x16x32_bf16 v[80:83], a[44:47], a[76:79], v[80:83]\n"
        "  v_mul_f32 v238, v220, v225\n"
        "  v_mul_f32 v239, v221, v226\n"
        "  v_mfma_f32_16x16x32_bf16 v[84:87], a[44:47], a[80:83], v[84:87]\n"
        "  s_mov_b64 exec, s[70:71]\n"
        "  ds_write2_b32 v230, v225, v238 offset0:32 offset1:192\n"
        "  ds_write2_b32 v230, v226, v239 offset0:48 offset1:208\n"
        "  s_mov_b64 exec, -1\n"
        "  v_mfma_f32_16x16x32_bf16 v[0:3], a[64:67], a[44:47], v[0:3]\n"
        "  v_mov_b32 v164, v72\n"
        "  v_mov_b32 v166, v72\n"
        "  v_mfma_f32_16x16x32_bf16 v[48:51], a[68:71], a[48:51], v[48:51]\n"
        "  v_mov_b32 v165, v76\n"
        "  v_mov_b32 v167, v76\n"
        "  v_mfma_f32_16x16x32_bf16 v[52:55], a[72:75], a[48:51], v[52:55]\n"
        "  v_mov_b32_dpp v148, v112 row_newbcast:0 row_mask:0xf bank_mask:0xf\n"
        "  v_mov_b32_dpp v149, v113 row_newbcast:0 row_mask:0xf bank_mask:0xf\n"
        "  v_mfma_f32_16x16x32_bf16 v[88:91], a[48:51], a[76:79], v[88:91]\n"
        "  v_mov_b32_dpp v150, v114 row_newbcast:0 row_mask:0xf bank_mask:0xf\n"
        "  v_mov_b32_dpp v151, v115 row_newbcast:0 row_mask:0xf bank_mask:0xf\n"
        "  v_mfma_f32_16x16x32_bf16 v[92:95], a[48:51], a[80:83], v[92:95]\n"
        "  s_add_u32 m0, s94, 149504\n"
        "  s_mov_b64 exec, 0xff\n"
        "  buffer_load_dwordx4 v182, s[36:39], s63 offen offset:0 lds\n"
        "  s_mov_b64 exec, -1\n"
        "  v_mfma_f32_16x16x32_bf16 v[8:11], a[64:67], a[48:51], v[8:11]\n"
        "  v_permlane16_swap_b32 v164, v166\n"
        "  v_permlane16_swap_b32 v165, v167\n"
        "  v_mfma_f32_16x16x32_bf16 v[56:59], a[68:71], a[52:55], v[56:59]\n"
        "  s_waitcnt lgkmcnt(6)  ; auto\n"
        "  v_mul_f32 v218, 0xbb124925, v208\n"
        "  s_waitcnt lgkmcnt(5)  ; auto\n"
        "  v_mul_f32 v219, 0xbb124925, v209\n"
        "  v_mfma_f32_16x16x32_bf16 v[60:63], a[72:75], a[52:55], v[60:63]\n"
        "  v_add_u32 v233, s59, v182\n"
        "  v_lshl_add_u32 v234, v206, 8, s59\n"
        "  v_mfma_f32_16x16x32_bf16 v[96:99], a[52:55], a[76:79], v[96:99]\n"
        "  v_lshl_add_u32 v235, v205, 4, s59\n"
        "  v_mfma_f32_16x16x32_bf16 v[100:103], a[52:55], a[80:83], v[100:103]\n"
        "  v_mfma_f32_16x16x32_bf16 v[16:19], a[64:67], a[52:55], v[16:19]\n"
        "  v_mfma_f32_16x16x32_bf16 v[64:67], a[68:71], a[56:59], v[64:67]\n"
        "  v_mfma_f32_16x16x32_bf16 v[68:71], a[72:75], a[56:59], v[68:71]\n"
        "  v_mfma_f32_16x16x32_bf16 v[104:107], a[56:59], a[76:79], v[104:107]\n"
        "  v_mfma_f32_16x16x32_bf16 v[108:111], a[56:59], a[80:83], v[108:111]\n"
        "  v_mfma_f32_16x16x32_bf16 v[24:27], a[64:67], a[56:59], v[24:27]\n"
        "  s_waitcnt lgkmcnt(0)\n"
        "  s_barrier\n"
        "  ds_read_b128 v[152:155], v234 offset:2048\n"
        "  ds_read_b128 v[156:159], v234 offset:0\n"
        "  ds_read_b128 v[160:163], v234 offset:1024\n"
        "  ds_read_b128 v[32:35], v233 offset:0\n"
        "  ds_read_b128 v[36:39], v233 offset:1024\n"
        ".Lae:\n"
        "  ds_read_b32 v223, v230 offset:0\n"
        "  ds_read_b32 v224, v230 offset:64\n"
        "  ds_read_b32 v225, v230 offset:128\n"
        "  ds_read_b32 v226, v230 offset:192\n"
        "  ds_read2_b32 v[228:229], v232 offset0:0 offset1:160\n"
        "  v_lshlrev_b32 v232, 4, v206\n"
        "  v_add_u32 v232, 147968, v232\n"
        "  s_waitcnt lgkmcnt(5)\n"
        "  v_pk_fma_f32 v[0:1], v[218:219], v[148:149], v[0:1] op_sel:[0,0,0] op_sel_hi:[0,1,1]\n"
        "  v_pk_fma_f32 v[2:3], v[218:219], v[150:151], v[2:3] op_sel:[0,0,0] op_sel_hi:[0,1,1]\n"
        "  v_pk_fma_f32 v[4:5], v[218:219], v[152:153], v[4:5] op_sel:[0,0,0] op_sel_hi:[0,1,1]\n"
        "  v_pk_fma_f32 v[6:7], v[218:219], v[154:155], v[6:7] op_sel:[0,0,0] op_sel_hi:[0,1,1]\n"
        "  v_pk_fma_f32 v[8:9], v[218:219], v[148:149], v[8:9] op_sel:[1,0,0] op_sel_hi:[1,1,1]\n"
        "  v_pk_fma_f32 v[10:11], v[218:219], v[150:151], v[10:11] op_sel:[1,0,0] op_sel_hi:[1,1,1]\n"
        "  v_pk_fma_f32 v[12:13], v[218:219], v[152:153], v[12:13] op_sel:[1,0,0] op_sel_hi:[1,1,1]\n"
        "  v_pk_fma_f32 v[14:15], v[218:219], v[154:155], v[14:15] op_sel:[1,0,0] op_sel_hi:[1,1,1]\n"
        "  s_add_u32 m0, s59, 0\n"
        "  s_nop 0  ; hazard\n"
        "  buffer_load_dwordx4 v172, s[16:19], s60 offen offset:0 lds\n"
        "  v_pk_fma_f32 v[16:17], v[220:221], v[148:149], v[16:17] op_sel:[0,0,0] op_sel_hi:[0,1,1]\n"
        "  v_pk_fma_f32 v[18:19], v[220:221], v[150:151], v[18:19] op_sel:[0,0,0] op_sel_hi:[0,1,1]\n"
        "  v_pk_fma_f32 v[20:21], v[220:221], v[152:153], v[20:21] op_sel:[0,0,0] op_sel_hi:[0,1,1]\n"
        "  v_pk_fma_f32 v[22:23], v[220:221], v[154:155], v[22:23] op_sel:[0,0,0] op_sel_hi:[0,1,1]\n"
        "  v_pk_fma_f32 v[24:25], v[220:221], v[148:149], v[24:25] op_sel:[1,0,0] op_sel_hi:[1,1,1]\n"
        "  v_pk_fma_f32 v[26:27], v[220:221], v[150:151], v[26:27] op_sel:[1,0,0] op_sel_hi:[1,1,1]\n"
        "  v_pk_fma_f32 v[28:29], v[220:221], v[152:153], v[28:29] op_sel:[1,0,0] op_sel_hi:[1,1,1]\n"
        "  v_pk_fma_f32 v[30:31], v[220:221], v[154:155], v[30:31] op_sel:[1,0,0] op_sel_hi:[1,1,1]\n"
        "  v_cvt_pk_bf16_f32 v0, v0, v1\n"
        "  v_cvt_pk_bf16_f32 v1, v2, v3\n"
        "  v_cvt_pk_bf16_f32 v2, v4, v5\n"
        "  v_cvt_pk_bf16_f32 v3, v6, v7\n"
        "  v_cvt_pk_bf16_f32 v8, v8, v9\n"
        "  v_cvt_pk_bf16_f32 v9, v10, v11\n"
        "  v_cvt_pk_bf16_f32 v10, v12, v13\n"
        "  v_cvt_pk_bf16_f32 v11, v14, v15\n"
        "  v_cvt_pk_bf16_f32 v16, v16, v17\n"
        "  v_cvt_pk_bf16_f32 v17, v18, v19\n"
        "  v_cvt_pk_bf16_f32 v18, v20, v21\n"
        "  v_cvt_pk_bf16_f32 v19, v22, v23\n"
        "  v_cvt_pk_bf16_f32 v24, v24, v25\n"
        "  v_cvt_pk_bf16_f32 v25, v26, v27\n"
        "  v_cvt_pk_bf16_f32 v26, v28, v29\n"
        "  v_cvt_pk_bf16_f32 v27, v30, v31\n"
        "  buffer_load_dwordx4 v173, s[16:19], s60 offen offset:1024 lds\n"
        "  ds_read_b128 v[4:7], v231 offset:0\n"
        "  ds_read_b128 v[120:123], v231 offset:640\n"
        "  ds_read_b128 v[12:15], v231 offset:64\n"
        "  ds_read_b128 v[124:127], v231 offset:704\n"
        "  ds_read_b128 v[20:23], v231 offset:128\n"
        "  ds_read_b128 v[128:131], v231 offset:768\n"
        "  ds_read_b128 v[28:31], v231 offset:192\n"
        "  ds_read_b128 v[132:135], v231 offset:832\n"
        "  ds_read_b128 v[112:115], v232 offset:0\n"
        "  ds_read_b128 v[136:139], v232 offset:640\n"
        "  v_pk_fma_f32 v[40:41], v[218:219], v[156:157], v[40:41] op_sel:[0,0,0] op_sel_hi:[0,1,1]\n"
        "  v_pk_fma_f32 v[42:43], v[218:219], v[158:159], v[42:43] op_sel:[0,0,0] op_sel_hi:[0,1,1]\n"
        "  v_pk_fma_f32 v[44:45], v[218:219], v[160:161], v[44:45] op_sel:[0,0,0] op_sel_hi:[0,1,1]\n"
        "  v_pk_fma_f32 v[46:47], v[218:219], v[162:163], v[46:47] op_sel:[0,0,0] op_sel_hi:[0,1,1]\n"
        "  v_pk_fma_f32 v[48:49], v[218:219], v[156:157], v[48:49] op_sel:[1,0,0] op_sel_hi:[1,1,1]\n"
        "  v_pk_fma_f32 v[50:51], v[218:219], v[158:159], v[50:51] op_sel:[1,0,0] op_sel_hi:[1,1,1]\n"
        "  v_pk_fma_f32 v[52:53], v[218:219], v[160:161], v[52:53] op_sel:[1,0,0] op_sel_hi:[1,1,1]\n"
        "  v_pk_fma_f32 v[54:55], v[218:219], v[162:163], v[54:55] op_sel:[1,0,0] op_sel_hi:[1,1,1]\n"
        "  v_pk_fma_f32 v[56:57], v[220:221], v[156:157], v[56:57] op_sel:[0,0,0] op_sel_hi:[0,1,1]\n"
        "  v_pk_fma_f32 v[58:59], v[220:221], v[158:159], v[58:59] op_sel:[0,0,0] op_sel_hi:[0,1,1]\n"
        "  v_pk_fma_f32 v[60:61], v[220:221], v[160:161], v[60:61] op_sel:[0,0,0] op_sel_hi:[0,1,1]\n"
        "  v_pk_fma_f32 v[62:63], v[220:221], v[162:163], v[62:63] op_sel:[0,0,0] op_sel_hi:[0,1,1]\n"
        "  v_pk_fma_f32 v[64:65], v[220:221], v[156:157], v[64:65] op_sel:[1,0,0] op_sel_hi:[1,1,1]\n"
        "  v_pk_fma_f32 v[66:67], v[220:221], v[158:159], v[66:67] op_sel:[1,0,0] op_sel_hi:[1,1,1]\n"
        "  buffer_load_dwordx4 v174, s[16:19], s60 offen offset:2048 lds\n"
        "  v_pk_fma_f32 v[68:69], v[220:221], v[160:161], v[68:69] op_sel:[1,0,0] op_sel_hi:[1,1,1]\n"
        "  v_pk_fma_f32 v[70:71], v[220:221], v[162:163], v[70:71] op_sel:[1,0,0] op_sel_hi:[1,1,1]\n"
        "  s_waitcnt lgkmcnt(10)  ; auto\n"
        "  v_pk_mul_f32 v[32:33], v[32:33], v[228:229] op_sel:[0,0] op_sel_hi:[1,0]\n"
        "  v_pk_mul_f32 v[34:35], v[34:35], v[228:229] op_sel:[0,0] op_sel_hi:[1,0]\n"
        "  v_pk_mul_f32 v[36:37], v[36:37], v[228:229] op_sel:[0,0] op_sel_hi:[1,0]\n"
        "  v_pk_mul_f32 v[38:39], v[38:39], v[228:229] op_sel:[0,0] op_sel_hi:[1,0]\n"
        "  v_pk_fma_f32 v[32:33], v[228:229], v[156:157], v[32:33] op_sel:[1,0,0] op_sel_hi:[1,1,1]\n"
        "  v_pk_fma_f32 v[34:35], v[228:229], v[158:159], v[34:35] op_sel:[1,0,0] op_sel_hi:[1,1,1]\n"
        "  v_pk_fma_f32 v[36:37], v[228:229], v[160:161], v[36:37] op_sel:[1,0,0] op_sel_hi:[1,1,1]\n"
        "  v_pk_fma_f32 v[38:39], v[228:229], v[162:163], v[38:39] op_sel:[1,0,0] op_sel_hi:[1,1,1]\n"
        "  v_pk_mul_f32 v[40:41], v[40:41], v[222:223] op_sel:[0,1] op_sel_hi:[1,1]\n"
        "  v_pk_mul_f32 v[42:43], v[42:43], v[222:223] op_sel:[0,1] op_sel_hi:[1,1]\n"
        "  v_pk_mul_f32 v[44:45], v[44:45], v[222:223] op_sel:[0,1] op_sel_hi:[1,1]\n"
        "  v_pk_mul_f32 v[46:47], v[46:47], v[222:223] op_sel:[0,1] op_sel_hi:[1,1]\n"
        "  v_pk_mul_f32 v[48:49], v[48:49], v[224:225] op_sel:[0,0] op_sel_hi:[1,0]\n"
        "  v_pk_mul_f32 v[50:51], v[50:51], v[224:225] op_sel:[0,0] op_sel_hi:[1,0]\n"
        "  v_pk_mul_f32 v[52:53], v[52:53], v[224:225] op_sel:[0,0] op_sel_hi:[1,0]\n"
        "  v_pk_mul_f32 v[54:55], v[54:55], v[224:225] op_sel:[0,0] op_sel_hi:[1,0]\n"
        "  v_pk_mul_f32 v[56:57], v[56:57], v[224:225] op_sel:[0,1] op_sel_hi:[1,1]\n"
        "  v_pk_mul_f32 v[58:59], v[58:59], v[224:225] op_sel:[0,1] op_sel_hi:[1,1]\n"
        "  v_pk_mul_f32 v[60:61], v[60:61], v[224:225] op_sel:[0,1] op_sel_hi:[1,1]\n"
        "  v_pk_mul_f32 v[62:63], v[62:63], v[224:225] op_sel:[0,1] op_sel_hi:[1,1]\n"
        "  v_pk_mul_f32 v[64:65], v[64:65], v[226:227] op_sel:[0,0] op_sel_hi:[1,0]\n"
        "  v_pk_mul_f32 v[66:67], v[66:67], v[226:227] op_sel:[0,0] op_sel_hi:[1,0]\n"
        "  buffer_load_dwordx4 v175, s[16:19], s60 offen offset:3072 lds\n"
        "  v_pk_mul_f32 v[68:69], v[68:69], v[226:227] op_sel:[0,0] op_sel_hi:[1,0]\n"
        "  v_pk_mul_f32 v[70:71], v[70:71], v[226:227] op_sel:[0,0] op_sel_hi:[1,0]\n"
        "  v_cvt_pk_bf16_f32 v32, v32, v33\n"
        "  v_cvt_pk_bf16_f32 v33, v34, v35\n"
        "  v_cvt_pk_bf16_f32 v34, v36, v37\n"
        "  v_cvt_pk_bf16_f32 v35, v38, v39\n"
        "  v_cvt_pk_bf16_f32 v40, v40, v41\n"
        "  v_cvt_pk_bf16_f32 v41, v42, v43\n"
        "  v_cvt_pk_bf16_f32 v42, v44, v45\n"
        "  v_cvt_pk_bf16_f32 v43, v46, v47\n"
        "  v_cvt_pk_bf16_f32 v48, v48, v49\n"
        "  v_cvt_pk_bf16_f32 v49, v50, v51\n"
        "  v_cvt_pk_bf16_f32 v50, v52, v53\n"
        "  v_cvt_pk_bf16_f32 v51, v54, v55\n"
        "  v_cvt_pk_bf16_f32 v56, v56, v57\n"
        "  v_cvt_pk_bf16_f32 v57, v58, v59\n"
        "  v_cvt_pk_bf16_f32 v58, v60, v61\n"
        "  v_cvt_pk_bf16_f32 v59, v62, v63\n"
        "  v_cvt_pk_bf16_f32 v64, v64, v65\n"
        "  v_cvt_pk_bf16_f32 v65, v66, v67\n"
        "  v_cvt_pk_bf16_f32 v66, v68, v69\n"
        "  v_cvt_pk_bf16_f32 v67, v70, v71\n"
        "  v_mfma_f32_16x16x32_bf16 v[236:239], v[32:35], v[0:3], v[200:203]\n"
        "  v_mul_f32 v208, 0x3e8b97f6, v223\n"
        "  s_add_u32 s60, s60, 28672\n"
        "  s_add_u32 m0, s59, 4096\n"
        "  s_nop 0  ; hazard\n"
        "  buffer_load_dwordx4 v172, s[16:19], s60 offen offset:0 lds\n"
        "  v_mfma_f32_16x16x32_bf16 v[240:243], v[40:43], v[0:3], v[196:199]\n"
        "  v_mul_f32 v209, 0x3e8b97f6, v224\n"
        "  v_mfma_f32_16x16x32_bf16 v[140:143], v[40:43], v[8:11], v[192:195]\n"
        "  v_mul_f32 v210, 0x3e8b97f6, v225\n"
        "  v_mfma_f32_16x16x32_bf16 v[144:147], v[48:51], v[8:11], v[196:199]\n"
        "  v_mul_f32 v211, 0x3e8b97f6, v226\n"
        "  v_mfma_f32_16x16x32_bf16 v[148:151], v[48:51], v[16:19], v[192:195]\n"
        "  v_mfma_f32_16x16x32_bf16 v[152:155], v[56:59], v[16:19], v[196:199]\n"
        "  v_mfma_f32_16x16x32_bf16 v[244:247], v[56:59], v[24:27], v[192:195]\n"
        "  v_mfma_f32_16x16x32_bf16 v[248:251], v[64:67], v[24:27], v[196:199]\n"
        "  s_waitcnt lgkmcnt(1)  ; auto\n"
        "  v_pk_mul_f32 v[72:73], v[72:73], v[112:113] op_sel:[0,0] op_sel_hi:[1,1]\n"
        "  v_pk_mul_f32 v[74:75], v[74:75], v[114:115] op_sel:[0,0] op_sel_hi:[1,1]\n"
        "  v_pk_mul_f32 v[76:77], v[76:77], v[112:113] op_sel:[0,0] op_sel_hi:[1,1]\n"
        "  v_pk_mul_f32 v[78:79], v[78:79], v[114:115] op_sel:[0,0] op_sel_hi:[1,1]\n"
        "  v_pk_mul_f32 v[80:81], v[80:81], v[4:5] op_sel:[0,0] op_sel_hi:[1,1]\n"
        "  v_pk_mul_f32 v[82:83], v[82:83], v[6:7] op_sel:[0,0] op_sel_hi:[1,1]\n"
        "  v_pk_mul_f32 v[84:85], v[84:85], v[4:5] op_sel:[0,0] op_sel_hi:[1,1]\n"
        "  v_pk_mul_f32 v[86:87], v[86:87], v[6:7] op_sel:[0,0] op_sel_hi:[1,1]\n"
        "  v_pk_mul_f32 v[88:89], v[88:89], v[12:13] op_sel:[0,0] op_sel_hi:[1,1]\n"
        "  v_pk_mul_f32 v[90:91], v[90:91], v[14:15] op_sel:[0,0] op_sel_hi:[1,1]\n"
        "  v_pk_mul_f32 v[92:93], v[92:93], v[12:13] op_sel:[0,0] op_sel_hi:[1,1]\n"
        "  v_pk_mul_f32 v[94:95], v[94:95], v[14:15] op_sel:[0,0] op_sel_hi:[1,1]\n"
        "  v_pk_mul_f32 v[96:97], v[96:97], v[20:21] op_sel:[0,0] op_sel_hi:[1,1]\n"
        "  v_pk_mul_f32 v[98:99], v[98:99], v[22:23] op_sel:[0,0] op_sel_hi:[1,1]\n"
        "  buffer_load_dwordx4 v173, s[16:19], s60 offen offset:1024 lds\n"
        "  v_pk_mul_f32 v[100:101], v[100:101], v[20:21] op_sel:[0,0] op_sel_hi:[1,1]\n"
        "  v_pk_mul_f32 v[102:103], v[102:103], v[22:23] op_sel:[0,0] op_sel_hi:[1,1]\n"
        "  v_pk_mul_f32 v[104:105], v[104:105], v[28:29] op_sel:[0,0] op_sel_hi:[1,1]\n"
        "  v_pk_mul_f32 v[106:107], v[106:107], v[30:31] op_sel:[0,0] op_sel_hi:[1,1]\n"
        "  v_pk_mul_f32 v[108:109], v[108:109], v[28:29] op_sel:[0,0] op_sel_hi:[1,1]\n"
        "  v_pk_mul_f32 v[110:111], v[110:111], v[30:31] op_sel:[0,0] op_sel_hi:[1,1]\n"
        "  s_waitcnt lgkmcnt(0)  ; auto\n"
        "  v_pk_fma_f32 v[72:73], v[136:137], v[164:165], v[72:73] op_sel:[0,0,0] op_sel_hi:[1,0,1]\n"
        "  v_pk_fma_f32 v[74:75], v[138:139], v[164:165], v[74:75] op_sel:[0,0,0] op_sel_hi:[1,0,1]\n"
        "  v_pk_fma_f32 v[76:77], v[136:137], v[164:165], v[76:77] op_sel:[0,1,0] op_sel_hi:[1,1,1]\n"
        "  v_pk_fma_f32 v[78:79], v[138:139], v[164:165], v[78:79] op_sel:[0,1,0] op_sel_hi:[1,1,1]\n"
        "  v_pk_fma_f32 v[80:81], v[120:121], v[164:165], v[80:81] op_sel:[0,0,0] op_sel_hi:[1,0,1]\n"
        "  v_pk_fma_f32 v[82:83], v[122:123], v[164:165], v[82:83] op_sel:[0,0,0] op_sel_hi:[1,0,1]\n"
        "  v_pk_fma_f32 v[84:85], v[120:121], v[164:165], v[84:85] op_sel:[0,1,0] op_sel_hi:[1,1,1]\n"
        "  v_pk_fma_f32 v[86:87], v[122:123], v[164:165], v[86:87] op_sel:[0,1,0] op_sel_hi:[1,1,1]\n"
        "  v_pk_fma_f32 v[88:89], v[124:125], v[164:165], v[88:89] op_sel:[0,0,0] op_sel_hi:[1,0,1]\n"
        "  v_pk_fma_f32 v[90:91], v[126:127], v[164:165], v[90:91] op_sel:[0,0,0] op_sel_hi:[1,0,1]\n"
        "  v_pk_fma_f32 v[92:93], v[124:125], v[164:165], v[92:93] op_sel:[0,1,0] op_sel_hi:[1,1,1]\n"
        "  v_pk_fma_f32 v[94:95], v[126:127], v[164:165], v[94:95] op_sel:[0,1,0] op_sel_hi:[1,1,1]\n"
        "  v_pk_fma_f32 v[96:97], v[128:129], v[164:165], v[96:97] op_sel:[0,0,0] op_sel_hi:[1,0,1]\n"
        "  v_pk_fma_f32 v[98:99], v[130:131], v[164:165], v[98:99] op_sel:[0,0,0] op_sel_hi:[1,0,1]\n"
        "  v_pk_fma_f32 v[100:101], v[128:129], v[164:165], v[100:101] op_sel:[0,1,0] op_sel_hi:[1,1,1]\n"
        "  v_pk_fma_f32 v[102:103], v[130:131], v[164:165], v[102:103] op_sel:[0,1,0] op_sel_hi:[1,1,1]\n"
        "  v_pk_fma_f32 v[104:105], v[132:133], v[164:165], v[104:105] op_sel:[0,0,0] op_sel_hi:[1,0,1]\n"
        "  v_pk_fma_f32 v[106:107], v[134:135], v[164:165], v[106:107] op_sel:[0,0,0] op_sel_hi:[1,0,1]\n"
        "  v_pk_fma_f32 v[108:109], v[132:133], v[164:165], v[108:109] op_sel:[0,1,0] op_sel_hi:[1,1,1]\n"
        "  buffer_load_dwordx4 v174, s[16:19], s60 offen offset:2048 lds\n"
        "  v_pk_fma_f32 v[110:111], v[134:135], v[164:165], v[110:111] op_sel:[0,1,0] op_sel_hi:[1,1,1]\n"
        "  v_max_f32 v236, v236, v240\n"
        "  v_max_f32 v140, v140, v144\n"
        "  v_max_f32 v148, v148, v152\n"
        "  v_max_f32 v244, v244, v248\n"
        "  v_max_f32 v237, v237, v241\n"
        "  v_max_f32 v141, v141, v145\n"
        "  v_max_f32 v149, v149, v153\n"
        "  v_max_f32 v245, v245, v249\n"
        "  v_max_f32 v238, v238, v242\n"
        "  v_max_f32 v142, v142, v146\n"
        "  v_max_f32 v150, v150, v154\n"
        "  v_max_f32 v246, v246, v250\n"
        "  v_max_f32 v239, v239, v243\n"
        "  v_max_f32 v143, v143, v147\n"
        "  v_max_f32 v151, v151, v155\n"
        "  v_max_f32 v247, v247, v251\n"
        "  v_max3_f32 v213, v236, v237, v238\n"
        "  v_max3_f32 v214, v140, v141, v142\n"
        "  v_max3_f32 v215, v148, v149, v150\n"
        "  v_max3_f32 v216, v244, v245, v246\n"
        "  v_max_f32 v213, v213, v239\n"
        "  v_max_f32 v214, v214, v143\n"
        "  v_max_f32 v215, v215, v151\n"
        "  buffer_load_dwordx4 v175, s[16:19], s60 offen offset:3072 lds\n"
        "  v_max_f32 v216, v216, v247\n"
        "  v_cvt_pk_bf16_f32 v156, v72, v73\n"
        "  v_cvt_pk_bf16_f32 v157, v74, v75\n"
        "  v_cvt_pk_bf16_f32 v192, v76, v77\n"
        "  v_permlane16_swap_b32 v213, v214\n"
        "  v_permlane16_swap_b32 v215, v216\n"
        "  v_cvt_pk_bf16_f32 v193, v78, v79\n"
        "  v_cvt_pk_bf16_f32 v158, v80, v81\n"
        "  v_cvt_pk_bf16_f32 v159, v82, v83\n"
        "  v_max_f32 v213, v213, v214\n"
        "  v_max_f32 v215, v215, v216\n"
        "  v_cvt_pk_bf16_f32 v194, v84, v85\n"
        "  v_cvt_pk_bf16_f32 v195, v86, v87\n"
        "  v_cvt_pk_bf16_f32 v160, v88, v89\n"
        "  v_permlane32_swap_b32 v213, v215\n"
        "  v_cvt_pk_bf16_f32 v161, v90, v91\n"
        "  v_cvt_pk_bf16_f32 v196, v92, v93\n"
        "  v_cvt_pk_bf16_f32 v197, v94, v95\n"
        "  v_max_f32 v213, v213, v215\n"
        "  v_cvt_pk_bf16_f32 v162, v96, v97\n"
        "  v_cvt_pk_bf16_f32 v163, v98, v99\n"
        "  v_cvt_pk_bf16_f32 v198, v100, v101\n"
        "  v_mov_b32 v215, v213\n"
        "  v_cvt_pk_bf16_f32 v199, v102, v103\n"
        "  s_add_u32 s60, s60, 28672\n"
        "  s_add_u32 m0, s59, 8192\n"
        "  s_nop 0  ; hazard\n"
        "  buffer_load_dwordx4 v172, s[16:19], s60 offen offset:0 lds\n"
        "  v_cvt_pk_bf16_f32 v164, v104, v105\n"
        "  v_cvt_pk_bf16_f32 v165, v106, v107\n"
        "  v_permlane32_swap_b32 v213, v215\n"
        "  v_cvt_pk_bf16_f32 v200, v108, v109\n"
        "  v_cvt_pk_bf16_f32 v201, v110, v111\n"
        "  v_cndmask_b32_e64 v192, v192, v171, s[66:67]\n"
        "  v_mov_b32 v214, v213\n"
        "  v_mov_b32 v216, v215\n"
        "  v_cndmask_b32_e64 v193, v193, v171, s[66:67]\n"
        "  v_cndmask_b32_e64 v194, v194, v171, s[66:67]\n"
        "  v_cndmask_b32_e64 v195, v195, v171, s[66:67]\n"
        "  v_permlane16_swap_b32 v213, v214\n"
        "  v_permlane16_swap_b32 v215, v216\n"
        "  v_cndmask_b32_e64 v196, v196, v171, s[66:67]\n"
        "  v_cndmask_b32_e64 v197, v197, v171, s[66:67]\n"
        "  v_cndmask_b32_e64 v198, v198, v171, s[66:67]\n"
        "  v_cndmask_b32_e64 v199, v199, v171, s[66:67]\n"
        "  v_cndmask_b32_e64 v200, v200, v171, s[66:67]\n"
        "  v_cndmask_b32_e64 v201, v201, v171, s[66:67]\n"
        "  v_mul_f32 v213, v213, v208\n"
        "  v_mul_f32 v214, v214, v209\n"
        "  v_mul_f32 v215, v215, v210\n"
        "  v_mul_f32 v216, v216, v211\n"
        "  v_pk_fma_f32 v[236:237], v[236:237], v[208:209], v[212:213] op_sel:[0,0,1] op_sel_hi:[1,0,1] neg_lo:[0,0,1] neg_hi:[0,0,1]\n"
        "  buffer_load_dwordx4 v173, s[16:19], s60 offen offset:1024 lds\n"
        "  v_pk_fma_f32 v[140:141], v[140:141], v[208:209], v[214:215] op_sel:[0,1,0] op_sel_hi:[1,1,0] neg_lo:[0,0,1] neg_hi:[0,0,1]\n"
        "  v_pk_fma_f32 v[148:149], v[148:149], v[210:211], v[214:215] op_sel:[0,0,1] op_sel_hi:[1,0,1] neg_lo:[0,0,1] neg_hi:[0,0,1]\n"
        "  v_pk_fma_f32 v[244:245], v[244:245], v[210:211], v[216:217] op_sel:[0,1,0] op_sel_hi:[1,1,0] neg_lo:[0,0,1] neg_hi:[0,0,1]\n"
        "  v_pk_fma_f32 v[238:239], v[238:239], v[208:209], v[212:213] op_sel:[0,0,1] op_sel_hi:[1,0,1] neg_lo:[0,0,1] neg_hi:[0,0,1]\n"
        "  v_pk_fma_f32 v[142:143], v[142:143], v[208:209], v[214:215] op_sel:[0,1,0] op_sel_hi:[1,1,0] neg_lo:[0,0,1] neg_hi:[0,0,1]\n"
        "  v_pk_fma_f32 v[150:151], v[150:151], v[210:211], v[214:215] op_sel:[0,0,1] op_sel_hi:[1,0,1] neg_lo:[0,0,1] neg_hi:[0,0,1]\n"
        "  v_pk_fma_f32 v[246:247], v[246:247], v[210:211], v[216:217] op_sel:[0,1,0] op_sel_hi:[1,1,0] neg_lo:[0,0,1] neg_hi:[0,0,1]\n"
        "  v_exp_f32 v236, v236\n"
        "  v_exp_f32 v140, v140\n"
        "  v_exp_f32 v148, v148\n"
        "  v_exp_f32 v244, v244\n"
        "  v_exp_f32 v237, v237\n"
        "  v_exp_f32 v141, v141\n"
        "  v_exp_f32 v149, v149\n"
        "  v_exp_f32 v245, v245\n"
        "  v_exp_f32 v238, v238\n"
        "  v_exp_f32 v142, v142\n"
        "  v_exp_f32 v150, v150\n"
        "  v_exp_f32 v246, v246\n"
        "  v_exp_f32 v239, v239\n"
        "  v_exp_f32 v143, v143\n"
        "  v_exp_f32 v151, v151\n"
        "  v_exp_f32 v247, v247\n"
        "  v_cvt_pk_bf16_f32 v239, v238, v239\n"
        "  buffer_load_dwordx4 v174, s[16:19], s60 offen offset:2048 lds\n"
        "  v_cvt_pk_bf16_f32 v143, v142, v143\n"
        "  v_cvt_pk_bf16_f32 v151, v150, v151\n"
        "  v_cvt_pk_bf16_f32 v247, v246, v247\n"
        "  v_cvt_pk_bf16_f32 v238, v236, v237\n"
        "  v_cvt_pk_bf16_f32 v142, v140, v141\n"
        "  v_cvt_pk_bf16_f32 v150, v148, v149\n"
        "  v_cvt_pk_bf16_f32 v246, v244, v245\n"
        "  v_and_b32 v236, v170, v238\n"
        "  v_and_b32 v140, v170, v142\n"
        "  v_and_b32 v148, v170, v150\n"
        "  v_and_b32 v244, v170, v246\n"
        "  v_and_b32 v237, v254, v239\n"
        "  v_and_b32 v141, v254, v143\n"
        "  v_and_b32 v149, v254, v151\n"
        "  v_and_b32 v245, v254, v247\n"
        "  v_xor_b32 v238, v236, v238\n"
        "  v_xor_b32 v142, v140, v142\n"
        "  v_xor_b32 v150, v148, v150\n"
        "  v_xor_b32 v246, v244, v246\n"
        "  v_xor_b32 v239, v237, v239\n"
        "  v_xor_b32 v143, v141, v143\n"
        "  v_xor_b32 v151, v149, v151\n"
        "  v_xor_b32 v247, v245, v247\n"
        "  v_mfma_f32_16x16x32_bf16 v[76:79], v[192:195], v[236:239], 0\n"
        "  buffer_load_dwordx4 v175, s[16:19], s60 offen offset:3072 lds\n"
        "  s_add_u32 s60, s60, 28672\n"
        "  v_mfma_f32_16x16x32_bf16 v[84:87], v[194:197], v[140:143], 0\n"
        "  v_mfma_f32_16x16x32_bf16 v[92:95], v[196:199], v[148:151], 0\n"
        "  v_mfma_f32_16x16x32_bf16 v[100:103], v[198:201], v[244:247], 0\n"
        "  v_mfma_f32_16x16x32_bf16 v[72:75], v[156:159], v[236:239], 0\n"
        "  v_mfma_f32_16x16x32_bf16 v[80:83], v[158:161], v[140:143], 0\n"
        "  v_mfma_f32_16x16x32_bf16 v[88:91], v[160:163], v[148:151], 0\n"
        "  v_mfma_f32_16x16x32_bf16 v[96:99], v[162:165], v[244:247], 0\n"
        "  ds_bpermute_b32 v213, v207, v76\n"
        "  s_nop 0  ; hazard\n"
        "  ds_bpermute_b32 v214, v207, v84\n"
        "  ds_bpermute_b32 v215, v207, v92\n"
        "  ds_bpermute_b32 v216, v207, v100\n"
        "  s_waitcnt lgkmcnt(3)  ; auto\n"
        "  v_rcp_f32 v213, v213\n"
        "  s_waitcnt lgkmcnt(2)  ; auto\n"
        "  v_rcp_f32 v214, v214\n"
        "  s_waitcnt lgkmcnt(1)  ; auto\n"
        "  v_rcp_f32 v215, v215\n"
        "  s_waitcnt lgkmcnt(0)  ; auto\n"
        "  v_rcp_f32 v216, v216\n"
        "  v_pk_mul_f32 v[72:73], v[72:73], v[212:213] op_sel:[0,1] op_sel_hi:[1,1]\n"
        "  v_pk_mul_f32 v[80:81], v[80:81], v[214:215] op_sel:[0,0] op_sel_hi:[1,0]\n"
        "  v_pk_mul_f32 v[88:89], v[88:89], v[214:215] op_sel:[0,1] op_sel_hi:[1,1]\n"
        "  v_pk_mul_f32 v[96:97], v[96:97], v[216:217] op_sel:[0,0] op_sel_hi:[1,0]\n"
        "  v_pk_mul_f32 v[74:75], v[74:75], v[212:213] op_sel:[0,1] op_sel_hi:[1,1]\n"
        "  v_pk_mul_f32 v[82:83], v[82:83], v[214:215] op_sel:[0,0] op_sel_hi:[1,0]\n"
        "  v_pk_mul_f32 v[90:91], v[90:91], v[214:215] op_sel:[0,1] op_sel_hi:[1,1]\n"
        "  v_pk_mul_f32 v[98:99], v[98:99], v[216:217] op_sel:[0,0] op_sel_hi:[1,0]\n"
        "  v_pk_mul_f32 v[76:77], v[76:77], v[212:213] op_sel:[0,1] op_sel_hi:[1,1]\n"
        "  v_pk_mul_f32 v[84:85], v[84:85], v[214:215] op_sel:[0,0] op_sel_hi:[1,0]\n"
        "  v_pk_mul_f32 v[92:93], v[92:93], v[214:215] op_sel:[0,1] op_sel_hi:[1,1]\n"
        "  v_pk_mul_f32 v[100:101], v[100:101], v[216:217] op_sel:[0,0] op_sel_hi:[1,0]\n"
        "  v_pk_mul_f32 v[78:79], v[78:79], v[212:213] op_sel:[0,1] op_sel_hi:[1,1]\n"
        "  v_pk_mul_f32 v[86:87], v[86:87], v[214:215] op_sel:[0,0] op_sel_hi:[1,0]\n"
        "  v_pk_mul_f32 v[94:95], v[94:95], v[214:215] op_sel:[0,1] op_sel_hi:[1,1]\n"
        "  v_pk_mul_f32 v[102:103], v[102:103], v[216:217] op_sel:[0,0] op_sel_hi:[1,0]\n"
        "  v_cvt_pk_bf16_f32 v72, v72, v73\n"
        "  v_cvt_pk_bf16_f32 v80, v80, v81\n"
        "  v_cvt_pk_bf16_f32 v88, v88, v89\n"
        "  v_cvt_pk_bf16_f32 v96, v96, v97\n"
        "  v_cvt_pk_bf16_f32 v73, v74, v75\n"
        "  v_cvt_pk_bf16_f32 v81, v82, v83\n"
        "  v_cvt_pk_bf16_f32 v89, v90, v91\n"
        "  v_cvt_pk_bf16_f32 v97, v98, v99\n"
        "  v_cvt_pk_bf16_f32 v74, v76, v77\n"
        "  v_cvt_pk_bf16_f32 v82, v84, v85\n"
        "  v_cvt_pk_bf16_f32 v90, v92, v93\n"
        "  v_cvt_pk_bf16_f32 v98, v100, v101\n"
        "  v_cvt_pk_bf16_f32 v75, v78, v79\n"
        "  v_cvt_pk_bf16_f32 v83, v86, v87\n"
        "  v_cvt_pk_bf16_f32 v91, v94, v95\n"
        "  v_cvt_pk_bf16_f32 v99, v102, v103\n"
        "  buffer_store_dwordx4 v[72:75], v182, s[24:27], s82 offen offset:0 sc1\n"
        "  buffer_store_dwordx4 v[80:83], v182, s[24:27], s82 offen offset:1024 sc1\n"
        "  buffer_store_dwordx4 v[88:91], v182, s[24:27], s82 offen offset:2048 sc1\n"
        "  buffer_store_dwordx4 v[96:99], v182, s[24:27], s82 offen offset:3072 sc1\n"
        "  s_waitcnt vmcnt(16)\n"
        "  s_mov_b32 s87, 262144\n"
        "  ds_read_b64_tr_b16 a[96:97], v178 offset:49152\n"
        "  ds_read_b64_tr_b16 a[98:99], v178 offset:51200\n"
        "  ds_read_b64_tr_b16 a[100:101], v179 offset:49152\n"
        "  ds_read_b64_tr_b16 a[102:103], v179 offset:51200\n"
        "  ds_read_b64_tr_b16 a[104:105], v180 offset:49152\n"
        "  ds_read_b64_tr_b16 a[106:107], v180 offset:51200\n"
        "  ds_read_b64_tr_b16 a[108:109], v181 offset:49152\n"
        "  ds_read_b64_tr_b16 a[110:111], v181 offset:51200\n"
        "  ds_read_b64_tr_b16 a[112:113], v178 offset:53248\n"
        "  ds_read_b64_tr_b16 a[114:115], v178 offset:55296\n"
        "  ds_read_b64_tr_b16 a[116:117], v179 offset:53248\n"
        "  ds_read_b64_tr_b16 a[118:119], v179 offset:55296\n"
        "  ds_read_b64_tr_b16 a[120:121], v180 offset:53248\n"
        "  ds_read_b64_tr_b16 a[122:123], v180 offset:55296\n"
        "  ds_read_b64_tr_b16 a[124:125], v181 offset:53248\n"
        "  ds_read_b64_tr_b16 a[126:127], v181 offset:55296\n"
        "  ds_read_b64_tr_b16 a[128:129], v178 offset:57344\n"
        "  ds_read_b64_tr_b16 a[130:131], v178 offset:59392\n"
        "  ds_read_b64_tr_b16 a[132:133], v179 offset:57344\n"
        "  ds_read_b64_tr_b16 a[134:135], v179 offset:59392\n"
        "  ds_read_b64_tr_b16 a[136:137], v180 offset:57344\n"
        "  ds_read_b64_tr_b16 a[138:139], v180 offset:59392\n"
        "  ds_read_b64_tr_b16 a[140:141], v181 offset:57344\n"
        "  ds_read_b64_tr_b16 a[142:143], v181 offset:59392\n"
        "  s_add_u32 m0, s59, 49152\n"
        "  s_waitcnt lgkmcnt(15)  ; auto\n"
        "  buffer_load_dwordx4 v172, s[16:19], s60 offen offset:0 lds\n"
        "  buffer_load_dwordx4 v173, s[16:19], s60 offen offset:1024 lds\n"
        "  buffer_load_dwordx4 v174, s[16:19], s60 offen offset:2048 lds\n"
        "  buffer_load_dwordx4 v175, s[16:19], s60 offen offset:3072 lds\n"
        "  s_add_u32 s60, s60, 28672\n"
        "  s_add_u32 m0, s59, 53248\n"
        "  s_waitcnt lgkmcnt(8)  ; auto\n"
        "  buffer_load_dwordx4 v172, s[16:19], s60 offen offset:0 lds\n"
        "  buffer_load_dwordx4 v173, s[16:19], s60 offen offset:1024 lds\n"
        "  buffer_load_dwordx4 v174, s[16:19], s60 offen offset:2048 lds\n"
        "  buffer_load_dwordx4 v175, s[16:19], s60 offen offset:3072 lds\n"
        "  s_add_u32 s60, s60, 28672\n"
        "  s_add_u32 m0, s59, 57344\n"
        "  s_waitcnt lgkmcnt(0)  ; auto\n"
        "  buffer_load_dwordx4 v172, s[16:19], s60 offen offset:0 lds\n"
        "  buffer_load_dwordx4 v173, s[16:19], s60 offen offset:1024 lds\n"
        "  buffer_load_dwordx4 v174, s[16:19], s60 offen offset:2048 lds\n"
        "  buffer_load_dwordx4 v175, s[16:19], s60 offen offset:3072 lds\n"
        "  s_add_u32 s60, s60, 28672\n"
        "  s_waitcnt vmcnt(12)  ; auto\n"
        "  s_barrier\n"
        "  s_mov_b64 exec, s[78:79]\n"
        "  v_mov_b32 v190, v188\n"
        "  v_mov_b32 v186, v188\n"
        "  buffer_store_dwordx2 v[190:191], v185, s[24:27], 0 offen sc1\n"
        "  s_mov_b64 exec, -1\n"
        "  s_cmp_eq_u32 s86, 0\n"
        "  s_cbranch_scc1 .Lfin\n"
        "  s_branch .Lpub\n"
        ".Lffn:\n"
        "  s_sub_u32 s94, s42, 4\n"
        "  s_cmp_lt_u32 s94, 28\n"
        "  s_cselect_b32 s86, 1, 0\n"
        "  s_cselect_b32 s94, s94, 0\n"
        "  s_cselect_b32 s76, 15, 0\n"
        "  s_mov_b32 s77, 0\n"
        "  s_cmp_eq_u32 s40, 3\n"
        "  s_cselect_b32 s78, 0x30, 0xf0\n"
        "  s_mov_b32 s79, 0\n"
        "  s_or_b64 s[80:81], s[76:77], s[78:79]\n"
        "  s_and_b32 s97, s94, 1\n"
        "  s_bfe_u32 s95, s94, 0x10001\n"
        "  s_lshl1_add_u32 s96, s41, s95\n"
        "  s_lshr_b32 s94, s94, 2\n"
        "  s_lshl_b32 s63, s94, 7\n"
        "  s_lshl2_add_u32 s95, %[wg], s40\n"
        "  s_lshl_b32 s95, s95, 7\n"
        "  s_add_u32 s95, s95, 9437184\n"
        "  v_and_b32 v216, 3, v204\n"
        "  v_lshrrev_b32 v217, 2, v204\n"
        "  v_lshlrev_b32 v217, 17, v217\n"
        "  v_lshl_add_u32 v217, v216, 3, v217\n"
        "  s_add_u32 m0, s51, s56\n"
        "  s_nop 0  ; hazard\n"
        "  buffer_load_dwordx4 v148, s[4:7], s48 offen offset:0 lds\n"
        "  v_add_u32 v184, s95, v217\n"
        "  v_lshlrev_b32 v182, 4, v204\n"
        "  s_lshl1_add_u32 s94, s94, s97\n"
        "  s_lshl2_add_u32 s95, s96, s40\n"
        "  s_lshl_b32 s95, s95, 10\n"
        "  s_lshr_b32 s55, s94, 2\n"
        "  s_lshl_b32 s55, s55, 7\n"
        "  s_add_u32 s95, s95, s55\n"
        "  s_and_b32 s55, s94, 3\n"
        "  s_lshl3_add_u32 s95, s55, s95\n"
        "  s_add_u32 s95, s95, 9568256\n"
        "  v_and_b32 v217, 2, v216\n"
        "  v_mul_u32_u24 v217, 63, v217\n"
        "  v_add_u32 v217, v217, v216\n"
        "  v_lshl_add_u32 v217, v217, 9, s95\n"
        "  s_and_b32 s55, s43, 1\n"
        "  s_lshl_b32 s55, s55, 1\n"
        "  s_lshl3_add_u32 s95, s40, s55\n"
        "  s_add_u32 s88, s88, 1\n"
        "  buffer_load_dwordx4 v149, s[4:7], s48 offen offset:1024 lds\n"
        "  s_add_u32 s95, s95, 4\n"
        "  s_lshl_b32 s95, s95, 12\n"
        "  s_lshr_b32 s55, s43, 1\n"
        "  s_lshl2_add_u32 s55, s55, s44\n"
        "  s_lshl_b32 s55, s55, 7\n"
        "  s_add_u32 s95, s95, s55\n"
        "  s_lshl3_add_u32 s95, s45, s95\n"
        "  s_add_u32 s95, s95, 9437184\n"
        "  v_and_b32 v218, 2, v216\n"
        "  v_add_u32 v218, v218, v216\n"
        "  v_lshl_add_u32 v218, v218, 12, s95\n"
        "  v_cndmask_b32_e64 v217, v184, v217, s[76:77]\n"
        "  v_cndmask_b32_e64 v185, v217, v218, s[78:79]\n"
        "  v_mul_u32_u24 v216, 896, v208\n"
        "  v_lshl_add_u32 v216, v213, 5, v216\n"
        "  v_lshl_add_u32 v172, v214, 4, v216\n"
        "  v_add_u32 v173, 6144, v172\n"
        "  v_add_u32 v174, 12288, v172\n"
        "  s_cmp_eq_u32 s88, 7\n"
        "  s_cselect_b32 s88, 0, s88\n"
        "  s_cselect_b32 s89, -6, 1\n"
        "  buffer_load_dwordx4 v150, s[4:7], s48 offen offset:2048 lds\n"
        "  v_add_u32 v175, 18432, v172\n"
        "  v_add_u32 v176, 8704, v172\n"
        "  v_add_u32 v177, 14848, v172\n"
        "  s_mov_b32 exec_hi, 0\n"
        "  v_mov_b32 v176, 0x40000000\n"
        "  s_mov_b32 exec_hi, -1\n"
        "  s_mul_i32 s60, s40, 401408\n"
        "  s_add_u32 s60, s60, s63\n"
        "  s_mul_i32 s61, s40, 100352\n"
        "  s_add_u32 s61, s61, s63\n"
        "  s_mul_i32 s59, s40, 12288\n"
        "  s_lshl1_add_u32 s95, s96, s97\n"
        "  s_mul_i32 s62, s95, 294912\n"
        "  s_mul_i32 s95, s40, 73728\n"
        "  s_add_u32 s62, s62, s95\n"
        "  v_and_b32 v205, 15, v204\n"
        "  v_bfe_u32 v219, v204, 2, 3\n"
        "  v_lshlrev_b32 v216, 3, v204\n"
        "  v_and_b32 v216, 24, v216\n"
        "  v_lshl_add_u32 v220, v219, 7, v216\n"
        "  buffer_load_dwordx4 v151, s[4:7], s48 offen offset:3072 lds\n"
        "  v_lshrrev_b32 v217, 1, v219\n"
        "  v_lshl_add_u32 v220, v217, 5, v220\n"
        "  v_lshrrev_b32 v221, 5, v204\n"
        "  v_lshl_add_u32 v217, v221, 10, v220\n"
        "  v_add_u32 v178, s59, v217\n"
        "  v_xor_b32 v179, 32, v178\n"
        "  v_xor_b32 v180, 64, v178\n"
        "  v_xor_b32 v181, 96, v178\n"
        "  v_lshrrev_b32 v217, 1, v205\n"
        "  v_xor_b32 v217, v217, v206\n"
        "  v_lshlrev_b32 v217, 4, v217\n"
        "  v_lshl_add_u32 v160, v205, 7, v217\n"
        "  v_xor_b32 v216, v209, v208\n"
        "  v_mul_u32_u24 v217, 1792, v208\n"
        "  v_lshl_add_u32 v252, v216, 4, v217\n"
        "  v_add_u32 v253, 13312, v252\n"
        "  s_mov_b32 s70, 0xf0000f\n"
        "  s_mov_b32 s71, 0xf0000f00\n"
        "  s_add_u32 m0, s51, s57\n"
        "  s_nop 0  ; hazard\n"
        "  buffer_load_dwordx4 v153, s[8:11], s50 offen offset:0 lds\n"
        "  s_mov_b32 s72, 0xaaaaaaaa\n"
        "  s_mov_b32 s73, s72\n"
        "  s_mov_b32 s74, 0xcccccccc\n"
        "  s_mov_b32 s75, s74\n"
        "  v_mov_b32 v228, 0\n"
        "  v_mov_b32 v229, 0\n"
        "  v_mov_b32 v230, 0\n"
        "  v_mov_b32 v231, 0\n"
        "  s_and_b32 s94, s45, 1\n"
        "  s_lshl_b32 s95, s46, 13\n"
        "  v_add_u32 v160, s95, v160\n"
        "  v_xor_b32 v161, 64, v160\n"
        "  v_mul_u32_u24 v217, 7168, v221\n"
        "  v_add_u32 v217, 18432, v217\n"
        "  s_lshl_b32 s95, s46, 10\n"
        "  s_lshl_b32 s55, s46, 6\n"
        "  s_cmp_eq_u32 s47, 0\n"
        "  s_cselect_b32 s95, s95, 0x800\n"
        "  s_cselect_b32 s55, 0, s55\n"
        "  s_cselect_b64 vcc, -1, 0\n"
        "  v_xor_b32 v216, s55, v220\n"
        "  s_lshl_b32 s90, s89, 7\n"
        "  s_add_u32 s48, s48, s90\n"
        "  buffer_load_dwordx4 v154, s[8:11], s50 offen offset:1024 lds\n"
        "  v_add3_u32 v162, v216, v217, s95\n"
        "  s_xor_b32 s95, s95, 0x400\n"
        "  v_add3_u32 v166, v220, v217, s95\n"
        "  v_lshrrev_b32 v216, 2, v219\n"
        "  v_lshlrev_b32 v216, 5, v216\n"
        "  v_lshl_add_u32 v216, v219, 6, v216\n"
        "  v_and_b32 v218, 24, v220\n"
        "  v_add3_u32 v216, v216, v218, v217\n"
        "  v_add_u32 v216, 6144, v216\n"
        "  v_cndmask_b32 v166, v216, v166, vcc\n"
        "  v_xor_b32 v163, 32, v162\n"
        "  v_xor_b32 v167, 32, v166\n"
        "  v_xor_b32 v164, 64, v162\n"
        "  v_xor_b32 v168, 64, v166\n"
        "  v_xor_b32 v165, 96, v162\n"
        "  v_xor_b32 v169, 96, v166\n"
        "  s_mov_b32 s95, 0x3f803f80\n"
        "  v_accvgpr_write_b32 a128, s95\n"
        "  s_mov_b32 s68, s50\n"
        "  s_mov_b32 s69, s51\n"
        "  buffer_load_dwordx4 v155, s[8:11], s50 offen offset:2048 lds\n"
        "  v_accvgpr_write_b32 a129, s95\n"
        "  v_accvgpr_write_b32 a130, s95\n"
        "  v_accvgpr_write_b32 a131, s95\n"
        "  s_sub_u32 s55, 1, s47\n"
        "  s_lshl_b32 s83, s46, s55\n"
        "  s_lshl_b32 s55, s83, 7\n"
        "  v_lshlrev_b32 v216, 4, v206\n"
        "  v_add_u32 v199, 148480, v216\n"
        "  v_add_u32 v197, s55, v199\n"
        "  v_subrev_u32 v198, s55, v199\n"
        "  s_lshl_b32 s95, s94, 7\n"
        "  s_add_u32 s95, s95, -1024\n"
        "  v_add_u32 v194, s95, v197\n"
        "  v_add_u32 v195, s95, v198\n"
        "  s_mul_i32 s95, s94, -768\n"
        "  s_add_u32 s95, s95, 148224\n"
        "  v_add_u32 v196, s95, v216\n"
        "  s_lshl_b32 s95, s46, 8\n"
        "  s_add_u32 m0, s51, s58\n"
        "  s_mul_i32 s90, s89, s85\n"
        "  s_add_u32 s50, s50, s90\n"
        "  s_mov_b64 exec, s[64:65]\n"
        "  buffer_load_dwordx4 v152, s[12:15], s49 offen offset:0 lds\n"
        "  s_mov_b64 exec, -1\n"
        "  s_mul_i32 s90, s89, s84\n"
        "  s_add_u32 s49, s49, s90\n"
        "  s_add_u32 s95, s95, 150528\n"
        "  v_lshl_add_u32 v207, v205, 2, s95\n"
        "  s_mul_i32 s95, s44, 18\n"
        "  s_mul_i32 s55, s94, 7\n"
        "  s_add_u32 s95, s95, s55\n"
        "  s_lshl_b32 s95, s95, 12\n"
        "  s_lshl1_add_u32 s55, s43, s46\n"
        "  s_mul_i32 s55, s55, 294912\n"
        "  s_add_u32 s95, s95, s55\n"
        "  s_lshl_b32 s55, s94, 12\n"
        "  s_add_u32 s55, s95, s55\n"
        "  s_lshl_b32 s83, s83, 12\n"
        "  s_add_u32 s82, s55, s83\n"
        "  s_sub_u32 s91, s55, s83\n"
        "  s_mul_i32 s55, s94, -24576\n"
        "  s_add_u32 s55, s55, 24576\n"
        "  s_add_u32 s83, s95, s55\n"
        "  v_mfma_f32_32x32x16_bf16 v[0:15], v[228:231], v[228:231], 0\n"
        "  v_mfma_f32_32x32x16_bf16 v[16:31], v[228:231], v[228:231], 0\n"
        "  v_mfma_f32_32x32x16_bf16 v[32:47], v[228:231], v[228:231], 0\n"
        "  v_mfma_f32_32x32x16_bf16 v[48:63], v[228:231], v[228:231], 0\n"
        "  v_mfma_f32_32x32x16_bf16 v[64:79], v[228:231], v[228:231], 0\n"
        "  v_mfma_f32_32x32x16_bf16 v[80:95], v[228:231], v[228:231], 0\n"
        "  v_mfma_f32_32x32x16_bf16 v[96:111], v[228:231], v[228:231], 0\n"
        "  v_mfma_f32_32x32x16_bf16 v[112:127], v[228:231], v[228:231], 0\n"
        "  v_mfma_f32_32x32x16_bf16 v[128:143], v[228:231], v[228:231], 0\n"
        "  s_mov_b32 s51, 98304\n"
        "  s_mov_b64 exec, s[80:81]\n"
        "  v_mov_b32 v188, 0\n"
        "  v_mov_b32 v189, 0\n"
        "  buffer_atomic_or_x2 v[188:189], v184, s[24:27], 0 offen sc0 sc1\n"
        "  s_mov_b64 exec, -1\n"
        "  s_waitcnt vmcnt(9) lgkmcnt(0)\n"
        "  s_barrier\n"
        "  s_cmp_eq_u32 s47, 0\n"
        "  s_cbranch_scc1 .Lfw0\n"
        "  ds_read_b128 a[0:3], v160 offset:0\n"
        "  ds_read_b128 a[4:7], v160 offset:2048\n"
        "  ds_read_b128 a[8:11], v160 offset:4096\n"
        "  ds_read_b128 a[12:15], v160 offset:6144\n"
        "  ds_read_b64_tr_b16 a[16:17], v162 offset:0\n"
        "  ds_read_b64_tr_b16 a[18:19], v162 offset:3072\n"
        "  ds_read_b64_tr_b16 a[20:21], v163 offset:0\n"
        "  ds_read_b64_tr_b16 a[22:23], v163 offset:3072\n"
        "  ds_read_b64_tr_b16 a[24:25], v164 offset:0\n"
        "  ds_read_b64_tr_b16 a[26:27], v164 offset:3072\n"
        "  ds_read_b64_tr_b16 a[28:29], v165 offset:0\n"
        "  ds_read_b64_tr_b16 a[30:31], v165 offset:3072\n"
        "  ds_read_b64_tr_b16 a[32:33], v166 offset:0\n"
        "  ds_read_b64_tr_b16 a[34:35], v166 offset:512\n"
        "  ds_read_b64_tr_b16 a[36:37], v167 offset:0\n"
        "  ds_read_b64_tr_b16 a[38:39], v167 offset:512\n"
        "  s_mov_b32 s52, 49152\n"
        "  s_waitcnt lgkmcnt(15)  ; auto\n"
        "  v_mfma_f32_16x16x32_bf16 v[96:99], a[0:3], a[0:3], v[96:99]\n"
        "  s_add_u32 m0, s69, s87\n"
        "  ds_read_b128 a[48:51], v161 offset:0\n"
        "  s_waitcnt lgkmcnt(15)  ; auto\n"
        "  v_mfma_f32_16x16x32_bf16 v[100:103], a[4:7], a[4:7], v[100:103]\n"
        "  buffer_load_dwordx4 v156, s[8:11], s68 offen offset:0 lds\n"
        "  s_waitcnt lgkmcnt(14)  ; auto\n"
        "  v_mfma_f32_16x16x32_bf16 v[104:107], a[8:11], a[8:11], v[104:107]\n"
        "  ds_read_b128 a[52:55], v161 offset:2048\n"
        "  s_waitcnt lgkmcnt(14)  ; auto\n"
        "  v_mfma_f32_16x16x32_bf16 v[108:111], a[12:15], a[12:15], v[108:111]\n"
        "  buffer_load_dwordx4 v157, s[8:11], s68 offen offset:1024 lds\n"
        "  v_mfma_f32_16x16x32_bf16 v[112:115], a[128:131], a[0:3], v[112:115]\n"
        "  ds_read_b128 a[56:59], v161 offset:4096\n"
        "  v_mfma_f32_16x16x32_bf16 v[116:119], a[128:131], a[4:7], v[116:119]\n"
        "  ds_read_b128 a[60:63], v161 offset:6144\n"
        "  v_mfma_f32_16x16x32_bf16 v[120:123], a[128:131], a[8:11], v[120:123]\n"
        "  buffer_load_dwordx4 v158, s[8:11], s68 offen offset:2048 lds\n"
        "  v_mfma_f32_16x16x32_bf16 v[124:127], a[128:131], a[12:15], v[124:127]\n"
        "  s_add_u32 m0, s51, s56\n"
        "  ds_read_b64_tr_b16 a[64:65], v162 offset:14336\n"
        "  s_waitcnt lgkmcnt(15)  ; auto\n"
        "  v_mfma_f32_16x16x32_bf16 v[0:3], a[16:19], a[0:3], v[0:3]\n"
        "  ds_read_b64_tr_b16 a[66:67], v162 offset:17408\n"
        "  v_mfma_f32_16x16x32_bf16 v[24:27], a[16:19], a[4:7], v[24:27]\n"
        "  buffer_load_dwordx4 v148, s[4:7], s48 offen offset:0 lds\n"
        "  v_mfma_f32_16x16x32_bf16 v[48:51], a[16:19], a[8:11], v[48:51]\n"
        "  s_add_u32 s88, s88, 1\n"
        "  ds_read_b64_tr_b16 a[68:69], v163 offset:14336\n"
        "  v_mfma_f32_16x16x32_bf16 v[72:75], a[16:19], a[12:15], v[72:75]\n"
        "  ds_read_b64_tr_b16 a[70:71], v163 offset:17408\n"
        "  v_mfma_f32_16x16x32_bf16 v[128:131], a[16:19], a[128:131], v[128:131]\n"
        "  buffer_load_dwordx4 v149, s[4:7], s48 offen offset:1024 lds\n"
        "  s_waitcnt lgkmcnt(15)  ; auto\n"
        "  v_mfma_f32_16x16x32_bf16 v[4:7], a[20:23], a[0:3], v[4:7]\n"
        "  s_cmp_eq_u32 s88, 7\n"
        "  s_cselect_b32 s88, 0, s88\n"
        "  s_cselect_b32 s89, -6, 1\n"
        "  ds_read_b64_tr_b16 a[72:73], v164 offset:14336\n"
        "  v_mfma_f32_16x16x32_bf16 v[28:31], a[20:23], a[4:7], v[28:31]\n"
        "  ds_read_b64_tr_b16 a[74:75], v164 offset:17408\n"
        "  v_mfma_f32_16x16x32_bf16 v[52:55], a[20:23], a[8:11], v[52:55]\n"
        "  buffer_load_dwordx4 v150, s[4:7], s48 offen offset:2048 lds\n"
        "  v_mfma_f32_16x16x32_bf16 v[76:79], a[20:23], a[12:15], v[76:79]\n"
        "  ds_read_b64_tr_b16 a[76:77], v165 offset:14336\n"
        "  v_mfma_f32_16x16x32_bf16 v[132:135], a[20:23], a[128:131], v[132:135]\n"
        "  ds_read_b64_tr_b16 a[78:79], v165 offset:17408\n"
        "  s_waitcnt lgkmcnt(15)  ; auto\n"
        "  v_mfma_f32_16x16x32_bf16 v[8:11], a[24:27], a[0:3], v[8:11]\n"
        "  buffer_load_dwordx4 v151, s[4:7], s48 offen offset:3072 lds\n"
        "  v_mfma_f32_16x16x32_bf16 v[32:35], a[24:27], a[4:7], v[32:35]\n"
        "  s_add_u32 m0, s51, s57\n"
        "  ds_read_b64_tr_b16 a[80:81], v166 offset:14336\n"
        "  v_mfma_f32_16x16x32_bf16 v[56:59], a[24:27], a[8:11], v[56:59]\n"
        "  ds_read_b64_tr_b16 a[82:83], v166 offset:14848\n"
        "  v_mfma_f32_16x16x32_bf16 v[80:83], a[24:27], a[12:15], v[80:83]\n"
        "  buffer_load_dwordx4 v153, s[8:11], s50 offen offset:0 lds\n"
        "  v_mfma_f32_16x16x32_bf16 v[12:15], a[28:31], a[0:3], v[12:15]\n"
        "  s_lshl_b32 s90, s89, 7\n"
        "  ds_read_b64_tr_b16 a[84:85], v167 offset:14336\n"
        "  v_mfma_f32_16x16x32_bf16 v[36:39], a[28:31], a[4:7], v[36:39]\n"
        "  s_add_u32 s48, s48, s90\n"
        "  ds_read_b64_tr_b16 a[86:87], v167 offset:14848\n"
        "  v_add_u32 v160, s52, v160\n"
        "  v_mfma_f32_16x16x32_bf16 v[60:63], a[28:31], a[8:11], v[60:63]\n"
        "  buffer_load_dwordx4 v154, s[8:11], s50 offen offset:1024 lds\n"
        "  v_add_u32 v161, s52, v161\n"
        "  v_add_u32 v162, s52, v162\n"
        "  v_mfma_f32_16x16x32_bf16 v[84:87], a[28:31], a[12:15], v[84:87]\n"
        "  s_mov_b32 s68, s50\n"
        "  v_add_u32 v163, s52, v163\n"
        "  v_add_u32 v164, s52, v164\n"
        "  s_waitcnt lgkmcnt(15)  ; auto\n"
        "  v_mfma_f32_16x16x32_bf16 v[16:19], a[32:35], a[0:3], v[16:19]\n"
        "  s_mov_b32 s69, s51\n"
        "  v_add_u32 v165, s52, v165\n"
        "  v_add_u32 v166, s52, v166\n"
        "  v_mfma_f32_16x16x32_bf16 v[40:43], a[32:35], a[4:7], v[40:43]\n"
        "  buffer_load_dwordx4 v155, s[8:11], s50 offen offset:2048 lds\n"
        "  v_add_u32 v167, s52, v167\n"
        "  v_mfma_f32_16x16x32_bf16 v[64:67], a[32:35], a[8:11], v[64:67]\n"
        "  s_add_u32 m0, s51, s58\n"
        "  s_mul_i32 s90, s89, s85\n"
        "  v_mfma_f32_16x16x32_bf16 v[88:91], a[32:35], a[12:15], v[88:91]\n"
        "  s_add_u32 s50, s50, s90\n"
        "  v_mfma_f32_16x16x32_bf16 v[136:139], a[32:35], a[128:131], v[136:139]\n"
        "  s_mov_b64 exec, s[64:65]\n"
        "  buffer_load_dwordx4 v152, s[12:15], s49 offen offset:0 lds\n"
        "  s_mov_b64 exec, -1\n"
        "  v_mfma_f32_16x16x32_bf16 v[20:23], a[36:39], a[0:3], v[20:23]\n"
        "  s_mul_i32 s90, s89, s84\n"
        "  s_add_u32 s49, s49, s90\n"
        "  s_and_b32 s94, s45, 1\n"
        "  v_mfma_f32_16x16x32_bf16 v[44:47], a[36:39], a[4:7], v[44:47]\n"
        "  s_lshl1_add_u32 s95, s44, s94\n"
        "  s_mul_i32 s95, s95, 448\n"
        "  v_lshlrev_b32 v216, 3, v204\n"
        "  v_mfma_f32_16x16x32_bf16 v[68:71], a[36:39], a[8:11], v[68:71]\n"
        "  s_mov_b32 exec_hi, 0xffffff\n"
        "  buffer_load_dwordx2 v[192:193], v216, s[32:35], s95 offen\n"
        "  s_mov_b32 exec_hi, -1\n"
        "  v_mfma_f32_16x16x32_bf16 v[92:95], a[36:39], a[12:15], v[92:95]\n"
        "  s_mov_b32 s51, 0\n"
        "  s_mov_b32 s54, 49152\n"
        "  v_mfma_f32_16x16x32_bf16 v[140:143], a[36:39], a[128:131], v[140:143]\n"
        "  s_waitcnt vmcnt(9) lgkmcnt(0)\n"
        "  s_barrier\n"
        "  s_mov_b32 s53, 4\n"
        "  .p2align 3\n"
        ".Lf1_1:\n"
        "  v_mfma_f32_16x16x32_bf16 v[96:99], a[48:51], a[48:51], v[96:99]\n"
        "  s_add_u32 m0, s69, s87\n"
        "  ds_read_b128 a[0:3], v160 offset:0\n"
        "  v_mfma_f32_16x16x32_bf16 v[100:103], a[52:55], a[52:55], v[100:103]\n"
        "  buffer_load_dwordx4 v156, s[8:11], s68 offen offset:0 lds\n"
        "  v_mfma_f32_16x16x32_bf16 v[104:107], a[56:59], a[56:59], v[104:107]\n"
        "  ds_read_b128 a[4:7], v160 offset:2048\n"
        "  v_mfma_f32_16x16x32_bf16 v[108:111], a[60:63], a[60:63], v[108:111]\n"
        "  ds_read_b128 a[8:11], v160 offset:4096\n"
        "  v_mfma_f32_16x16x32_bf16 v[112:115], a[128:131], a[48:51], v[112:115]\n"
        "  ds_read_b128 a[12:15], v160 offset:6144\n"
        "  v_mfma_f32_16x16x32_bf16 v[116:119], a[128:131], a[52:55], v[116:119]\n"
        "  ds_read_b64_tr_b16 a[16:17], v162 offset:0\n"
        "  v_mfma_f32_16x16x32_bf16 v[120:123], a[128:131], a[56:59], v[120:123]\n"
        "  ds_read_b64_tr_b16 a[18:19], v162 offset:3072\n"
        "  v_mfma_f32_16x16x32_bf16 v[124:127], a[128:131], a[60:63], v[124:127]\n"
        "  buffer_load_dwordx4 v157, s[8:11], s68 offen offset:1024 lds\n"
        "  v_mfma_f32_16x16x32_bf16 v[0:3], a[64:67], a[48:51], v[0:3]\n"
        "  ds_read_b64_tr_b16 a[20:21], v163 offset:0\n"
        "  v_mfma_f32_16x16x32_bf16 v[24:27], a[64:67], a[52:55], v[24:27]\n"
        "  ds_read_b64_tr_b16 a[22:23], v163 offset:3072\n"
        "  v_mfma_f32_16x16x32_bf16 v[48:51], a[64:67], a[56:59], v[48:51]\n"
        "  ds_read_b64_tr_b16 a[24:25], v164 offset:0\n"
        "  v_mfma_f32_16x16x32_bf16 v[72:75], a[64:67], a[60:63], v[72:75]\n"
        "  ds_read_b64_tr_b16 a[26:27], v164 offset:3072\n"
        "  v_mfma_f32_16x16x32_bf16 v[128:131], a[64:67], a[128:131], v[128:131]\n"
        "  ds_read_b64_tr_b16 a[28:29], v165 offset:0\n"
        "  v_mfma_f32_16x16x32_bf16 v[4:7], a[68:71], a[48:51], v[4:7]\n"
        "  ds_read_b64_tr_b16 a[30:31], v165 offset:3072\n"
        "  v_mfma_f32_16x16x32_bf16 v[28:31], a[68:71], a[52:55], v[28:31]\n"
        "  buffer_load_dwordx4 v158, s[8:11], s68 offen offset:2048 lds\n"
        "  v_mfma_f32_16x16x32_bf16 v[52:55], a[68:71], a[56:59], v[52:55]\n"
        "  s_add_u32 m0, s51, s56\n"
        "  ds_read_b64_tr_b16 a[32:33], v166 offset:0\n"
        "  v_mfma_f32_16x16x32_bf16 v[76:79], a[68:71], a[60:63], v[76:79]\n"
        "  ds_read_b64_tr_b16 a[34:35], v166 offset:512\n"
        "  v_mfma_f32_16x16x32_bf16 v[132:135], a[68:71], a[128:131], v[132:135]\n"
        "  ds_read_b64_tr_b16 a[36:37], v167 offset:0\n"
        "  v_mfma_f32_16x16x32_bf16 v[8:11], a[72:75], a[48:51], v[8:11]\n"
        "  ds_read_b64_tr_b16 a[38:39], v167 offset:512\n"
        "  v_mfma_f32_16x16x32_bf16 v[32:35], a[72:75], a[52:55], v[32:35]\n"
        "  ds_read_b64_tr_b16 a[64:65], v162 offset:14336\n"
        "  v_mfma_f32_16x16x32_bf16 v[56:59], a[72:75], a[56:59], v[56:59]\n"
        "  buffer_load_dwordx4 v148, s[4:7], s48 offen offset:0 lds\n"
        "  v_mfma_f32_16x16x32_bf16 v[80:83], a[72:75], a[60:63], v[80:83]\n"
        "  s_add_u32 s88, s88, 1\n"
        "  ds_read_b64_tr_b16 a[66:67], v162 offset:17408\n"
        "  v_mfma_f32_16x16x32_bf16 v[12:15], a[76:79], a[48:51], v[12:15]\n"
        "  ds_read_b64_tr_b16 a[68:69], v163 offset:14336\n"
        "  v_mfma_f32_16x16x32_bf16 v[36:39], a[76:79], a[52:55], v[36:39]\n"
        "  ds_read_b64_tr_b16 a[70:71], v163 offset:17408\n"
        "  v_mfma_f32_16x16x32_bf16 v[60:63], a[76:79], a[56:59], v[60:63]\n"
        "  ds_read_b64_tr_b16 a[72:73], v164 offset:14336\n"
        "  v_mfma_f32_16x16x32_bf16 v[84:87], a[76:79], a[60:63], v[84:87]\n"
        "  ds_read_b64_tr_b16 a[74:75], v164 offset:17408\n"
        "  v_mfma_f32_16x16x32_bf16 v[16:19], a[80:83], a[48:51], v[16:19]\n"
        "  ds_read_b64_tr_b16 a[76:77], v165 offset:14336\n"
        "  v_mfma_f32_16x16x32_bf16 v[40:43], a[80:83], a[52:55], v[40:43]\n"
        "  buffer_load_dwordx4 v149, s[4:7], s48 offen offset:1024 lds\n"
        "  v_mfma_f32_16x16x32_bf16 v[64:67], a[80:83], a[56:59], v[64:67]\n"
        "  s_cmp_eq_u32 s88, 7\n"
        "  s_cselect_b32 s88, 0, s88\n"
        "  s_cselect_b32 s89, -6, 1\n"
        "  ds_read_b64_tr_b16 a[78:79], v165 offset:17408\n"
        "  v_mfma_f32_16x16x32_bf16 v[88:91], a[80:83], a[60:63], v[88:91]\n"
        "  v_mfma_f32_16x16x32_bf16 v[136:139], a[80:83], a[128:131], v[136:139]\n"
        "  v_mfma_f32_16x16x32_bf16 v[20:23], a[84:87], a[48:51], v[20:23]\n"
        "  ds_read_b64_tr_b16 a[80:81], v166 offset:14336\n"
        "  s_add_u32 s55, s54, 49152\n"
        "  v_mfma_f32_16x16x32_bf16 v[44:47], a[84:87], a[52:55], v[44:47]\n"
        "  ds_read_b64_tr_b16 a[82:83], v166 offset:14848\n"
        "  v_mfma_f32_16x16x32_bf16 v[68:71], a[84:87], a[56:59], v[68:71]\n"
        "  buffer_load_dwordx4 v150, s[4:7], s48 offen offset:2048 lds\n"
        "  s_cmp_eq_u32 s54, 98304\n"
        "  s_cselect_b32 s55, 0, s55\n"
        "  v_mfma_f32_16x16x32_bf16 v[92:95], a[84:87], a[60:63], v[92:95]\n"
        "  ds_read_b128 a[48:51], v161 offset:0\n"
        "  s_sub_u32 s52, s55, s54\n"
        "  v_mfma_f32_16x16x32_bf16 v[140:143], a[84:87], a[128:131], v[140:143]\n"
        "  ds_read_b128 a[52:55], v161 offset:2048\n"
        "  s_waitcnt lgkmcnt(12)\n"
        "  v_mfma_f32_16x16x32_bf16 v[96:99], a[0:3], a[0:3], v[96:99]\n"
        "  ds_read_b128 a[56:59], v161 offset:4096\n"
        "  v_mfma_f32_16x16x32_bf16 v[100:103], a[4:7], a[4:7], v[100:103]\n"
        "  ds_read_b128 a[60:63], v161 offset:6144\n"
        "  v_mfma_f32_16x16x32_bf16 v[104:107], a[8:11], a[8:11], v[104:107]\n"
        "  ds_read_b64_tr_b16 a[84:85], v167 offset:14336\n"
        "  v_mfma_f32_16x16x32_bf16 v[108:111], a[12:15], a[12:15], v[108:111]\n"
        "  ds_read_b64_tr_b16 a[86:87], v167 offset:14848\n"
        "  v_add_u32 v160, s52, v160\n"
        "  v_add_u32 v161, s52, v161\n"
        "  v_mfma_f32_16x16x32_bf16 v[112:115], a[128:131], a[0:3], v[112:115]\n"
        "  buffer_load_dwordx4 v151, s[4:7], s48 offen offset:3072 lds\n"
        "  v_add_u32 v162, s52, v162\n"
        "  v_add_u32 v163, s52, v163\n"
        "  v_mfma_f32_16x16x32_bf16 v[116:119], a[128:131], a[4:7], v[116:119]\n"
        "  s_add_u32 m0, s51, s57\n"
        "  v_add_u32 v164, s52, v164\n"
        "  v_add_u32 v165, s52, v165\n"
        "  v_mfma_f32_16x16x32_bf16 v[120:123], a[128:131], a[8:11], v[120:123]\n"
        "  v_add_u32 v166, s52, v166\n"
        "  v_add_u32 v167, s52, v167\n"
        "  v_mfma_f32_16x16x32_bf16 v[124:127], a[128:131], a[12:15], v[124:127]\n"
        "  v_mfma_f32_16x16x32_bf16 v[0:3], a[16:19], a[0:3], v[0:3]\n"
        "  v_mfma_f32_16x16x32_bf16 v[24:27], a[16:19], a[4:7], v[24:27]\n"
        "  v_mfma_f32_16x16x32_bf16 v[48:51], a[16:19], a[8:11], v[48:51]\n"
        "  buffer_load_dwordx4 v153, s[8:11], s50 offen offset:0 lds\n"
        "  v_mfma_f32_16x16x32_bf16 v[72:75], a[16:19], a[12:15], v[72:75]\n"
        "  s_lshl_b32 s90, s89, 7\n"
        "  v_mfma_f32_16x16x32_bf16 v[128:131], a[16:19], a[128:131], v[128:131]\n"
        "  v_mfma_f32_16x16x32_bf16 v[4:7], a[20:23], a[0:3], v[4:7]\n"
        "  v_mfma_f32_16x16x32_bf16 v[28:31], a[20:23], a[4:7], v[28:31]\n"
        "  s_add_u32 s48, s48, s90\n"
        "  v_mfma_f32_16x16x32_bf16 v[52:55], a[20:23], a[8:11], v[52:55]\n"
        "  v_mfma_f32_16x16x32_bf16 v[76:79], a[20:23], a[12:15], v[76:79]\n"
        "  v_mfma_f32_16x16x32_bf16 v[132:135], a[20:23], a[128:131], v[132:135]\n"
        "  buffer_load_dwordx4 v154, s[8:11], s50 offen offset:1024 lds\n"
        "  v_mfma_f32_16x16x32_bf16 v[8:11], a[24:27], a[0:3], v[8:11]\n"
        "  s_mov_b32 s68, s50\n"
        "  v_mfma_f32_16x16x32_bf16 v[32:35], a[24:27], a[4:7], v[32:35]\n"
        "  v_mfma_f32_16x16x32_bf16 v[56:59], a[24:27], a[8:11], v[56:59]\n"
        "  s_mov_b32 s69, s51\n"
        "  v_mfma_f32_16x16x32_bf16 v[80:83], a[24:27], a[12:15], v[80:83]\n"
        "  v_mfma_f32_16x16x32_bf16 v[12:15], a[28:31], a[0:3], v[12:15]\n"
        "  v_mfma_f32_16x16x32_bf16 v[36:39], a[28:31], a[4:7], v[36:39]\n"
        "  buffer_load_dwordx4 v155, s[8:11], s50 offen offset:2048 lds\n"
        "  v_mfma_f32_16x16x32_bf16 v[60:63], a[28:31], a[8:11], v[60:63]\n"
        "  s_add_u32 m0, s51, s58\n"
        "  v_mfma_f32_16x16x32_bf16 v[84:87], a[28:31], a[12:15], v[84:87]\n"
        "  v_mfma_f32_16x16x32_bf16 v[16:19], a[32:35], a[0:3], v[16:19]\n"
        "  s_mul_i32 s90, s89, s85\n"
        "  v_mfma_f32_16x16x32_bf16 v[40:43], a[32:35], a[4:7], v[40:43]\n"
        "  v_mfma_f32_16x16x32_bf16 v[64:67], a[32:35], a[8:11], v[64:67]\n"
        "  s_add_u32 s50, s50, s90\n"
        "  v_mfma_f32_16x16x32_bf16 v[88:91], a[32:35], a[12:15], v[88:91]\n"
        "  v_mfma_f32_16x16x32_bf16 v[136:139], a[32:35], a[128:131], v[136:139]\n"
        "  s_mov_b64 exec, s[64:65]\n"
        "  buffer_load_dwordx4 v152, s[12:15], s49 offen offset:0 lds\n"
        "  s_mov_b64 exec, -1\n"
        "  s_mul_i32 s90, s89, s84\n"
        "  v_mfma_f32_16x16x32_bf16 v[20:23], a[36:39], a[0:3], v[20:23]\n"
        "  s_add_u32 s49, s49, s90\n"
        "  v_mfma_f32_16x16x32_bf16 v[44:47], a[36:39], a[4:7], v[44:47]\n"
        "  v_mfma_f32_16x16x32_bf16 v[68:71], a[36:39], a[8:11], v[68:71]\n"
        "  s_mov_b32 s51, s54\n"
        "  s_mov_b32 s54, s55\n"
        "  v_mfma_f32_16x16x32_bf16 v[92:95], a[36:39], a[12:15], v[92:95]\n"
        "  s_sub_u32 s53, s53, 1\n"
        "  s_cmp_lg_u32 s53, 0\n"
        "  v_mfma_f32_16x16x32_bf16 v[140:143], a[36:39], a[128:131], v[140:143]\n"
        "  s_waitcnt vmcnt(8) lgkmcnt(0)\n"
        "  s_barrier\n"
        "  s_cbranch_scc1 .Lf1_1\n"
        "  v_mfma_f32_16x16x32_bf16 v[96:99], a[48:51], a[48:51], v[96:99]\n"
        "  s_lshr_b32 s94, s63, 7\n"
        "  s_add_u32 s89, s40, s94\n"
        "  s_and_b32 s89, s89, 3\n"
        "  ds_read_b128 a[0:3], v160 offset:0\n"
        "  v_mfma_f32_16x16x32_bf16 v[100:103], a[52:55], a[52:55], v[100:103]\n"
        "  s_mov_b32 s84, 0\n"
        "  s_mov_b32 s85, s84\n"
        "  s_lshl_b32 s88, s96, 7\n"
        "  ds_read_b128 a[4:7], v160 offset:2048\n"
        "  v_mfma_f32_16x16x32_bf16 v[104:107], a[56:59], a[56:59], v[104:107]\n"
        "  s_add_u32 s88, s88, s97\n"
        "  s_lshl_b32 s95, s89, 5\n"
        "  s_add_u32 s88, s88, s95\n"
        "  ds_read_b128 a[8:11], v160 offset:4096\n"
        "  v_mfma_f32_16x16x32_bf16 v[108:111], a[60:63], a[60:63], v[108:111]\n"
        "  s_add_u32 m0, s69, s87\n"
        "  ds_read_b128 a[12:15], v160 offset:6144\n"
        "  v_mfma_f32_16x16x32_bf16 v[112:115], a[128:131], a[48:51], v[112:115]\n"
        "  buffer_load_dwordx4 v156, s[8:11], s68 offen offset:0 lds\n"
        "  v_mfma_f32_16x16x32_bf16 v[116:119], a[128:131], a[52:55], v[116:119]\n"
        "  ds_read_b64_tr_b16 a[16:17], v162 offset:0\n"
        "  v_mfma_f32_16x16x32_bf16 v[120:123], a[128:131], a[56:59], v[120:123]\n"
        "  ds_read_b64_tr_b16 a[18:19], v162 offset:3072\n"
        "  v_mfma_f32_16x16x32_bf16 v[124:127], a[128:131], a[60:63], v[124:127]\n"
        "  ds_read_b64_tr_b16 a[20:21], v163 offset:0\n"
        "  v_mfma_f32_16x16x32_bf16 v[0:3], a[64:67], a[48:51], v[0:3]\n"
        "  buffer_load_dwordx4 v157, s[8:11], s68 offen offset:1024 lds\n"
        "  v_cndmask_b32_e64 v186, v189, v188, s[76:77]\n"
        "  v_mfma_f32_16x16x32_bf16 v[24:27], a[64:67], a[52:55], v[24:27]\n"
        "  ds_read_b64_tr_b16 a[22:23], v163 offset:3072\n"
        "  v_add_u32 v186, 1, v186\n"
        "  v_mfma_f32_16x16x32_bf16 v[48:51], a[64:67], a[56:59], v[48:51]\n"
        "  ds_read_b64_tr_b16 a[24:25], v164 offset:0\n"
        "  v_mov_b32 v191, v186\n"
        "  v_mfma_f32_16x16x32_bf16 v[72:75], a[64:67], a[60:63], v[72:75]\n"
        "  ds_read_b64_tr_b16 a[26:27], v164 offset:3072\n"
        "  s_mov_b64 exec, 15\n"
        "  buffer_store_dword v186, v185, s[24:27], 0 offen sc1\n"
        "  s_mov_b64 exec, -1\n"
        "  v_mfma_f32_16x16x32_bf16 v[128:131], a[64:67], a[128:131], v[128:131]\n"
        "  buffer_load_dwordx4 v158, s[8:11], s68 offen offset:2048 lds\n"
        "  v_lshlrev_b32 v208, 16, v192\n"
        "  v_mfma_f32_16x16x32_bf16 v[4:7], a[68:71], a[48:51], v[4:7]\n"
        "  s_add_u32 m0, s59, 49152\n"
        "  ds_read_b64_tr_b16 a[28:29], v165 offset:0\n"
        "  v_mfma_f32_16x16x32_bf16 v[28:31], a[68:71], a[52:55], v[28:31]\n"
        "  ds_read_b64_tr_b16 a[30:31], v165 offset:3072\n"
        "  v_and_b32 v209, 0xffff0000, v192\n"
        "  v_mfma_f32_16x16x32_bf16 v[52:55], a[68:71], a[56:59], v[52:55]\n"
        "  ds_read_b64_tr_b16 a[32:33], v166 offset:0\n"
        "  v_lshlrev_b32 v210, 16, v193\n"
        "  v_mfma_f32_16x16x32_bf16 v[76:79], a[68:71], a[60:63], v[76:79]\n"
        "  ds_read_b64_tr_b16 a[34:35], v166 offset:512\n"
        "  v_and_b32 v211, 0xffff0000, v193\n"
        "  v_mfma_f32_16x16x32_bf16 v[132:135], a[68:71], a[128:131], v[132:135]\n"
        "  buffer_load_dwordx4 v172, s[16:19], s60 offen offset:0 lds\n"
        "  v_add_u32 v212, 147456, v182\n"
        "  v_mfma_f32_16x16x32_bf16 v[8:11], a[72:75], a[48:51], v[8:11]\n"
        "  ds_read_b64_tr_b16 a[36:37], v167 offset:0\n"
        "  s_mov_b32 exec_hi, 0xffffff\n"
        "  ds_write_b128 v212, v[208:211]\n"
        "  s_mov_b32 exec_hi, -1\n"
        "  v_mfma_f32_16x16x32_bf16 v[32:35], a[72:75], a[52:55], v[32:35]\n"
        "  ds_read_b64_tr_b16 a[38:39], v167 offset:512\n"
        "  v_mfma_f32_16x16x32_bf16 v[56:59], a[72:75], a[56:59], v[56:59]\n"
        "  ds_read_b64_tr_b16 a[64:65], v162 offset:14336\n"
        "  v_mfma_f32_16x16x32_bf16 v[80:83], a[72:75], a[60:63], v[80:83]\n"
        "  buffer_load_dwordx4 v173, s[16:19], s60 offen offset:1024 lds\n"
        "  v_mfma_f32_16x16x32_bf16 v[12:15], a[76:79], a[48:51], v[12:15]\n"
        "  ds_read_b64_tr_b16 a[66:67], v162 offset:17408\n"
        "  v_mfma_f32_16x16x32_bf16 v[36:39], a[76:79], a[52:55], v[36:39]\n"
        "  ds_read_b64_tr_b16 a[68:69], v163 offset:14336\n"
        "  v_mfma_f32_16x16x32_bf16 v[60:63], a[76:79], a[56:59], v[60:63]\n"
        "  ds_read_b64_tr_b16 a[70:71], v163 offset:17408\n"
        "  v_mfma_f32_16x16x32_bf16 v[84:87], a[76:79], a[60:63], v[84:87]\n"
        "  ds_read_b64_tr_b16 a[72:73], v164 offset:14336\n"
        "  v_mfma_f32_16x16x32_bf16 v[16:19], a[80:83], a[48:51], v[16:19]\n"
        "  buffer_load_dwordx4 v174, s[16:19], s60 offen offset:2048 lds\n"
        "  v_mfma_f32_16x16x32_bf16 v[40:43], a[80:83], a[52:55], v[40:43]\n"
        "  ds_read_b64_tr_b16 a[74:75], v164 offset:17408\n"
        "  v_mfma_f32_16x16x32_bf16 v[64:67], a[80:83], a[56:59], v[64:67]\n"
        "  ds_read_b64_tr_b16 a[76:77], v165 offset:14336\n"
        "  v_mfma_f32_16x16x32_bf16 v[88:91], a[80:83], a[60:63], v[88:91]\n"
        "  ds_read_b64_tr_b16 a[78:79], v165 offset:17408\n"
        "  v_mfma_f32_16x16x32_bf16 v[136:139], a[80:83], a[128:131], v[136:139]\n"
        "  buffer_load_dwordx4 v175, s[16:19], s60 offen offset:3072 lds\n"
        "  v_mfma_f32_16x16x32_bf16 v[20:23], a[84:87], a[48:51], v[20:23]\n"
        "  s_add_u32 s60, s60, 28672\n"
        "  ds_read_b64_tr_b16 a[80:81], v166 offset:14336\n"
        "  s_add_u32 s55, s54, 49152\n"
        "  v_mfma_f32_16x16x32_bf16 v[44:47], a[84:87], a[52:55], v[44:47]\n"
        "  ds_read_b64_tr_b16 a[82:83], v166 offset:14848\n"
        "  v_mfma_f32_16x16x32_bf16 v[68:71], a[84:87], a[56:59], v[68:71]\n"
        "  s_add_u32 m0, s59, 53248\n"
        "  ds_read_b128 a[48:51], v161 offset:0\n"
        "  s_cmp_eq_u32 s54, 98304\n"
        "  s_cselect_b32 s55, 0, s55\n"
        "  v_mfma_f32_16x16x32_bf16 v[92:95], a[84:87], a[60:63], v[92:95]\n"
        "  ds_read_b128 a[52:55], v161 offset:2048\n"
        "  s_sub_u32 s52, s55, s54\n"
        "  v_mfma_f32_16x16x32_bf16 v[140:143], a[84:87], a[128:131], v[140:143]\n"
        "  buffer_load_dwordx4 v172, s[16:19], s60 offen offset:0 lds\n"
        "  s_waitcnt lgkmcnt(12)\n"
        "  v_mfma_f32_16x16x32_bf16 v[96:99], a[0:3], a[0:3], v[96:99]\n"
        "  ds_read_b128 a[56:59], v161 offset:4096\n"
        "  v_mfma_f32_16x16x32_bf16 v[100:103], a[4:7], a[4:7], v[100:103]\n"
        "  ds_read_b128 a[60:63], v161 offset:6144\n"
        "  v_mfma_f32_16x16x32_bf16 v[104:107], a[8:11], a[8:11], v[104:107]\n"
        "  ds_read_b64_tr_b16 a[84:85], v167 offset:14336\n"
        "  v_mfma_f32_16x16x32_bf16 v[108:111], a[12:15], a[12:15], v[108:111]\n"
        "  buffer_load_dwordx4 v173, s[16:19], s60 offen offset:1024 lds\n"
        "  v_mfma_f32_16x16x32_bf16 v[112:115], a[128:131], a[0:3], v[112:115]\n"
        "  ds_read_b64_tr_b16 a[86:87], v167 offset:14848\n"
        "  v_add_u32 v160, s52, v160\n"
        "  v_add_u32 v161, s52, v161\n"
        "  v_mfma_f32_16x16x32_bf16 v[116:119], a[128:131], a[4:7], v[116:119]\n"
        "  v_add_u32 v162, s52, v162\n"
        "  v_add_u32 v163, s52, v163\n"
        "  v_add_u32 v164, s52, v164\n"
        "  v_mfma_f32_16x16x32_bf16 v[120:123], a[128:131], a[8:11], v[120:123]\n"
        "  v_add_u32 v165, s52, v165\n"
        "  v_add_u32 v166, s52, v166\n"
        "  v_add_u32 v167, s52, v167\n"
        "  v_mfma_f32_16x16x32_bf16 v[124:127], a[128:131], a[12:15], v[124:127]\n"
        "  buffer_load_dwordx4 v174, s[16:19], s60 offen offset:2048 lds\n"
        "  v_mfma_f32_16x16x32_bf16 v[0:3], a[16:19], a[0:3], v[0:3]\n"
        "  v_mfma_f32_16x16x32_bf16 v[24:27], a[16:19], a[4:7], v[24:27]\n"
        "  v_mfma_f32_16x16x32_bf16 v[48:51], a[16:19], a[8:11], v[48:51]\n"
        "  v_mfma_f32_16x16x32_bf16 v[72:75], a[16:19], a[12:15], v[72:75]\n"
        "  v_mfma_f32_16x16x32_bf16 v[128:131], a[16:19], a[128:131], v[128:131]\n"
        "  buffer_load_dwordx4 v175, s[16:19], s60 offen offset:3072 lds\n"
        "  v_mfma_f32_16x16x32_bf16 v[4:7], a[20:23], a[0:3], v[4:7]\n"
        "  s_add_u32 s60, s60, 28672\n"
        "  v_mfma_f32_16x16x32_bf16 v[28:31], a[20:23], a[4:7], v[28:31]\n"
        "  s_add_u32 m0, s59, 57344\n"
        "  v_mfma_f32_16x16x32_bf16 v[52:55], a[20:23], a[8:11], v[52:55]\n"
        "  v_mfma_f32_16x16x32_bf16 v[76:79], a[20:23], a[12:15], v[76:79]\n"
        "  buffer_load_dwordx4 v172, s[16:19], s60 offen offset:0 lds\n"
        "  v_mfma_f32_16x16x32_bf16 v[132:135], a[20:23], a[128:131], v[132:135]\n"
        "  v_mfma_f32_16x16x32_bf16 v[8:11], a[24:27], a[0:3], v[8:11]\n"
        "  v_mfma_f32_16x16x32_bf16 v[32:35], a[24:27], a[4:7], v[32:35]\n"
        "  v_mfma_f32_16x16x32_bf16 v[56:59], a[24:27], a[8:11], v[56:59]\n"
        "  v_mfma_f32_16x16x32_bf16 v[80:83], a[24:27], a[12:15], v[80:83]\n"
        "  buffer_load_dwordx4 v173, s[16:19], s60 offen offset:1024 lds\n"
        "  v_mfma_f32_16x16x32_bf16 v[12:15], a[28:31], a[0:3], v[12:15]\n"
        "  v_mfma_f32_16x16x32_bf16 v[36:39], a[28:31], a[4:7], v[36:39]\n"
        "  v_mfma_f32_16x16x32_bf16 v[60:63], a[28:31], a[8:11], v[60:63]\n"
        "  v_mfma_f32_16x16x32_bf16 v[84:87], a[28:31], a[12:15], v[84:87]\n"
        "  buffer_load_dwordx4 v174, s[16:19], s60 offen offset:2048 lds\n"
        "  v_mfma_f32_16x16x32_bf16 v[16:19], a[32:35], a[0:3], v[16:19]\n"
        "  v_mfma_f32_16x16x32_bf16 v[40:43], a[32:35], a[4:7], v[40:43]\n"
        "  v_mfma_f32_16x16x32_bf16 v[64:67], a[32:35], a[8:11], v[64:67]\n"
        "  v_mfma_f32_16x16x32_bf16 v[88:91], a[32:35], a[12:15], v[88:91]\n"
        "  v_mfma_f32_16x16x32_bf16 v[136:139], a[32:35], a[128:131], v[136:139]\n"
        "  buffer_load_dwordx4 v175, s[16:19], s60 offen offset:3072 lds\n"
        "  s_add_u32 s60, s60, 28672\n"
        "  v_mfma_f32_16x16x32_bf16 v[20:23], a[36:39], a[0:3], v[20:23]\n"
        "  v_mfma_f32_16x16x32_bf16 v[44:47], a[36:39], a[4:7], v[44:47]\n"
        "  v_mfma_f32_16x16x32_bf16 v[68:71], a[36:39], a[8:11], v[68:71]\n"
        "  s_mov_b32 s51, s54\n"
        "  s_mov_b32 s54, s55\n"
        "  v_mfma_f32_16x16x32_bf16 v[92:95], a[36:39], a[12:15], v[92:95]\n"
        "  v_mfma_f32_16x16x32_bf16 v[140:143], a[36:39], a[128:131], v[140:143]\n"
        "  s_waitcnt vmcnt(12) lgkmcnt(0)\n"
        "  s_barrier\n"
        "  v_mfma_f32_16x16x32_bf16 v[96:99], a[48:51], a[48:51], v[96:99]\n"
        "  s_add_u32 m0, s59, 98304\n"
        "  ds_read_b128 a[0:3], v160 offset:0\n"
        "  v_mfma_f32_16x16x32_bf16 v[100:103], a[52:55], a[52:55], v[100:103]\n"
        "  buffer_load_dwordx4 v172, s[16:19], s60 offen offset:0 lds\n"
        "  v_mfma_f32_16x16x32_bf16 v[104:107], a[56:59], a[56:59], v[104:107]\n"
        "  ds_read_b128 a[4:7], v160 offset:2048\n"
        "  v_mfma_f32_16x16x32_bf16 v[108:111], a[60:63], a[60:63], v[108:111]\n"
        "  ds_read_b128 a[8:11], v160 offset:4096\n"
        "  v_mfma_f32_16x16x32_bf16 v[112:115], a[128:131], a[48:51], v[112:115]\n"
        "  ds_read_b128 a[12:15], v160 offset:6144\n"
        "  v_mfma_f32_16x16x32_bf16 v[116:119], a[128:131], a[52:55], v[116:119]\n"
        "  buffer_load_dwordx4 v173, s[16:19], s60 offen offset:1024 lds\n"
        "  v_mfma_f32_16x16x32_bf16 v[120:123], a[128:131], a[56:59], v[120:123]\n"
        "  ds_read_b64_tr_b16 a[16:17], v162 offset:0\n"
        "  v_mfma_f32_16x16x32_bf16 v[124:127], a[128:131], a[60:63], v[124:127]\n"
        "  ds_read_b64_tr_b16 a[18:19], v162 offset:3072\n"
        "  v_mfma_f32_16x16x32_bf16 v[0:3], a[64:67], a[48:51], v[0:3]\n"
        "  ds_read_b64_tr_b16 a[20:21], v163 offset:0\n"
        "  v_mfma_f32_16x16x32_bf16 v[24:27], a[64:67], a[52:55], v[24:27]\n"
        "  ds_read_b64_tr_b16 a[22:23], v163 offset:3072\n"
        "  v_mfma_f32_16x16x32_bf16 v[48:51], a[64:67], a[56:59], v[48:51]\n"
        "  buffer_load_dwordx4 v174, s[16:19], s60 offen offset:2048 lds\n"
        "  v_mfma_f32_16x16x32_bf16 v[72:75], a[64:67], a[60:63], v[72:75]\n"
        "  ds_read_b64_tr_b16 a[24:25], v164 offset:0\n"
        "  v_mfma_f32_16x16x32_bf16 v[128:131], a[64:67], a[128:131], v[128:131]\n"
        "  ds_read_b64_tr_b16 a[26:27], v164 offset:3072\n"
        "  v_mfma_f32_16x16x32_bf16 v[4:7], a[68:71], a[48:51], v[4:7]\n"
        "  ds_read_b64_tr_b16 a[28:29], v165 offset:0\n"
        "  v_mfma_f32_16x16x32_bf16 v[28:31], a[68:71], a[52:55], v[28:31]\n"
        "  ds_read_b64_tr_b16 a[30:31], v165 offset:3072\n"
        "  v_mfma_f32_16x16x32_bf16 v[52:55], a[68:71], a[56:59], v[52:55]\n"
        "  buffer_load_dwordx4 v175, s[16:19], s60 offen offset:3072 lds\n"
        "  v_mfma_f32_16x16x32_bf16 v[76:79], a[68:71], a[60:63], v[76:79]\n"
        "  s_add_u32 s60, s60, 28672\n"
        "  ds_read_b64_tr_b16 a[32:33], v166 offset:0\n"
        "  v_mfma_f32_16x16x32_bf16 v[132:135], a[68:71], a[128:131], v[132:135]\n"
        "  ds_read_b64_tr_b16 a[34:35], v166 offset:512\n"
        "  v_mfma_f32_16x16x32_bf16 v[8:11], a[72:75], a[48:51], v[8:11]\n"
        "  s_add_u32 m0, s59, 102400\n"
        "  ds_read_b64_tr_b16 a[36:37], v167 offset:0\n"
        "  v_mfma_f32_16x16x32_bf16 v[32:35], a[72:75], a[52:55], v[32:35]\n"
        "  ds_read_b64_tr_b16 a[38:39], v167 offset:512\n"
        "  v_mfma_f32_16x16x32_bf16 v[56:59], a[72:75], a[56:59], v[56:59]\n"
        "  buffer_load_dwordx4 v172, s[16:19], s60 offen offset:0 lds\n"
        "  v_mfma_f32_16x16x32_bf16 v[80:83], a[72:75], a[60:63], v[80:83]\n"
        "  ds_read_b64_tr_b16 a[64:65], v162 offset:14336\n"
        "  v_mfma_f32_16x16x32_bf16 v[12:15], a[76:79], a[48:51], v[12:15]\n"
        "  ds_read_b64_tr_b16 a[66:67], v162 offset:17408\n"
        "  v_mfma_f32_16x16x32_bf16 v[36:39], a[76:79], a[52:55], v[36:39]\n"
        "  ds_read_b64_tr_b16 a[68:69], v163 offset:14336\n"
        "  v_mfma_f32_16x16x32_bf16 v[60:63], a[76:79], a[56:59], v[60:63]\n"
        "  ds_read_b64_tr_b16 a[70:71], v163 offset:17408\n"
        "  v_mfma_f32_16x16x32_bf16 v[84:87], a[76:79], a[60:63], v[84:87]\n"
        "  buffer_load_dwordx4 v173, s[16:19], s60 offen offset:1024 lds\n"
        "  v_mfma_f32_16x16x32_bf16 v[16:19], a[80:83], a[48:51], v[16:19]\n"
        "  ds_read_b64_tr_b16 a[72:73], v164 offset:14336\n"
        "  v_mfma_f32_16x16x32_bf16 v[40:43], a[80:83], a[52:55], v[40:43]\n"
        "  ds_read_b64_tr_b16 a[74:75], v164 offset:17408\n"
        "  v_mfma_f32_16x16x32_bf16 v[64:67], a[80:83], a[56:59], v[64:67]\n"
        "  ds_read_b64_tr_b16 a[76:77], v165 offset:14336\n"
        "  v_mfma_f32_16x16x32_bf16 v[88:91], a[80:83], a[60:63], v[88:91]\n"
        "  buffer_load_dwordx4 v174, s[16:19], s60 offen offset:2048 lds\n"
        "  v_mfma_f32_16x16x32_bf16 v[136:139], a[80:83], a[128:131], v[136:139]\n"
        "  ds_read_b64_tr_b16 a[78:79], v165 offset:17408\n"
        "  v_mfma_f32_16x16x32_bf16 v[20:23], a[84:87], a[48:51], v[20:23]\n"
        "  ds_read_b64_tr_b16 a[80:81], v166 offset:14336\n"
        "  s_add_u32 s55, s54, 49152\n"
        "  v_mfma_f32_16x16x32_bf16 v[44:47], a[84:87], a[52:55], v[44:47]\n"
        "  ds_read_b64_tr_b16 a[82:83], v166 offset:14848\n"
        "  v_mfma_f32_16x16x32_bf16 v[68:71], a[84:87], a[56:59], v[68:71]\n"
        "  ds_read_b128 a[48:51], v161 offset:0\n"
        "  s_cmp_eq_u32 s54, 98304\n"
        "  s_cselect_b32 s55, 0, s55\n"
        "  v_mfma_f32_16x16x32_bf16 v[92:95], a[84:87], a[60:63], v[92:95]\n"
        "  buffer_load_dwordx4 v175, s[16:19], s60 offen offset:3072 lds\n"
        "  s_sub_u32 s52, s55, s54\n"
        "  v_mfma_f32_16x16x32_bf16 v[140:143], a[84:87], a[128:131], v[140:143]\n"
        "  s_add_u32 s60, s60, 28672\n"
        "  ds_read_b128 a[52:55], v161 offset:2048\n"
        "  s_waitcnt lgkmcnt(12)\n"
        "  v_mfma_f32_16x16x32_bf16 v[96:99], a[0:3], a[0:3], v[96:99]\n"
        "  ds_read_b128 a[56:59], v161 offset:4096\n"
        "  v_mfma_f32_16x16x32_bf16 v[100:103], a[4:7], a[4:7], v[100:103]\n"
        "  s_add_u32 m0, s59, 106496\n"
        "  ds_read_b128 a[60:63], v161 offset:6144\n"
        "  v_mfma_f32_16x16x32_bf16 v[104:107], a[8:11], a[8:11], v[104:107]\n"
        "  ds_read_b64_tr_b16 a[84:85], v167 offset:14336\n"
        "  v_mfma_f32_16x16x32_bf16 v[108:111], a[12:15], a[12:15], v[108:111]\n"
        "  buffer_load_dwordx4 v172, s[16:19], s60 offen offset:0 lds\n"
        "  v_mfma_f32_16x16x32_bf16 v[112:115], a[128:131], a[0:3], v[112:115]\n"
        "  ds_read_b64_tr_b16 a[86:87], v167 offset:14848\n"
        "  v_mfma_f32_16x16x32_bf16 v[116:119], a[128:131], a[4:7], v[116:119]\n"
        "  v_mfma_f32_16x16x32_bf16 v[120:123], a[128:131], a[8:11], v[120:123]\n"
        "  v_mfma_f32_16x16x32_bf16 v[124:127], a[128:131], a[12:15], v[124:127]\n"
        "  v_mfma_f32_16x16x32_bf16 v[128:131], a[16:19], a[128:131], v[128:131]\n"
        "  buffer_load_dwordx4 v173, s[16:19], s60 offen offset:1024 lds\n"
        "  v_mfma_f32_16x16x32_bf16 v[132:135], a[20:23], a[128:131], v[132:135]\n"
        "  v_mfma_f32_16x16x32_bf16 v[136:139], a[32:35], a[128:131], v[136:139]\n"
        "  v_mfma_f32_16x16x32_bf16 v[140:143], a[36:39], a[128:131], v[140:143]\n"
        "  v_mfma_f32_16x16x32_bf16 v[0:3], a[16:19], a[0:3], v[0:3]\n"
        "  v_mfma_f32_16x16x32_bf16 v[24:27], a[16:19], a[4:7], v[24:27]\n"
        "  buffer_load_dwordx4 v174, s[16:19], s60 offen offset:2048 lds\n"
        "  v_mfma_f32_16x16x32_bf16 v[48:51], a[16:19], a[8:11], v[48:51]\n"
        "  v_mfma_f32_16x16x32_bf16 v[72:75], a[16:19], a[12:15], v[72:75]\n"
        "  v_mfma_f32_16x16x32_bf16 v[4:7], a[20:23], a[0:3], v[4:7]\n"
        "  v_mfma_f32_16x16x32_bf16 v[28:31], a[20:23], a[4:7], v[28:31]\n"
        "  v_mfma_f32_16x16x32_bf16 v[52:55], a[20:23], a[8:11], v[52:55]\n"
        "  buffer_load_dwordx4 v175, s[16:19], s60 offen offset:3072 lds\n"
        "  s_add_u32 s60, s60, 28672\n"
        "  v_mfma_f32_16x16x32_bf16 v[76:79], a[20:23], a[12:15], v[76:79]\n"
        "  v_mfma_f32_16x16x32_bf16 v[8:11], a[24:27], a[0:3], v[8:11]\n"
        "  v_mfma_f32_16x16x32_bf16 v[32:35], a[24:27], a[4:7], v[32:35]\n"
        "  s_mov_b32 s51, s54\n"
        "  s_mov_b32 s54, s55\n"
        "  v_mfma_f32_16x16x32_bf16 v[56:59], a[24:27], a[8:11], v[56:59]\n"
        "  v_mfma_f32_16x16x32_bf16 v[80:83], a[24:27], a[12:15], v[80:83]\n"
        "  s_waitcnt lgkmcnt(0)\n"
        "  s_barrier\n"
        "  v_mfma_f32_16x16x32_bf16 v[96:99], a[48:51], a[48:51], v[96:99]\n"
        "  v_mfma_f32_16x16x32_bf16 v[100:103], a[52:55], a[52:55], v[100:103]\n"
        "  v_mfma_f32_16x16x32_bf16 v[104:107], a[56:59], a[56:59], v[104:107]\n"
        "  v_mfma_f32_16x16x32_bf16 v[108:111], a[60:63], a[60:63], v[108:111]\n"
        "  v_mfma_f32_16x16x32_bf16 v[112:115], a[128:131], a[48:51], v[112:115]\n"
        "  v_mfma_f32_16x16x32_bf16 v[116:119], a[128:131], a[52:55], v[116:119]\n"
        "  v_mfma_f32_16x16x32_bf16 v[120:123], a[128:131], a[56:59], v[120:123]\n"
        "  s_nop 1  ; hazard\n"
        "  v_cndmask_b32_e64 v208, v96, v97, s[72:73]\n"
        "  v_cndmask_b32_e64 v209, v100, v101, s[72:73]\n"
        "  v_mfma_f32_16x16x32_bf16 v[124:127], a[128:131], a[60:63], v[124:127]\n"
        "  v_cndmask_b32_e64 v210, v104, v105, s[72:73]\n"
        "  v_cndmask_b32_e64 v211, v108, v109, s[72:73]\n"
        "  v_mfma_f32_16x16x32_bf16 v[128:131], a[64:67], a[128:131], v[128:131]\n"
        "  v_cndmask_b32_e64 v212, v98, v99, s[72:73]\n"
        "  v_cndmask_b32_e64 v213, v102, v103, s[72:73]\n"
        "  v_mfma_f32_16x16x32_bf16 v[132:135], a[68:71], a[128:131], v[132:135]\n"
        "  v_cndmask_b32_e64 v214, v106, v107, s[72:73]\n"
        "  v_cndmask_b32_e64 v215, v110, v111, s[72:73]\n"
        "  v_mfma_f32_16x16x32_bf16 v[136:139], a[80:83], a[128:131], v[136:139]\n"
        "  v_cndmask_b32_e64 v208, v208, v212, s[74:75]\n"
        "  v_cndmask_b32_e64 v209, v209, v213, s[74:75]\n"
        "  v_mfma_f32_16x16x32_bf16 v[140:143], a[84:87], a[128:131], v[140:143]\n"
        "  v_cndmask_b32_e64 v210, v210, v214, s[74:75]\n"
        "  v_cndmask_b32_e64 v211, v211, v215, s[74:75]\n"
        "  v_mfma_f32_16x16x32_bf16 v[12:15], a[28:31], a[0:3], v[12:15]\n"
        "  v_mul_f32 v212, 0xbb124925, v112\n"
        "  v_mul_f32 v213, 0xbb124925, v116\n"
        "  v_mfma_f32_16x16x32_bf16 v[36:39], a[28:31], a[4:7], v[36:39]\n"
        "  v_mul_f32 v214, 0xbb124925, v120\n"
        "  v_mul_f32 v215, 0xbb124925, v124\n"
        "  v_mfma_f32_16x16x32_bf16 v[60:63], a[28:31], a[8:11], v[60:63]\n"
        "  v_mul_f32 v216, 0x3b124925, v208\n"
        "  v_mul_f32 v217, 0x3b124925, v209\n"
        "  v_mfma_f32_16x16x32_bf16 v[84:87], a[28:31], a[12:15], v[84:87]\n"
        "  v_mul_f32 v218, 0x3b124925, v210\n"
        "  v_mul_f32 v219, 0x3b124925, v211\n"
        "  v_mfma_f32_16x16x32_bf16 v[16:19], a[32:35], a[0:3], v[16:19]\n"
        "  v_fma_f32 v216, -v212, v212, v216\n"
        "  v_fma_f32 v217, -v213, v213, v217\n"
        "  v_mfma_f32_16x16x32_bf16 v[40:43], a[32:35], a[4:7], v[40:43]\n"
        "  s_mov_b64 exec, s[78:79]\n"
        "  v_mov_b32 v188, 0\n"
        "  v_mov_b32 v189, 0\n"
        "  buffer_atomic_or_x2 v[188:189], v184, s[24:27], 0 offen sc0 sc1\n"
        "  s_mov_b64 exec, -1\n"
        "  v_mfma_f32_16x16x32_bf16 v[64:67], a[32:35], a[8:11], v[64:67]\n"
        "  v_fma_f32 v218, -v214, v214, v218\n"
        "  v_fma_f32 v219, -v215, v215, v219\n"
        "  v_mfma_f32_16x16x32_bf16 v[88:91], a[32:35], a[12:15], v[88:91]\n"
        "  s_mul_i32 s95, s88, 896\n"
        "  s_add_u32 s95, s95, s63\n"
        "  s_lshl_b32 s94, s40, 11\n"
        "  s_add_u32 m0, s94, 155648\n"
        "  s_lshl_b32 s94, s40, 7\n"
        "  buffer_load_dwordx4 v252, s[4:7], s95 offen offset:0 lds\n"
        "  v_mfma_f32_16x16x32_bf16 v[20:23], a[36:39], a[0:3], v[20:23]\n"
        "  v_max_f32 v216, 0, v216\n"
        "  v_max_f32 v217, 0, v217\n"
        "  v_mfma_f32_16x16x32_bf16 v[44:47], a[36:39], a[4:7], v[44:47]\n"
        "  v_max_f32 v218, 0, v218\n"
        "  v_max_f32 v219, 0, v219\n"
        "  v_mfma_f32_16x16x32_bf16 v[68:71], a[36:39], a[8:11], v[68:71]\n"
        "  buffer_load_dwordx4 v253, s[4:7], s95 offen offset:1024 lds\n"
        "  v_add_f32 v216, 0x3727c5ac, v216\n"
        "  v_mfma_f32_16x16x32_bf16 v[92:95], a[36:39], a[12:15], v[92:95]\n"
        "  v_add_f32 v217, 0x3727c5ac, v217\n"
        "  v_add_f32 v218, 0x3727c5ac, v218\n"
        "  v_mfma_f32_16x16x32_bf16 v[0:3], a[64:67], a[48:51], v[0:3]\n"
        "  v_add_f32 v219, 0x3727c5ac, v219\n"
        "  v_rsq_f32 v216, v216\n"
        "  v_mfma_f32_16x16x32_bf16 v[24:27], a[64:67], a[52:55], v[24:27]\n"
        "  s_add_u32 m0, s94, 149504\n"
        "  s_mov_b64 exec, 0xff\n"
        "  buffer_load_dwordx4 v182, s[36:39], s63 offen offset:0 lds\n"
        "  s_mov_b64 exec, -1\n"
        "  v_mfma_f32_16x16x32_bf16 v[48:51], a[64:67], a[56:59], v[48:51]\n"
        "  v_rsq_f32 v217, v217\n"
        "  v_rsq_f32 v218, v218\n"
        "  v_mfma_f32_16x16x32_bf16 v[72:75], a[64:67], a[60:63], v[72:75]\n"
        "  v_rsq_f32 v219, v219\n"
        "  v_mul_f32 v220, v212, v216\n"
        "  v_mfma_f32_16x16x32_bf16 v[4:7], a[68:71], a[48:51], v[4:7]\n"
        "  s_add_u32 m0, s59, 0\n"
        "  s_nop 0  ; hazard\n"
        "  buffer_load_dwordx4 v172, s[16:19], s60 offen offset:0 lds\n"
        "  v_mfma_f32_16x16x32_bf16 v[28:31], a[68:71], a[52:55], v[28:31]\n"
        "  v_mul_f32 v221, v213, v217\n"
        "  v_mul_f32 v222, v214, v218\n"
        "  v_mfma_f32_16x16x32_bf16 v[52:55], a[68:71], a[56:59], v[52:55]\n"
        "  v_mul_f32 v223, v215, v219\n"
        "  s_mov_b64 exec, s[70:71]\n"
        "  ds_write2_b32 v207, v216, v220 offset0:0 offset1:128\n"
        "  ds_write2_b32 v207, v217, v221 offset0:16 offset1:144\n"
        "  ds_write2_b32 v207, v218, v222 offset0:32 offset1:160\n"
        "  ds_write2_b32 v207, v219, v223 offset0:48 offset1:176\n"
        "  s_mov_b64 exec, -1\n"
        "  v_mfma_f32_16x16x32_bf16 v[76:79], a[68:71], a[60:63], v[76:79]\n"
        "  buffer_load_dwordx4 v173, s[16:19], s60 offen offset:1024 lds\n"
        "  v_mfma_f32_16x16x32_bf16 v[8:11], a[72:75], a[48:51], v[8:11]\n"
        "  s_mov_b32 exec_lo, 0x10001\n"
        "  s_mov_b32 exec_hi, 0x10001\n"
        "  ds_write_b128 v197, v[128:131] offset:512\n"
        "  ds_write_b128 v197, v[132:135] offset:576\n"
        "  ds_write_b128 v199, v[136:139] offset:768\n"
        "  ds_write_b128 v199, v[140:143] offset:832\n"
        "  s_mov_b64 exec, -1\n"
        "  v_mfma_f32_16x16x32_bf16 v[32:35], a[72:75], a[52:55], v[32:35]\n"
        "  v_mfma_f32_16x16x32_bf16 v[56:59], a[72:75], a[56:59], v[56:59]\n"
        "  buffer_load_dwordx4 v174, s[16:19], s60 offen offset:2048 lds\n"
        "  v_mfma_f32_16x16x32_bf16 v[80:83], a[72:75], a[60:63], v[80:83]\n"
        "  v_mfma_f32_16x16x32_bf16 v[12:15], a[76:79], a[48:51], v[12:15]\n"
        "  v_mfma_f32_16x16x32_bf16 v[36:39], a[76:79], a[52:55], v[36:39]\n"
        "  buffer_load_dwordx4 v175, s[16:19], s60 offen offset:3072 lds\n"
        "  v_mfma_f32_16x16x32_bf16 v[60:63], a[76:79], a[56:59], v[60:63]\n"
        "  v_mfma_f32_16x16x32_bf16 v[84:87], a[76:79], a[60:63], v[84:87]\n"
        "  v_mfma_f32_16x16x32_bf16 v[16:19], a[80:83], a[48:51], v[16:19]\n"
        "  s_add_u32 s60, s60, 28672\n"
        "  s_add_u32 m0, s59, 4096\n"
        "  s_nop 0  ; hazard\n"
        "  buffer_load_dwordx4 v172, s[16:19], s60 offen offset:0 lds\n"
        "  v_mfma_f32_16x16x32_bf16 v[40:43], a[80:83], a[52:55], v[40:43]\n"
        "  v_mfma_f32_16x16x32_bf16 v[64:67], a[80:83], a[56:59], v[64:67]\n"
        "  v_mfma_f32_16x16x32_bf16 v[88:91], a[80:83], a[60:63], v[88:91]\n"
        "  buffer_load_dwordx4 v173, s[16:19], s60 offen offset:1024 lds\n"
        "  v_mfma_f32_16x16x32_bf16 v[20:23], a[84:87], a[48:51], v[20:23]\n"
        "  v_mfma_f32_16x16x32_bf16 v[44:47], a[84:87], a[52:55], v[44:47]\n"
        "  v_mfma_f32_16x16x32_bf16 v[68:71], a[84:87], a[56:59], v[68:71]\n"
        "  v_mfma_f32_16x16x32_bf16 v[92:95], a[84:87], a[60:63], v[92:95]\n"
        "  s_waitcnt lgkmcnt(0)\n"
        "  s_barrier\n"
        "  ds_read2_b32 v[232:233], v207 offset0:0 offset1:128\n"
        "  ds_read2_b32 v[234:235], v207 offset0:16 offset1:144\n"
        "  ds_read_b128 v[144:147], v197 offset:512\n"
        "  ds_read_b128 v[152:155], v194 offset:512\n"
        "  ds_read_b128 v[148:151], v197 offset:576\n"
        "  ds_read_b128 v[156:159], v194 offset:576\n"
        "  ds_read2_b32 v[236:237], v207 offset0:32 offset1:160\n"
        "  ds_read2_b32 v[238:239], v207 offset0:48 offset1:176\n"
        "  ds_read_b128 v[216:219], v198 offset:640\n"
        "  ds_read_b128 v[224:227], v195 offset:640\n"
        "  ds_read_b128 v[220:223], v198 offset:704\n"
        "  ds_read_b128 v[228:231], v195 offset:704\n"
        "  s_add_u32 s94, s82, 16384\n"
        "  s_waitcnt lgkmcnt(8)  ; auto\n"
        "  v_pk_fma_f32 v[208:209], v[232:233], v[144:145], v[152:153] op_sel:[1,0,0] op_sel_hi:[1,1,1]\n"
        "  v_pk_fma_f32 v[212:213], v[234:235], v[144:145], v[152:153] op_sel:[1,0,0] op_sel_hi:[1,1,1]\n"
        "  v_pk_fma_f32 v[210:211], v[232:233], v[146:147], v[154:155] op_sel:[1,0,0] op_sel_hi:[1,1,1]\n"
        "  v_pk_fma_f32 v[214:215], v[234:235], v[146:147], v[154:155] op_sel:[1,0,0] op_sel_hi:[1,1,1]\n"
        "  v_pk_fma_f32 v[0:1], v[232:233], v[0:1], v[208:209] op_sel:[0,0,0] op_sel_hi:[0,1,1]\n"
        "  v_pk_fma_f32 v[24:25], v[234:235], v[24:25], v[212:213] op_sel:[0,0,0] op_sel_hi:[0,1,1]\n"
        "  v_pk_fma_f32 v[2:3], v[232:233], v[2:3], v[210:211] op_sel:[0,0,0] op_sel_hi:[0,1,1]\n"
        "  v_pk_fma_f32 v[26:27], v[234:235], v[26:27], v[214:215] op_sel:[0,0,0] op_sel_hi:[0,1,1]\n"
        "  v_max_f32 v0, 0, v0\n"
        "  v_max_f32 v24, 0, v24\n"
        "  v_max_f32 v1, 0, v1\n"
        "  v_max_f32 v25, 0, v25\n"
        "  v_max_f32 v2, 0, v2\n"
        "  v_max_f32 v26, 0, v26\n"
        "  v_max_f32 v3, 0, v3\n"
        "  v_max_f32 v27, 0, v27\n"
        "  v_pk_mul_f32 v[0:1], v[0:1], v[0:1] op_sel:[0,0] op_sel_hi:[1,1]\n"
        "  v_pk_mul_f32 v[24:25], v[24:25], v[24:25] op_sel:[0,0] op_sel_hi:[1,1]\n"
        "  v_pk_mul_f32 v[2:3], v[2:3], v[2:3] op_sel:[0,0] op_sel_hi:[1,1]\n"
        "  v_pk_mul_f32 v[26:27], v[26:27], v[26:27] op_sel:[0,0] op_sel_hi:[1,1]\n"
        "  s_waitcnt lgkmcnt(6)  ; auto\n"
        "  v_pk_fma_f32 v[208:209], v[232:233], v[148:149], v[156:157] op_sel:[1,0,0] op_sel_hi:[1,1,1]\n"
        "  v_pk_fma_f32 v[212:213], v[234:235], v[148:149], v[156:157] op_sel:[1,0,0] op_sel_hi:[1,1,1]\n"
        "  v_pk_fma_f32 v[210:211], v[232:233], v[150:151], v[158:159] op_sel:[1,0,0] op_sel_hi:[1,1,1]\n"
        "  v_pk_fma_f32 v[214:215], v[234:235], v[150:151], v[158:159] op_sel:[1,0,0] op_sel_hi:[1,1,1]\n"
        "  v_pk_fma_f32 v[4:5], v[232:233], v[4:5], v[208:209] op_sel:[0,0,0] op_sel_hi:[0,1,1]\n"
        "  v_pk_fma_f32 v[28:29], v[234:235], v[28:29], v[212:213] op_sel:[0,0,0] op_sel_hi:[0,1,1]\n"
        "  v_pk_fma_f32 v[6:7], v[232:233], v[6:7], v[210:211] op_sel:[0,0,0] op_sel_hi:[0,1,1]\n"
        "  v_pk_fma_f32 v[30:31], v[234:235], v[30:31], v[214:215] op_sel:[0,0,0] op_sel_hi:[0,1,1]\n"
        "  v_max_f32 v4, 0, v4\n"
        "  v_max_f32 v28, 0, v28\n"
        "  v_max_f32 v5, 0, v5\n"
        "  v_max_f32 v29, 0, v29\n"
        "  v_max_f32 v6, 0, v6\n"
        "  v_max_f32 v30, 0, v30\n"
        "  v_max_f32 v7, 0, v7\n"
        "  v_max_f32 v31, 0, v31\n"
        "  v_pk_mul_f32 v[4:5], v[4:5], v[4:5] op_sel:[0,0] op_sel_hi:[1,1]\n"
        "  v_pk_mul_f32 v[28:29], v[28:29], v[28:29] op_sel:[0,0] op_sel_hi:[1,1]\n"
        "  v_pk_mul_f32 v[6:7], v[6:7], v[6:7] op_sel:[0,0] op_sel_hi:[1,1]\n"
        "  v_pk_mul_f32 v[30:31], v[30:31], v[30:31] op_sel:[0,0] op_sel_hi:[1,1]\n"
        "  v_cvt_pk_bf16_f32 v0, v0, v1\n"
        "  v_cvt_pk_bf16_f32 v24, v24, v25\n"
        "  v_cvt_pk_bf16_f32 v1, v2, v3\n"
        "  v_cvt_pk_bf16_f32 v25, v26, v27\n"
        "  v_cvt_pk_bf16_f32 v2, v4, v5\n"
        "  v_cvt_pk_bf16_f32 v26, v28, v29\n"
        "  v_cvt_pk_bf16_f32 v3, v6, v7\n"
        "  v_cvt_pk_bf16_f32 v27, v30, v31\n"
        "  buffer_store_dwordx4 v[0:3], v182, s[24:27], s94 offen offset:0 sc1\n"
        "  buffer_store_dwordx4 v[24:27], v182, s[24:27], s94 offen offset:1024 sc1\n"
        "  s_waitcnt lgkmcnt(5)  ; auto\n"
        "  v_pk_fma_f32 v[208:209], v[236:237], v[144:145], v[152:153] op_sel:[1,0,0] op_sel_hi:[1,1,1]\n"
        "  s_waitcnt lgkmcnt(4)  ; auto\n"
        "  v_pk_fma_f32 v[212:213], v[238:239], v[144:145], v[152:153] op_sel:[1,0,0] op_sel_hi:[1,1,1]\n"
        "  v_pk_fma_f32 v[210:211], v[236:237], v[146:147], v[154:155] op_sel:[1,0,0] op_sel_hi:[1,1,1]\n"
        "  v_pk_fma_f32 v[214:215], v[238:239], v[146:147], v[154:155] op_sel:[1,0,0] op_sel_hi:[1,1,1]\n"
        "  v_pk_fma_f32 v[48:49], v[236:237], v[48:49], v[208:209] op_sel:[0,0,0] op_sel_hi:[0,1,1]\n"
        "  v_pk_fma_f32 v[72:73], v[238:239], v[72:73], v[212:213] op_sel:[0,0,0] op_sel_hi:[0,1,1]\n"
        "  v_pk_fma_f32 v[50:51], v[236:237], v[50:51], v[210:211] op_sel:[0,0,0] op_sel_hi:[0,1,1]\n"
        "  v_pk_fma_f32 v[74:75], v[238:239], v[74:75], v[214:215] op_sel:[0,0,0] op_sel_hi:[0,1,1]\n"
        "  v_max_f32 v48, 0, v48\n"
        "  v_max_f32 v72, 0, v72\n"
        "  v_max_f32 v49, 0, v49\n"
        "  v_max_f32 v73, 0, v73\n"
        "  v_max_f32 v50, 0, v50\n"
        "  v_max_f32 v74, 0, v74\n"
        "  v_max_f32 v51, 0, v51\n"
        "  v_max_f32 v75, 0, v75\n"
        "  v_pk_mul_f32 v[48:49], v[48:49], v[48:49] op_sel:[0,0] op_sel_hi:[1,1]\n"
        "  v_pk_mul_f32 v[72:73], v[72:73], v[72:73] op_sel:[0,0] op_sel_hi:[1,1]\n"
        "  v_pk_mul_f32 v[50:51], v[50:51], v[50:51] op_sel:[0,0] op_sel_hi:[1,1]\n"
        "  v_pk_mul_f32 v[74:75], v[74:75], v[74:75] op_sel:[0,0] op_sel_hi:[1,1]\n"
        "  v_pk_fma_f32 v[208:209], v[236:237], v[148:149], v[156:157] op_sel:[1,0,0] op_sel_hi:[1,1,1]\n"
        "  v_pk_fma_f32 v[212:213], v[238:239], v[148:149], v[156:157] op_sel:[1,0,0] op_sel_hi:[1,1,1]\n"
        "  v_pk_fma_f32 v[210:211], v[236:237], v[150:151], v[158:159] op_sel:[1,0,0] op_sel_hi:[1,1,1]\n"
        "  v_pk_fma_f32 v[214:215], v[238:239], v[150:151], v[158:159] op_sel:[1,0,0] op_sel_hi:[1,1,1]\n"
        "  v_pk_fma_f32 v[52:53], v[236:237], v[52:53], v[208:209] op_sel:[0,0,0] op_sel_hi:[0,1,1]\n"
        "  v_pk_fma_f32 v[76:77], v[238:239], v[76:77], v[212:213] op_sel:[0,0,0] op_sel_hi:[0,1,1]\n"
        "  v_pk_fma_f32 v[54:55], v[236:237], v[54:55], v[210:211] op_sel:[0,0,0] op_sel_hi:[0,1,1]\n"
        "  v_pk_fma_f32 v[78:79], v[238:239], v[78:79], v[214:215] op_sel:[0,0,0] op_sel_hi:[0,1,1]\n"
        "  v_max_f32 v52, 0, v52\n"
        "  v_max_f32 v76, 0, v76\n"
        "  v_max_f32 v53, 0, v53\n"
        "  v_max_f32 v77, 0, v77\n"
        "  v_max_f32 v54, 0, v54\n"
        "  v_max_f32 v78, 0, v78\n"
        "  v_max_f32 v55, 0, v55\n"
        "  v_max_f32 v79, 0, v79\n"
        "  v_pk_mul_f32 v[52:53], v[52:53], v[52:53] op_sel:[0,0] op_sel_hi:[1,1]\n"
        "  v_pk_mul_f32 v[76:77], v[76:77], v[76:77] op_sel:[0,0] op_sel_hi:[1,1]\n"
        "  v_pk_mul_f32 v[54:55], v[54:55], v[54:55] op_sel:[0,0] op_sel_hi:[1,1]\n"
        "  v_pk_mul_f32 v[78:79], v[78:79], v[78:79] op_sel:[0,0] op_sel_hi:[1,1]\n"
        "  v_cvt_pk_bf16_f32 v48, v48, v49\n"
        "  v_cvt_pk_bf16_f32 v72, v72, v73\n"
        "  v_cvt_pk_bf16_f32 v49, v50, v51\n"
        "  v_cvt_pk_bf16_f32 v73, v74, v75\n"
        "  v_cvt_pk_bf16_f32 v50, v52, v53\n"
        "  v_cvt_pk_bf16_f32 v74, v76, v77\n"
        "  v_cvt_pk_bf16_f32 v51, v54, v55\n"
        "  v_cvt_pk_bf16_f32 v75, v78, v79\n"
        "  buffer_store_dwordx4 v[48:51], v182, s[24:27], s94 offen offset:2048 sc1\n"
        "  buffer_store_dwordx4 v[72:75], v182, s[24:27], s94 offen offset:3072 sc1\n"
        "  ds_read_b128 v[144:147], v199 offset:768\n"
        "  ds_read_b128 v[152:155], v196 offset:0\n"
        "  ds_read_b128 v[148:151], v199 offset:832\n"
        "  ds_read_b128 v[156:159], v196 offset:64\n"
        "  s_add_u32 s94, s91, 20480\n"
        "  s_waitcnt lgkmcnt(6)  ; auto\n"
        "  v_pk_fma_f32 v[208:209], v[232:233], v[216:217], v[224:225] op_sel:[1,0,0] op_sel_hi:[1,1,1]\n"
        "  v_pk_fma_f32 v[212:213], v[234:235], v[216:217], v[224:225] op_sel:[1,0,0] op_sel_hi:[1,1,1]\n"
        "  v_pk_fma_f32 v[210:211], v[232:233], v[218:219], v[226:227] op_sel:[1,0,0] op_sel_hi:[1,1,1]\n"
        "  v_pk_fma_f32 v[214:215], v[234:235], v[218:219], v[226:227] op_sel:[1,0,0] op_sel_hi:[1,1,1]\n"
        "  v_pk_fma_f32 v[8:9], v[232:233], v[8:9], v[208:209] op_sel:[0,0,0] op_sel_hi:[0,1,1]\n"
        "  v_pk_fma_f32 v[32:33], v[234:235], v[32:33], v[212:213] op_sel:[0,0,0] op_sel_hi:[0,1,1]\n"
        "  v_pk_fma_f32 v[10:11], v[232:233], v[10:11], v[210:211] op_sel:[0,0,0] op_sel_hi:[0,1,1]\n"
        "  v_pk_fma_f32 v[34:35], v[234:235], v[34:35], v[214:215] op_sel:[0,0,0] op_sel_hi:[0,1,1]\n"
        "  v_max_f32 v8, 0, v8\n"
        "  v_max_f32 v32, 0, v32\n"
        "  v_max_f32 v9, 0, v9\n"
        "  v_max_f32 v33, 0, v33\n"
        "  v_max_f32 v10, 0, v10\n"
        "  v_max_f32 v34, 0, v34\n"
        "  v_max_f32 v11, 0, v11\n"
        "  v_max_f32 v35, 0, v35\n"
        "  v_pk_mul_f32 v[8:9], v[8:9], v[8:9] op_sel:[0,0] op_sel_hi:[1,1]\n"
        "  v_pk_mul_f32 v[32:33], v[32:33], v[32:33] op_sel:[0,0] op_sel_hi:[1,1]\n"
        "  v_pk_mul_f32 v[10:11], v[10:11], v[10:11] op_sel:[0,0] op_sel_hi:[1,1]\n"
        "  v_pk_mul_f32 v[34:35], v[34:35], v[34:35] op_sel:[0,0] op_sel_hi:[1,1]\n"
        "  s_waitcnt lgkmcnt(4)  ; auto\n"
        "  v_pk_fma_f32 v[208:209], v[232:233], v[220:221], v[228:229] op_sel:[1,0,0] op_sel_hi:[1,1,1]\n"
        "  v_pk_fma_f32 v[212:213], v[234:235], v[220:221], v[228:229] op_sel:[1,0,0] op_sel_hi:[1,1,1]\n"
        "  v_pk_fma_f32 v[210:211], v[232:233], v[222:223], v[230:231] op_sel:[1,0,0] op_sel_hi:[1,1,1]\n"
        "  v_pk_fma_f32 v[214:215], v[234:235], v[222:223], v[230:231] op_sel:[1,0,0] op_sel_hi:[1,1,1]\n"
        "  v_pk_fma_f32 v[12:13], v[232:233], v[12:13], v[208:209] op_sel:[0,0,0] op_sel_hi:[0,1,1]\n"
        "  v_pk_fma_f32 v[36:37], v[234:235], v[36:37], v[212:213] op_sel:[0,0,0] op_sel_hi:[0,1,1]\n"
        "  v_pk_fma_f32 v[14:15], v[232:233], v[14:15], v[210:211] op_sel:[0,0,0] op_sel_hi:[0,1,1]\n"
        "  v_pk_fma_f32 v[38:39], v[234:235], v[38:39], v[214:215] op_sel:[0,0,0] op_sel_hi:[0,1,1]\n"
        "  v_max_f32 v12, 0, v12\n"
        "  v_max_f32 v36, 0, v36\n"
        "  v_max_f32 v13, 0, v13\n"
        "  v_max_f32 v37, 0, v37\n"
        "  v_max_f32 v14, 0, v14\n"
        "  v_max_f32 v38, 0, v38\n"
        "  v_max_f32 v15, 0, v15\n"
        "  v_max_f32 v39, 0, v39\n"
        "  v_pk_mul_f32 v[12:13], v[12:13], v[12:13] op_sel:[0,0] op_sel_hi:[1,1]\n"
        "  v_pk_mul_f32 v[36:37], v[36:37], v[36:37] op_sel:[0,0] op_sel_hi:[1,1]\n"
        "  v_pk_mul_f32 v[14:15], v[14:15], v[14:15] op_sel:[0,0] op_sel_hi:[1,1]\n"
        "  v_pk_mul_f32 v[38:39], v[38:39], v[38:39] op_sel:[0,0] op_sel_hi:[1,1]\n"
        "  v_cvt_pk_bf16_f32 v8, v8, v9\n"
        "  v_cvt_pk_bf16_f32 v32, v32, v33\n"
        "  v_cvt_pk_bf16_f32 v9, v10, v11\n"
        "  v_cvt_pk_bf16_f32 v33, v34, v35\n"
        "  v_cvt_pk_bf16_f32 v10, v12, v13\n"
        "  v_cvt_pk_bf16_f32 v34, v36, v37\n"
        "  v_cvt_pk_bf16_f32 v11, v14, v15\n"
        "  v_cvt_pk_bf16_f32 v35, v38, v39\n"
        "  buffer_store_dwordx4 v[8:11], v182, s[24:27], s94 offen offset:0 sc1\n"
        "  buffer_store_dwordx4 v[32:35], v182, s[24:27], s94 offen offset:1024 sc1\n"
        "  v_pk_fma_f32 v[208:209], v[236:237], v[216:217], v[224:225] op_sel:[1,0,0] op_sel_hi:[1,1,1]\n"
        "  v_pk_fma_f32 v[212:213], v[238:239], v[216:217], v[224:225] op_sel:[1,0,0] op_sel_hi:[1,1,1]\n"
        "  v_pk_fma_f32 v[210:211], v[236:237], v[218:219], v[226:227] op_sel:[1,0,0] op_sel_hi:[1,1,1]\n"
        "  v_pk_fma_f32 v[214:215], v[238:239], v[218:219], v[226:227] op_sel:[1,0,0] op_sel_hi:[1,1,1]\n"
        "  v_pk_fma_f32 v[56:57], v[236:237], v[56:57], v[208:209] op_sel:[0,0,0] op_sel_hi:[0,1,1]\n"
        "  v_pk_fma_f32 v[80:81], v[238:239], v[80:81], v[212:213] op_sel:[0,0,0] op_sel_hi:[0,1,1]\n"
        "  v_pk_fma_f32 v[58:59], v[236:237], v[58:59], v[210:211] op_sel:[0,0,0] op_sel_hi:[0,1,1]\n"
        "  v_pk_fma_f32 v[82:83], v[238:239], v[82:83], v[214:215] op_sel:[0,0,0] op_sel_hi:[0,1,1]\n"
        "  v_max_f32 v56, 0, v56\n"
        "  v_max_f32 v80, 0, v80\n"
        "  v_max_f32 v57, 0, v57\n"
        "  v_max_f32 v81, 0, v81\n"
        "  v_max_f32 v58, 0, v58\n"
        "  v_max_f32 v82, 0, v82\n"
        "  v_max_f32 v59, 0, v59\n"
        "  v_max_f32 v83, 0, v83\n"
        "  v_pk_mul_f32 v[56:57], v[56:57], v[56:57] op_sel:[0,0] op_sel_hi:[1,1]\n"
        "  v_pk_mul_f32 v[80:81], v[80:81], v[80:81] op_sel:[0,0] op_sel_hi:[1,1]\n"
        "  v_pk_mul_f32 v[58:59], v[58:59], v[58:59] op_sel:[0,0] op_sel_hi:[1,1]\n"
        "  v_pk_mul_f32 v[82:83], v[82:83], v[82:83] op_sel:[0,0] op_sel_hi:[1,1]\n"
        "  v_pk_fma_f32 v[208:209], v[236:237], v[220:221], v[228:229] op_sel:[1,0,0] op_sel_hi:[1,1,1]\n"
        "  v_pk_fma_f32 v[212:213], v[238:239], v[220:221], v[228:229] op_sel:[1,0,0] op_sel_hi:[1,1,1]\n"
        "  v_pk_fma_f32 v[210:211], v[236:237], v[222:223], v[230:231] op_sel:[1,0,0] op_sel_hi:[1,1,1]\n"
        "  v_pk_fma_f32 v[214:215], v[238:239], v[222:223], v[230:231] op_sel:[1,0,0] op_sel_hi:[1,1,1]\n"
        "  v_pk_fma_f32 v[60:61], v[236:237], v[60:61], v[208:209] op_sel:[0,0,0] op_sel_hi:[0,1,1]\n"
        "  v_pk_fma_f32 v[84:85], v[238:239], v[84:85], v[212:213] op_sel:[0,0,0] op_sel_hi:[0,1,1]\n"
        "  v_pk_fma_f32 v[62:63], v[236:237], v[62:63], v[210:211] op_sel:[0,0,0] op_sel_hi:[0,1,1]\n"
        "  v_pk_fma_f32 v[86:87], v[238:239], v[86:87], v[214:215] op_sel:[0,0,0] op_sel_hi:[0,1,1]\n"
        "  v_max_f32 v60, 0, v60\n"
        "  v_max_f32 v84, 0, v84\n"
        "  v_max_f32 v61, 0, v61\n"
        "  v_max_f32 v85, 0, v85\n"
        "  v_max_f32 v62, 0, v62\n"
        "  v_max_f32 v86, 0, v86\n"
        "  v_max_f32 v63, 0, v63\n"
        "  v_max_f32 v87, 0, v87\n"
        "  v_pk_mul_f32 v[60:61], v[60:61], v[60:61] op_sel:[0,0] op_sel_hi:[1,1]\n"
        "  v_pk_mul_f32 v[84:85], v[84:85], v[84:85] op_sel:[0,0] op_sel_hi:[1,1]\n"
        "  v_pk_mul_f32 v[62:63], v[62:63], v[62:63] op_sel:[0,0] op_sel_hi:[1,1]\n"
        "  v_pk_mul_f32 v[86:87], v[86:87], v[86:87] op_sel:[0,0] op_sel_hi:[1,1]\n"
        "  v_cvt_pk_bf16_f32 v56, v56, v57\n"
        "  v_cvt_pk_bf16_f32 v80, v80, v81\n"
        "  v_cvt_pk_bf16_f32 v57, v58, v59\n"
        "  v_cvt_pk_bf16_f32 v81, v82, v83\n"
        "  v_cvt_pk_bf16_f32 v58, v60, v61\n"
        "  v_cvt_pk_bf16_f32 v82, v84, v85\n"
        "  v_cvt_pk_bf16_f32 v59, v62, v63\n"
        "  v_cvt_pk_bf16_f32 v83, v86, v87\n"
        "  buffer_store_dwordx4 v[56:59], v182, s[24:27], s94 offen offset:2048 sc1\n"
        "  buffer_store_dwordx4 v[80:83], v182, s[24:27], s94 offen offset:3072 sc1\n"
        "  s_waitcnt lgkmcnt(2)  ; auto\n"
        "  v_pk_fma_f32 v[208:209], v[232:233], v[144:145], v[152:153] op_sel:[1,0,0] op_sel_hi:[1,1,1]\n"
        "  v_pk_fma_f32 v[212:213], v[234:235], v[144:145], v[152:153] op_sel:[1,0,0] op_sel_hi:[1,1,1]\n"
        "  v_pk_fma_f32 v[210:211], v[232:233], v[146:147], v[154:155] op_sel:[1,0,0] op_sel_hi:[1,1,1]\n"
        "  v_pk_fma_f32 v[214:215], v[234:235], v[146:147], v[154:155] op_sel:[1,0,0] op_sel_hi:[1,1,1]\n"
        "  v_pk_fma_f32 v[16:17], v[232:233], v[16:17], v[208:209] op_sel:[0,0,0] op_sel_hi:[0,1,1]\n"
        "  v_pk_fma_f32 v[40:41], v[234:235], v[40:41], v[212:213] op_sel:[0,0,0] op_sel_hi:[0,1,1]\n"
        "  v_pk_fma_f32 v[18:19], v[232:233], v[18:19], v[210:211] op_sel:[0,0,0] op_sel_hi:[0,1,1]\n"
        "  v_pk_fma_f32 v[42:43], v[234:235], v[42:43], v[214:215] op_sel:[0,0,0] op_sel_hi:[0,1,1]\n"
        "  v_max_f32 v16, 0, v16\n"
        "  v_max_f32 v40, 0, v40\n"
        "  v_max_f32 v17, 0, v17\n"
        "  v_max_f32 v41, 0, v41\n"
        "  v_max_f32 v18, 0, v18\n"
        "  v_max_f32 v42, 0, v42\n"
        "  v_max_f32 v19, 0, v19\n"
        "  v_max_f32 v43, 0, v43\n"
        "  v_pk_mul_f32 v[16:17], v[16:17], v[16:17] op_sel:[0,0] op_sel_hi:[1,1]\n"
        "  v_pk_mul_f32 v[40:41], v[40:41], v[40:41] op_sel:[0,0] op_sel_hi:[1,1]\n"
        "  v_pk_mul_f32 v[18:19], v[18:19], v[18:19] op_sel:[0,0] op_sel_hi:[1,1]\n"
        "  v_pk_mul_f32 v[42:43], v[42:43], v[42:43] op_sel:[0,0] op_sel_hi:[1,1]\n"
        "  s_waitcnt lgkmcnt(0)  ; auto\n"
        "  v_pk_fma_f32 v[208:209], v[232:233], v[148:149], v[156:157] op_sel:[1,0,0] op_sel_hi:[1,1,1]\n"
        "  v_pk_fma_f32 v[212:213], v[234:235], v[148:149], v[156:157] op_sel:[1,0,0] op_sel_hi:[1,1,1]\n"
        "  v_pk_fma_f32 v[210:211], v[232:233], v[150:151], v[158:159] op_sel:[1,0,0] op_sel_hi:[1,1,1]\n"
        "  v_pk_fma_f32 v[214:215], v[234:235], v[150:151], v[158:159] op_sel:[1,0,0] op_sel_hi:[1,1,1]\n"
        "  v_pk_fma_f32 v[20:21], v[232:233], v[20:21], v[208:209] op_sel:[0,0,0] op_sel_hi:[0,1,1]\n"
        "  v_pk_fma_f32 v[44:45], v[234:235], v[44:45], v[212:213] op_sel:[0,0,0] op_sel_hi:[0,1,1]\n"
        "  v_pk_fma_f32 v[22:23], v[232:233], v[22:23], v[210:211] op_sel:[0,0,0] op_sel_hi:[0,1,1]\n"
        "  v_pk_fma_f32 v[46:47], v[234:235], v[46:47], v[214:215] op_sel:[0,0,0] op_sel_hi:[0,1,1]\n"
        "  v_max_f32 v20, 0, v20\n"
        "  v_max_f32 v44, 0, v44\n"
        "  v_max_f32 v21, 0, v21\n"
        "  v_max_f32 v45, 0, v45\n"
        "  v_max_f32 v22, 0, v22\n"
        "  v_max_f32 v46, 0, v46\n"
        "  v_max_f32 v23, 0, v23\n"
        "  v_max_f32 v47, 0, v47\n"
        "  v_pk_mul_f32 v[20:21], v[20:21], v[20:21] op_sel:[0,0] op_sel_hi:[1,1]\n"
        "  v_pk_mul_f32 v[44:45], v[44:45], v[44:45] op_sel:[0,0] op_sel_hi:[1,1]\n"
        "  v_pk_mul_f32 v[22:23], v[22:23], v[22:23] op_sel:[0,0] op_sel_hi:[1,1]\n"
        "  v_pk_mul_f32 v[46:47], v[46:47], v[46:47] op_sel:[0,0] op_sel_hi:[1,1]\n"
        "  v_cvt_pk_bf16_f32 v16, v16, v17\n"
        "  v_cvt_pk_bf16_f32 v40, v40, v41\n"
        "  v_cvt_pk_bf16_f32 v17, v18, v19\n"
        "  v_cvt_pk_bf16_f32 v41, v42, v43\n"
        "  v_cvt_pk_bf16_f32 v18, v20, v21\n"
        "  v_cvt_pk_bf16_f32 v42, v44, v45\n"
        "  v_cvt_pk_bf16_f32 v19, v22, v23\n"
        "  v_cvt_pk_bf16_f32 v43, v46, v47\n"
        "  buffer_store_dwordx4 v[16:19], v182, s[24:27], s83 offen offset:0 sc1\n"
        "  buffer_store_dwordx4 v[40:43], v182, s[24:27], s83 offen offset:1024 sc1\n"
        "  v_pk_fma_f32 v[208:209], v[236:237], v[144:145], v[152:153] op_sel:[1,0,0] op_sel_hi:[1,1,1]\n"
        "  v_pk_fma_f32 v[212:213], v[238:239], v[144:145], v[152:153] op_sel:[1,0,0] op_sel_hi:[1,1,1]\n"
        "  v_pk_fma_f32 v[210:211], v[236:237], v[146:147], v[154:155] op_sel:[1,0,0] op_sel_hi:[1,1,1]\n"
        "  v_pk_fma_f32 v[214:215], v[238:239], v[146:147], v[154:155] op_sel:[1,0,0] op_sel_hi:[1,1,1]\n"
        "  v_pk_fma_f32 v[64:65], v[236:237], v[64:65], v[208:209] op_sel:[0,0,0] op_sel_hi:[0,1,1]\n"
        "  v_pk_fma_f32 v[88:89], v[238:239], v[88:89], v[212:213] op_sel:[0,0,0] op_sel_hi:[0,1,1]\n"
        "  v_pk_fma_f32 v[66:67], v[236:237], v[66:67], v[210:211] op_sel:[0,0,0] op_sel_hi:[0,1,1]\n"
        "  v_pk_fma_f32 v[90:91], v[238:239], v[90:91], v[214:215] op_sel:[0,0,0] op_sel_hi:[0,1,1]\n"
        "  v_max_f32 v64, 0, v64\n"
        "  v_max_f32 v88, 0, v88\n"
        "  v_max_f32 v65, 0, v65\n"
        "  v_max_f32 v89, 0, v89\n"
        "  v_max_f32 v66, 0, v66\n"
        "  v_max_f32 v90, 0, v90\n"
        "  v_max_f32 v67, 0, v67\n"
        "  v_max_f32 v91, 0, v91\n"
        "  v_pk_mul_f32 v[64:65], v[64:65], v[64:65] op_sel:[0,0] op_sel_hi:[1,1]\n"
        "  v_pk_mul_f32 v[88:89], v[88:89], v[88:89] op_sel:[0,0] op_sel_hi:[1,1]\n"
        "  v_pk_mul_f32 v[66:67], v[66:67], v[66:67] op_sel:[0,0] op_sel_hi:[1,1]\n"
        "  v_pk_mul_f32 v[90:91], v[90:91], v[90:91] op_sel:[0,0] op_sel_hi:[1,1]\n"
        "  v_pk_fma_f32 v[208:209], v[236:237], v[148:149], v[156:157] op_sel:[1,0,0] op_sel_hi:[1,1,1]\n"
        "  v_pk_fma_f32 v[212:213], v[238:239], v[148:149], v[156:157] op_sel:[1,0,0] op_sel_hi:[1,1,1]\n"
        "  v_pk_fma_f32 v[210:211], v[236:237], v[150:151], v[158:159] op_sel:[1,0,0] op_sel_hi:[1,1,1]\n"
        "  v_pk_fma_f32 v[214:215], v[238:239], v[150:151], v[158:159] op_sel:[1,0,0] op_sel_hi:[1,1,1]\n"
        "  v_pk_fma_f32 v[68:69], v[236:237], v[68:69], v[208:209] op_sel:[0,0,0] op_sel_hi:[0,1,1]\n"
        "  v_pk_fma_f32 v[92:93], v[238:239], v[92:93], v[212:213] op_sel:[0,0,0] op_sel_hi:[0,1,1]\n"
        "  v_pk_fma_f32 v[70:71], v[236:237], v[70:71], v[210:211] op_sel:[0,0,0] op_sel_hi:[0,1,1]\n"
        "  v_pk_fma_f32 v[94:95], v[238:239], v[94:95], v[214:215] op_sel:[0,0,0] op_sel_hi:[0,1,1]\n"
        "  v_max_f32 v68, 0, v68\n"
        "  v_max_f32 v92, 0, v92\n"
        "  v_max_f32 v69, 0, v69\n"
        "  v_max_f32 v93, 0, v93\n"
        "  v_max_f32 v70, 0, v70\n"
        "  v_max_f32 v94, 0, v94\n"
        "  v_max_f32 v71, 0, v71\n"
        "  v_max_f32 v95, 0, v95\n"
        "  v_pk_mul_f32 v[68:69], v[68:69], v[68:69] op_sel:[0,0] op_sel_hi:[1,1]\n"
        "  v_pk_mul_f32 v[92:93], v[92:93], v[92:93] op_sel:[0,0] op_sel_hi:[1,1]\n"
        "  v_pk_mul_f32 v[70:71], v[70:71], v[70:71] op_sel:[0,0] op_sel_hi:[1,1]\n"
        "  v_pk_mul_f32 v[94:95], v[94:95], v[94:95] op_sel:[0,0] op_sel_hi:[1,1]\n"
        "  v_cvt_pk_bf16_f32 v64, v64, v65\n"
        "  v_cvt_pk_bf16_f32 v88, v88, v89\n"
        "  v_cvt_pk_bf16_f32 v65, v66, v67\n"
        "  v_cvt_pk_bf16_f32 v89, v90, v91\n"
        "  v_cvt_pk_bf16_f32 v66, v68, v69\n"
        "  v_cvt_pk_bf16_f32 v90, v92, v93\n"
        "  v_cvt_pk_bf16_f32 v67, v70, v71\n"
        "  v_cvt_pk_bf16_f32 v91, v94, v95\n"
        "  buffer_store_dwordx4 v[64:67], v182, s[24:27], s83 offen offset:2048 sc1\n"
        "  buffer_store_dwordx4 v[88:91], v182, s[24:27], s83 offen offset:3072 sc1\n"
        "  buffer_load_dwordx4 v174, s[16:19], s60 offen offset:2048 lds\n"
        "  buffer_load_dwordx4 v175, s[16:19], s60 offen offset:3072 lds\n"
        "  s_add_u32 s60, s60, 28672\n"
        "  s_add_u32 m0, s59, 8192\n"
        "  s_nop 0  ; hazard\n"
        "  buffer_load_dwordx4 v172, s[16:19], s60 offen offset:0 lds\n"
        "  buffer_load_dwordx4 v173, s[16:19], s60 offen offset:1024 lds\n"
        "  buffer_load_dwordx4 v174, s[16:19], s60 offen offset:2048 lds\n"
        "  buffer_load_dwordx4 v175, s[16:19], s60 offen offset:3072 lds\n"
        "  s_add_u32 s60, s60, 28672\n"
        "  s_waitcnt vmcnt(16)\n"
        "  s_mov_b32 s87, 262144\n"
        "  ds_read_b64_tr_b16 a[96:97], v178 offset:49152\n"
        "  ds_read_b64_tr_b16 a[98:99], v178 offset:51200\n"
        "  ds_read_b64_tr_b16 a[100:101], v179 offset:49152\n"
        "  ds_read_b64_tr_b16 a[102:103], v179 offset:51200\n"
        "  ds_read_b64_tr_b16 a[104:105], v180 offset:49152\n"
        "  ds_read_b64_tr_b16 a[106:107], v180 offset:51200\n"
        "  ds_read_b64_tr_b16 a[108:109], v181 offset:49152\n"
        "  ds_read_b64_tr_b16 a[110:111], v181 offset:51200\n"
        "  ds_read_b64_tr_b16 a[112:113], v178 offset:53248\n"
        "  ds_read_b64_tr_b16 a[114:115], v178 offset:55296\n"
        "  ds_read_b64_tr_b16 a[116:117], v179 offset:53248\n"
        "  ds_read_b64_tr_b16 a[118:119], v179 offset:55296\n"
        "  ds_read_b64_tr_b16 a[120:121], v180 offset:53248\n"
        "  ds_read_b64_tr_b16 a[122:123], v180 offset:55296\n"
        "  ds_read_b64_tr_b16 a[124:125], v181 offset:53248\n"
        "  ds_read_b64_tr_b16 a[126:127], v181 offset:55296\n"
        "  ds_read_b64_tr_b16 a[128:129], v178 offset:57344\n"
        "  ds_read_b64_tr_b16 a[130:131], v178 offset:59392\n"
        "  ds_read_b64_tr_b16 a[132:133], v179 offset:57344\n"
        "  ds_read_b64_tr_b16 a[134:135], v179 offset:59392\n"
        "  ds_read_b64_tr_b16 a[136:137], v180 offset:57344\n"
        "  ds_read_b64_tr_b16 a[138:139], v180 offset:59392\n"
        "  ds_read_b64_tr_b16 a[140:141], v181 offset:57344\n"
        "  ds_read_b64_tr_b16 a[142:143], v181 offset:59392\n"
        "  s_add_u32 m0, s59, 49152\n"
        "  s_waitcnt lgkmcnt(15)  ; auto\n"
        "  buffer_load_dwordx4 v172, s[16:19], s60 offen offset:0 lds\n"
        "  buffer_load_dwordx4 v173, s[16:19], s60 offen offset:1024 lds\n"
        "  buffer_load_dwordx4 v174, s[16:19], s60 offen offset:2048 lds\n"
        "  buffer_load_dwordx4 v175, s[16:19], s60 offen offset:3072 lds\n"
        "  s_add_u32 s60, s60, 28672\n"
        "  s_add_u32 m0, s59, 53248\n"
        "  s_waitcnt lgkmcnt(8)  ; auto\n"
        "  buffer_load_dwordx4 v172, s[16:19], s60 offen offset:0 lds\n"
        "  buffer_load_dwordx4 v173, s[16:19], s60 offen offset:1024 lds\n"
        "  buffer_load_dwordx4 v174, s[16:19], s60 offen offset:2048 lds\n"
        "  buffer_load_dwordx4 v175, s[16:19], s60 offen offset:3072 lds\n"
        "  s_add_u32 s60, s60, 28672\n"
        "  s_add_u32 m0, s59, 57344\n"
        "  s_waitcnt lgkmcnt(0)  ; auto\n"
        "  buffer_load_dwordx4 v172, s[16:19], s60 offen offset:0 lds\n"
        "  buffer_load_dwordx4 v173, s[16:19], s60 offen offset:1024 lds\n"
        "  buffer_load_dwordx4 v174, s[16:19], s60 offen offset:2048 lds\n"
        "  buffer_load_dwordx4 v175, s[16:19], s60 offen offset:3072 lds\n"
        "  s_add_u32 s60, s60, 28672\n"
        "  s_waitcnt vmcnt(18)  ; auto\n"
        "  s_barrier\n"
        "  s_mov_b64 exec, s[78:79]\n"
        "  v_mov_b32 v190, v188\n"
        "  v_mov_b32 v186, v188\n"
        "  buffer_store_dwordx2 v[190:191], v185, s[24:27], 0 offen sc1\n"
        "  s_mov_b64 exec, -1\n"
        "  s_cmp_eq_u32 s86, 0\n"
        "  s_cbranch_scc1 .Lfin\n"
        "  s_branch .Lpub\n"
        ".Lfw0:\n"
        "  ds_read_b64_tr_b16 a[16:17], v162 offset:0\n"
        "  ds_read_b64_tr_b16 a[18:19], v162 offset:3072\n"
        "  ds_read_b128 a[0:3], v160 offset:0\n"
        "  ds_read_b128 a[4:7], v160 offset:2048\n"
        "  ds_read_b128 a[8:11], v160 offset:4096\n"
        "  ds_read_b128 a[12:15], v160 offset:6144\n"
        "  ds_read_b64_tr_b16 a[20:21], v163 offset:0\n"
        "  ds_read_b64_tr_b16 a[22:23], v163 offset:3072\n"
        "  ds_read_b64_tr_b16 a[24:25], v164 offset:0\n"
        "  ds_read_b64_tr_b16 a[26:27], v164 offset:3072\n"
        "  ds_read_b64_tr_b16 a[28:29], v165 offset:0\n"
        "  ds_read_b64_tr_b16 a[30:31], v165 offset:3072\n"
        "  ds_read_b64_tr_b16 a[32:33], v166 offset:0\n"
        "  ds_read_b64_tr_b16 a[34:35], v166 offset:3072\n"
        "  ds_read_b64_tr_b16 a[36:37], v167 offset:0\n"
        "  ds_read_b64_tr_b16 a[38:39], v167 offset:3072\n"
        "  ds_read_b64_tr_b16 a[40:41], v168 offset:0\n"
        "  ds_read_b64_tr_b16 a[42:43], v168 offset:3072\n"
        "  ds_read_b64_tr_b16 a[44:45], v169 offset:0\n"
        "  ds_read_b64_tr_b16 a[46:47], v169 offset:3072\n"
        "  s_mov_b32 s52, 49152\n"
        "  s_waitcnt lgkmcnt(15)  ; auto\n"
        "  v_mfma_f32_16x16x32_bf16 v[0:3], a[16:19], a[0:3], v[0:3]\n"
        "  s_add_u32 m0, s69, s87\n"
        "  ds_read_b64_tr_b16 a[64:65], v162 offset:14336\n"
        "  v_mfma_f32_16x16x32_bf16 v[32:35], a[16:19], a[4:7], v[32:35]\n"
        "  buffer_load_dwordx4 v156, s[8:11], s68 offen offset:0 lds\n"
        "  v_mfma_f32_16x16x32_bf16 v[64:67], a[16:19], a[8:11], v[64:67]\n"
        "  ds_read_b64_tr_b16 a[66:67], v162 offset:17408\n"
        "  s_waitcnt lgkmcnt(15)  ; auto\n"
        "  v_mfma_f32_16x16x32_bf16 v[96:99], a[16:19], a[12:15], v[96:99]\n"
        "  buffer_load_dwordx4 v157, s[8:11], s68 offen offset:1024 lds\n"
        "  v_mfma_f32_16x16x32_bf16 v[128:131], a[16:19], a[128:131], v[128:131]\n"
        "  ds_read_b128 a[48:51], v161 offset:0\n"
        "  s_waitcnt lgkmcnt(15)  ; auto\n"
        "  v_mfma_f32_16x16x32_bf16 v[4:7], a[20:23], a[0:3], v[4:7]\n"
        "  ds_read_b128 a[52:55], v161 offset:2048\n"
        "  v_mfma_f32_16x16x32_bf16 v[36:39], a[20:23], a[4:7], v[36:39]\n"
        "  buffer_load_dwordx4 v158, s[8:11], s68 offen offset:2048 lds\n"
        "  v_mfma_f32_16x16x32_bf16 v[68:71], a[20:23], a[8:11], v[68:71]\n"
        "  s_add_u32 m0, s51, s56\n"
        "  ds_read_b128 a[56:59], v161 offset:4096\n"
        "  v_mfma_f32_16x16x32_bf16 v[100:103], a[20:23], a[12:15], v[100:103]\n"
        "  ds_read_b128 a[60:63], v161 offset:6144\n"
        "  v_mfma_f32_16x16x32_bf16 v[132:135], a[20:23], a[128:131], v[132:135]\n"
        "  buffer_load_dwordx4 v148, s[4:7], s48 offen offset:0 lds\n"
        "  s_waitcnt lgkmcnt(15)  ; auto\n"
        "  v_mfma_f32_16x16x32_bf16 v[8:11], a[24:27], a[0:3], v[8:11]\n"
        "  s_add_u32 s88, s88, 1\n"
        "  ds_read_b64_tr_b16 a[68:69], v163 offset:14336\n"
        "  v_mfma_f32_16x16x32_bf16 v[40:43], a[24:27], a[4:7], v[40:43]\n"
        "  ds_read_b64_tr_b16 a[70:71], v163 offset:17408\n"
        "  v_mfma_f32_16x16x32_bf16 v[72:75], a[24:27], a[8:11], v[72:75]\n"
        "  buffer_load_dwordx4 v149, s[4:7], s48 offen offset:1024 lds\n"
        "  v_mfma_f32_16x16x32_bf16 v[104:107], a[24:27], a[12:15], v[104:107]\n"
        "  s_cmp_eq_u32 s88, 7\n"
        "  s_cselect_b32 s88, 0, s88\n"
        "  s_cselect_b32 s89, -6, 1\n"
        "  ds_read_b64_tr_b16 a[72:73], v164 offset:14336\n"
        "  v_mfma_f32_16x16x32_bf16 v[136:139], a[24:27], a[128:131], v[136:139]\n"
        "  ds_read_b64_tr_b16 a[74:75], v164 offset:17408\n"
        "  s_waitcnt lgkmcnt(15)  ; auto\n"
        "  v_mfma_f32_16x16x32_bf16 v[12:15], a[28:31], a[0:3], v[12:15]\n"
        "  buffer_load_dwordx4 v150, s[4:7], s48 offen offset:2048 lds\n"
        "  v_mfma_f32_16x16x32_bf16 v[44:47], a[28:31], a[4:7], v[44:47]\n"
        "  ds_read_b64_tr_b16 a[76:77], v165 offset:14336\n"
        "  v_mfma_f32_16x16x32_bf16 v[76:79], a[28:31], a[8:11], v[76:79]\n"
        "  ds_read_b64_tr_b16 a[78:79], v165 offset:17408\n"
        "  v_mfma_f32_16x16x32_bf16 v[108:111], a[28:31], a[12:15], v[108:111]\n"
        "  buffer_load_dwordx4 v151, s[4:7], s48 offen offset:3072 lds\n"
        "  v_mfma_f32_16x16x32_bf16 v[140:143], a[28:31], a[128:131], v[140:143]\n"
        "  s_add_u32 m0, s51, s57\n"
        "  ds_read_b64_tr_b16 a[80:81], v166 offset:14336\n"
        "  v_mfma_f32_16x16x32_bf16 v[16:19], a[32:35], a[0:3], v[16:19]\n"
        "  ds_read_b64_tr_b16 a[82:83], v166 offset:17408\n"
        "  v_mfma_f32_16x16x32_bf16 v[48:51], a[32:35], a[4:7], v[48:51]\n"
        "  buffer_load_dwordx4 v153, s[8:11], s50 offen offset:0 lds\n"
        "  v_mfma_f32_16x16x32_bf16 v[80:83], a[32:35], a[8:11], v[80:83]\n"
        "  s_lshl_b32 s90, s89, 7\n"
        "  ds_read_b64_tr_b16 a[84:85], v167 offset:14336\n"
        "  v_mfma_f32_16x16x32_bf16 v[112:115], a[32:35], a[12:15], v[112:115]\n"
        "  s_add_u32 s48, s48, s90\n"
        "  ds_read_b64_tr_b16 a[86:87], v167 offset:17408\n"
        "  s_waitcnt lgkmcnt(15)  ; auto\n"
        "  v_mfma_f32_16x16x32_bf16 v[20:23], a[36:39], a[0:3], v[20:23]\n"
        "  buffer_load_dwordx4 v154, s[8:11], s50 offen offset:1024 lds\n"
        "  v_mfma_f32_16x16x32_bf16 v[52:55], a[36:39], a[4:7], v[52:55]\n"
        "  s_mov_b32 s68, s50\n"
        "  ds_read_b64_tr_b16 a[88:89], v168 offset:14336\n"
        "  v_mfma_f32_16x16x32_bf16 v[84:87], a[36:39], a[8:11], v[84:87]\n"
        "  s_mov_b32 s69, s51\n"
        "  ds_read_b64_tr_b16 a[90:91], v168 offset:17408\n"
        "  v_mfma_f32_16x16x32_bf16 v[116:119], a[36:39], a[12:15], v[116:119]\n"
        "  buffer_load_dwordx4 v155, s[8:11], s50 offen offset:2048 lds\n"
        "  v_mfma_f32_16x16x32_bf16 v[24:27], a[40:43], a[0:3], v[24:27]\n"
        "  s_add_u32 m0, s51, s58\n"
        "  s_mul_i32 s90, s89, s85\n"
        "  ds_read_b64_tr_b16 a[92:93], v169 offset:14336\n"
        "  v_mfma_f32_16x16x32_bf16 v[56:59], a[40:43], a[4:7], v[56:59]\n"
        "  s_add_u32 s50, s50, s90\n"
        "  ds_read_b64_tr_b16 a[94:95], v169 offset:17408\n"
        "  v_add_u32 v160, s52, v160\n"
        "  v_mfma_f32_16x16x32_bf16 v[88:91], a[40:43], a[8:11], v[88:91]\n"
        "  s_mov_b64 exec, s[64:65]\n"
        "  buffer_load_dwordx4 v152, s[12:15], s49 offen offset:0 lds\n"
        "  s_mov_b64 exec, -1\n"
        "  v_mfma_f32_16x16x32_bf16 v[120:123], a[40:43], a[12:15], v[120:123]\n"
        "  s_mul_i32 s90, s89, s84\n"
        "  s_add_u32 s49, s49, s90\n"
        "  s_and_b32 s94, s45, 1\n"
        "  v_mfma_f32_16x16x32_bf16 v[28:31], a[44:47], a[0:3], v[28:31]\n"
        "  s_lshl1_add_u32 s95, s44, s94\n"
        "  s_mul_i32 s95, s95, 448\n"
        "  v_lshlrev_b32 v216, 3, v204\n"
        "  v_mfma_f32_16x16x32_bf16 v[60:63], a[44:47], a[4:7], v[60:63]\n"
        "  s_mov_b32 exec_hi, 0xffffff\n"
        "  buffer_load_dwordx2 v[192:193], v216, s[32:35], s95 offen\n"
        "  s_mov_b32 exec_hi, -1\n"
        "  v_mfma_f32_16x16x32_bf16 v[92:95], a[44:47], a[8:11], v[92:95]\n"
        "  v_add_u32 v161, s52, v161\n"
        "  v_add_u32 v162, s52, v162\n"
        "  v_add_u32 v163, s52, v163\n"
        "  v_add_u32 v164, s52, v164\n"
        "  v_add_u32 v165, s52, v165\n"
        "  v_add_u32 v166, s52, v166\n"
        "  v_add_u32 v167, s52, v167\n"
        "  v_add_u32 v168, s52, v168\n"
        "  v_add_u32 v169, s52, v169\n"
        "  s_mov_b32 s51, 0\n"
        "  s_mov_b32 s54, 49152\n"
        "  v_mfma_f32_16x16x32_bf16 v[124:127], a[44:47], a[12:15], v[124:127]\n"
        "  s_waitcnt vmcnt(9) lgkmcnt(0)\n"
        "  s_barrier\n"
        "  s_mov_b32 s53, 4\n"
        "  .p2align 3\n"
        ".Lf1_0:\n"
        "  v_mfma_f32_16x16x32_bf16 v[0:3], a[64:67], a[48:51], v[0:3]\n"
        "  s_add_u32 m0, s69, s87\n"
        "  ds_read_b64_tr_b16 a[16:17], v162 offset:0\n"
        "  v_mfma_f32_16x16x32_bf16 v[32:35], a[64:67], a[52:55], v[32:35]\n"
        "  buffer_load_dwordx4 v156, s[8:11], s68 offen offset:0 lds\n"
        "  v_mfma_f32_16x16x32_bf16 v[64:67], a[64:67], a[56:59], v[64:67]\n"
        "  ds_read_b64_tr_b16 a[18:19], v162 offset:3072\n"
        "  v_mfma_f32_16x16x32_bf16 v[96:99], a[64:67], a[60:63], v[96:99]\n"
        "  ds_read_b128 a[0:3], v160 offset:0\n"
        "  v_mfma_f32_16x16x32_bf16 v[128:131], a[64:67], a[128:131], v[128:131]\n"
        "  ds_read_b128 a[4:7], v160 offset:2048\n"
        "  v_mfma_f32_16x16x32_bf16 v[4:7], a[68:71], a[48:51], v[4:7]\n"
        "  ds_read_b128 a[8:11], v160 offset:4096\n"
        "  v_mfma_f32_16x16x32_bf16 v[36:39], a[68:71], a[52:55], v[36:39]\n"
        "  ds_read_b128 a[12:15], v160 offset:6144\n"
        "  v_mfma_f32_16x16x32_bf16 v[68:71], a[68:71], a[56:59], v[68:71]\n"
        "  buffer_load_dwordx4 v157, s[8:11], s68 offen offset:1024 lds\n"
        "  v_mfma_f32_16x16x32_bf16 v[100:103], a[68:71], a[60:63], v[100:103]\n"
        "  ds_read_b64_tr_b16 a[20:21], v163 offset:0\n"
        "  v_mfma_f32_16x16x32_bf16 v[132:135], a[68:71], a[128:131], v[132:135]\n"
        "  ds_read_b64_tr_b16 a[22:23], v163 offset:3072\n"
        "  v_mfma_f32_16x16x32_bf16 v[8:11], a[72:75], a[48:51], v[8:11]\n"
        "  ds_read_b64_tr_b16 a[24:25], v164 offset:0\n"
        "  v_mfma_f32_16x16x32_bf16 v[40:43], a[72:75], a[52:55], v[40:43]\n"
        "  ds_read_b64_tr_b16 a[26:27], v164 offset:3072\n"
        "  v_mfma_f32_16x16x32_bf16 v[72:75], a[72:75], a[56:59], v[72:75]\n"
        "  ds_read_b64_tr_b16 a[28:29], v165 offset:0\n"
        "  v_mfma_f32_16x16x32_bf16 v[104:107], a[72:75], a[60:63], v[104:107]\n"
        "  ds_read_b64_tr_b16 a[30:31], v165 offset:3072\n"
        "  v_mfma_f32_16x16x32_bf16 v[136:139], a[72:75], a[128:131], v[136:139]\n"
        "  buffer_load_dwordx4 v158, s[8:11], s68 offen offset:2048 lds\n"
        "  v_mfma_f32_16x16x32_bf16 v[12:15], a[76:79], a[48:51], v[12:15]\n"
        "  s_add_u32 m0, s51, s56\n"
        "  ds_read_b64_tr_b16 a[32:33], v166 offset:0\n"
        "  v_mfma_f32_16x16x32_bf16 v[44:47], a[76:79], a[52:55], v[44:47]\n"
        "  ds_read_b64_tr_b16 a[34:35], v166 offset:3072\n"
        "  v_mfma_f32_16x16x32_bf16 v[76:79], a[76:79], a[56:59], v[76:79]\n"
        "  ds_read_b64_tr_b16 a[36:37], v167 offset:0\n"
        "  v_mfma_f32_16x16x32_bf16 v[108:111], a[76:79], a[60:63], v[108:111]\n"
        "  ds_read_b64_tr_b16 a[38:39], v167 offset:3072\n"
        "  v_mfma_f32_16x16x32_bf16 v[140:143], a[76:79], a[128:131], v[140:143]\n"
        "  ds_read_b64_tr_b16 a[40:41], v168 offset:0\n"
        "  v_mfma_f32_16x16x32_bf16 v[16:19], a[80:83], a[48:51], v[16:19]\n"
        "  buffer_load_dwordx4 v148, s[4:7], s48 offen offset:0 lds\n"
        "  v_mfma_f32_16x16x32_bf16 v[48:51], a[80:83], a[52:55], v[48:51]\n"
        "  s_add_u32 s88, s88, 1\n"
        "  ds_read_b64_tr_b16 a[42:43], v168 offset:3072\n"
        "  v_mfma_f32_16x16x32_bf16 v[80:83], a[80:83], a[56:59], v[80:83]\n"
        "  ds_read_b64_tr_b16 a[44:45], v169 offset:0\n"
        "  v_mfma_f32_16x16x32_bf16 v[112:115], a[80:83], a[60:63], v[112:115]\n"
        "  ds_read_b64_tr_b16 a[46:47], v169 offset:3072\n"
        "  v_mfma_f32_16x16x32_bf16 v[20:23], a[84:87], a[48:51], v[20:23]\n"
        "  ds_read_b64_tr_b16 a[64:65], v162 offset:14336\n"
        "  v_mfma_f32_16x16x32_bf16 v[52:55], a[84:87], a[52:55], v[52:55]\n"
        "  ds_read_b64_tr_b16 a[66:67], v162 offset:17408\n"
        "  v_mfma_f32_16x16x32_bf16 v[84:87], a[84:87], a[56:59], v[84:87]\n"
        "  ds_read_b64_tr_b16 a[68:69], v163 offset:14336\n"
        "  v_mfma_f32_16x16x32_bf16 v[116:119], a[84:87], a[60:63], v[116:119]\n"
        "  buffer_load_dwordx4 v149, s[4:7], s48 offen offset:1024 lds\n"
        "  v_mfma_f32_16x16x32_bf16 v[24:27], a[88:91], a[48:51], v[24:27]\n"
        "  s_cmp_eq_u32 s88, 7\n"
        "  s_cselect_b32 s88, 0, s88\n"
        "  s_cselect_b32 s89, -6, 1\n"
        "  ds_read_b64_tr_b16 a[70:71], v163 offset:17408\n"
        "  v_mfma_f32_16x16x32_bf16 v[56:59], a[88:91], a[52:55], v[56:59]\n"
        "  ds_read_b64_tr_b16 a[72:73], v164 offset:14336\n"
        "  v_mfma_f32_16x16x32_bf16 v[88:91], a[88:91], a[56:59], v[88:91]\n"
        "  ds_read_b64_tr_b16 a[74:75], v164 offset:17408\n"
        "  v_mfma_f32_16x16x32_bf16 v[120:123], a[88:91], a[60:63], v[120:123]\n"
        "  ds_read_b64_tr_b16 a[76:77], v165 offset:14336\n"
        "  s_add_u32 s55, s54, 49152\n"
        "  v_mfma_f32_16x16x32_bf16 v[28:31], a[92:95], a[48:51], v[28:31]\n"
        "  ds_read_b64_tr_b16 a[78:79], v165 offset:17408\n"
        "  v_mfma_f32_16x16x32_bf16 v[60:63], a[92:95], a[52:55], v[60:63]\n"
        "  buffer_load_dwordx4 v150, s[4:7], s48 offen offset:2048 lds\n"
        "  s_cmp_eq_u32 s54, 98304\n"
        "  s_cselect_b32 s55, 0, s55\n"
        "  v_mfma_f32_16x16x32_bf16 v[92:95], a[92:95], a[56:59], v[92:95]\n"
        "  ds_read_b64_tr_b16 a[80:81], v166 offset:14336\n"
        "  s_sub_u32 s52, s55, s54\n"
        "  v_mfma_f32_16x16x32_bf16 v[124:127], a[92:95], a[60:63], v[124:127]\n"
        "  ds_read_b64_tr_b16 a[82:83], v166 offset:17408\n"
        "  s_waitcnt lgkmcnt(10)\n"
        "  v_mfma_f32_16x16x32_bf16 v[0:3], a[16:19], a[0:3], v[0:3]\n"
        "  ds_read_b64_tr_b16 a[84:85], v167 offset:14336\n"
        "  v_mfma_f32_16x16x32_bf16 v[32:35], a[16:19], a[4:7], v[32:35]\n"
        "  ds_read_b64_tr_b16 a[86:87], v167 offset:17408\n"
        "  v_mfma_f32_16x16x32_bf16 v[64:67], a[16:19], a[8:11], v[64:67]\n"
        "  ds_read_b64_tr_b16 a[88:89], v168 offset:14336\n"
        "  v_mfma_f32_16x16x32_bf16 v[96:99], a[16:19], a[12:15], v[96:99]\n"
        "  ds_read_b64_tr_b16 a[90:91], v168 offset:17408\n"
        "  v_mfma_f32_16x16x32_bf16 v[128:131], a[16:19], a[128:131], v[128:131]\n"
        "  buffer_load_dwordx4 v151, s[4:7], s48 offen offset:3072 lds\n"
        "  v_mfma_f32_16x16x32_bf16 v[4:7], a[20:23], a[0:3], v[4:7]\n"
        "  s_add_u32 m0, s51, s57\n"
        "  ds_read_b128 a[48:51], v161 offset:0\n"
        "  v_mfma_f32_16x16x32_bf16 v[36:39], a[20:23], a[4:7], v[36:39]\n"
        "  ds_read_b128 a[52:55], v161 offset:2048\n"
        "  v_mfma_f32_16x16x32_bf16 v[68:71], a[20:23], a[8:11], v[68:71]\n"
        "  ds_read_b128 a[56:59], v161 offset:4096\n"
        "  v_mfma_f32_16x16x32_bf16 v[100:103], a[20:23], a[12:15], v[100:103]\n"
        "  ds_read_b128 a[60:63], v161 offset:6144\n"
        "  v_mfma_f32_16x16x32_bf16 v[132:135], a[20:23], a[128:131], v[132:135]\n"
        "  ds_read_b64_tr_b16 a[92:93], v169 offset:14336\n"
        "  v_mfma_f32_16x16x32_bf16 v[8:11], a[24:27], a[0:3], v[8:11]\n"
        "  buffer_load_dwordx4 v153, s[8:11], s50 offen offset:0 lds\n"
        "  v_mfma_f32_16x16x32_bf16 v[40:43], a[24:27], a[4:7], v[40:43]\n"
        "  s_lshl_b32 s90, s89, 7\n"
        "  ds_read_b64_tr_b16 a[94:95], v169 offset:17408\n"
        "  v_add_u32 v160, s52, v160\n"
        "  v_mfma_f32_16x16x32_bf16 v[72:75], a[24:27], a[8:11], v[72:75]\n"
        "  v_add_u32 v161, s52, v161\n"
        "  v_add_u32 v162, s52, v162\n"
        "  v_add_u32 v163, s52, v163\n"
        "  v_mfma_f32_16x16x32_bf16 v[104:107], a[24:27], a[12:15], v[104:107]\n"
        "  v_add_u32 v164, s52, v164\n"
        "  v_add_u32 v165, s52, v165\n"
        "  v_add_u32 v166, s52, v166\n"
        "  v_mfma_f32_16x16x32_bf16 v[136:139], a[24:27], a[128:131], v[136:139]\n"
        "  s_add_u32 s48, s48, s90\n"
        "  v_add_u32 v167, s52, v167\n"
        "  v_add_u32 v168, s52, v168\n"
        "  v_mfma_f32_16x16x32_bf16 v[12:15], a[28:31], a[0:3], v[12:15]\n"
        "  v_add_u32 v169, s52, v169\n"
        "  v_mfma_f32_16x16x32_bf16 v[44:47], a[28:31], a[4:7], v[44:47]\n"
        "  v_mfma_f32_16x16x32_bf16 v[76:79], a[28:31], a[8:11], v[76:79]\n"
        "  buffer_load_dwordx4 v154, s[8:11], s50 offen offset:1024 lds\n"
        "  v_mfma_f32_16x16x32_bf16 v[108:111], a[28:31], a[12:15], v[108:111]\n"
        "  s_mov_b32 s68, s50\n"
        "  v_mfma_f32_16x16x32_bf16 v[140:143], a[28:31], a[128:131], v[140:143]\n"
        "  v_mfma_f32_16x16x32_bf16 v[16:19], a[32:35], a[0:3], v[16:19]\n"
        "  s_mov_b32 s69, s51\n"
        "  v_mfma_f32_16x16x32_bf16 v[48:51], a[32:35], a[4:7], v[48:51]\n"
        "  v_mfma_f32_16x16x32_bf16 v[80:83], a[32:35], a[8:11], v[80:83]\n"
        "  v_mfma_f32_16x16x32_bf16 v[112:115], a[32:35], a[12:15], v[112:115]\n"
        "  buffer_load_dwordx4 v155, s[8:11], s50 offen offset:2048 lds\n"
        "  v_mfma_f32_16x16x32_bf16 v[20:23], a[36:39], a[0:3], v[20:23]\n"
        "  s_add_u32 m0, s51, s58\n"
        "  v_mfma_f32_16x16x32_bf16 v[52:55], a[36:39], a[4:7], v[52:55]\n"
        "  v_mfma_f32_16x16x32_bf16 v[84:87], a[36:39], a[8:11], v[84:87]\n"
        "  s_mul_i32 s90, s89, s85\n"
        "  v_mfma_f32_16x16x32_bf16 v[116:119], a[36:39], a[12:15], v[116:119]\n"
        "  v_mfma_f32_16x16x32_bf16 v[24:27], a[40:43], a[0:3], v[24:27]\n"
        "  s_add_u32 s50, s50, s90\n"
        "  v_mfma_f32_16x16x32_bf16 v[56:59], a[40:43], a[4:7], v[56:59]\n"
        "  v_mfma_f32_16x16x32_bf16 v[88:91], a[40:43], a[8:11], v[88:91]\n"
        "  s_mov_b64 exec, s[64:65]\n"
        "  buffer_load_dwordx4 v152, s[12:15], s49 offen offset:0 lds\n"
        "  s_mov_b64 exec, -1\n"
        "  s_mul_i32 s90, s89, s84\n"
        "  v_mfma_f32_16x16x32_bf16 v[120:123], a[40:43], a[12:15], v[120:123]\n"
        "  s_add_u32 s49, s49, s90\n"
        "  v_mfma_f32_16x16x32_bf16 v[28:31], a[44:47], a[0:3], v[28:31]\n"
        "  v_mfma_f32_16x16x32_bf16 v[60:63], a[44:47], a[4:7], v[60:63]\n"
        "  s_mov_b32 s51, s54\n"
        "  s_mov_b32 s54, s55\n"
        "  v_mfma_f32_16x16x32_bf16 v[92:95], a[44:47], a[8:11], v[92:95]\n"
        "  s_sub_u32 s53, s53, 1\n"
        "  s_cmp_lg_u32 s53, 0\n"
        "  v_mfma_f32_16x16x32_bf16 v[124:127], a[44:47], a[12:15], v[124:127]\n"
        "  s_waitcnt vmcnt(8) lgkmcnt(0)\n"
        "  s_barrier\n"
        "  s_cbranch_scc1 .Lf1_0\n"
        "  v_mfma_f32_16x16x32_bf16 v[0:3], a[64:67], a[48:51], v[0:3]\n"
        "  s_lshr_b32 s94, s63, 7\n"
        "  s_add_u32 s89, s40, s94\n"
        "  s_and_b32 s89, s89, 3\n"
        "  ds_read_b64_tr_b16 a[16:17], v162 offset:0\n"
        "  v_mfma_f32_16x16x32_bf16 v[32:35], a[64:67], a[52:55], v[32:35]\n"
        "  s_mov_b32 s84, 0\n"
        "  s_mov_b32 s85, s84\n"
        "  s_lshl_b32 s88, s96, 7\n"
        "  ds_read_b64_tr_b16 a[18:19], v162 offset:3072\n"
        "  v_mfma_f32_16x16x32_bf16 v[64:67], a[64:67], a[56:59], v[64:67]\n"
        "  s_add_u32 s88, s88, s97\n"
        "  s_lshl_b32 s95, s89, 5\n"
        "  s_add_u32 s88, s88, s95\n"
        "  ds_read_b128 a[0:3], v160 offset:0\n"
        "  v_mfma_f32_16x16x32_bf16 v[96:99], a[64:67], a[60:63], v[96:99]\n"
        "  s_add_u32 m0, s69, s87\n"
        "  ds_read_b128 a[4:7], v160 offset:2048\n"
        "  v_mfma_f32_16x16x32_bf16 v[128:131], a[64:67], a[128:131], v[128:131]\n"
        "  buffer_load_dwordx4 v156, s[8:11], s68 offen offset:0 lds\n"
        "  v_mfma_f32_16x16x32_bf16 v[4:7], a[68:71], a[48:51], v[4:7]\n"
        "  ds_read_b128 a[8:11], v160 offset:4096\n"
        "  v_mfma_f32_16x16x32_bf16 v[36:39], a[68:71], a[52:55], v[36:39]\n"
        "  ds_read_b128 a[12:15], v160 offset:6144\n"
        "  v_mfma_f32_16x16x32_bf16 v[68:71], a[68:71], a[56:59], v[68:71]\n"
        "  ds_read_b64_tr_b16 a[20:21], v163 offset:0\n"
        "  v_mfma_f32_16x16x32_bf16 v[100:103], a[68:71], a[60:63], v[100:103]\n"
        "  buffer_load_dwordx4 v157, s[8:11], s68 offen offset:1024 lds\n"
        "  v_cndmask_b32_e64 v186, v189, v188, s[76:77]\n"
        "  v_mfma_f32_16x16x32_bf16 v[132:135], a[68:71], a[128:131], v[132:135]\n"
        "  ds_read_b64_tr_b16 a[22:23], v163 offset:3072\n"
        "  v_add_u32 v186, 1, v186\n"
        "  v_mfma_f32_16x16x32_bf16 v[8:11], a[72:75], a[48:51], v[8:11]\n"
        "  ds_read_b64_tr_b16 a[24:25], v164 offset:0\n"
        "  v_mov_b32 v191, v186\n"
        "  v_mfma_f32_16x16x32_bf16 v[40:43], a[72:75], a[52:55], v[40:43]\n"
        "  ds_read_b64_tr_b16 a[26:27], v164 offset:3072\n"
        "  s_mov_b64 exec, 15\n"
        "  buffer_store_dword v186, v185, s[24:27], 0 offen sc1\n"
        "  s_mov_b64 exec, -1\n"
        "  v_mfma_f32_16x16x32_bf16 v[72:75], a[72:75], a[56:59], v[72:75]\n"
        "  buffer_load_dwordx4 v158, s[8:11], s68 offen offset:2048 lds\n"
        "  v_lshlrev_b32 v208, 16, v192\n"
        "  v_mfma_f32_16x16x32_bf16 v[104:107], a[72:75], a[60:63], v[104:107]\n"
        "  s_add_u32 m0, s59, 49152\n"
        "  ds_read_b64_tr_b16 a[28:29], v165 offset:0\n"
        "  v_mfma_f32_16x16x32_bf16 v[136:139], a[72:75], a[128:131], v[136:139]\n"
        "  ds_read_b64_tr_b16 a[30:31], v165 offset:3072\n"
        "  v_and_b32 v209, 0xffff0000, v192\n"
        "  v_mfma_f32_16x16x32_bf16 v[12:15], a[76:79], a[48:51], v[12:15]\n"
        "  ds_read_b64_tr_b16 a[32:33], v166 offset:0\n"
        "  v_lshlrev_b32 v210, 16, v193\n"
        "  v_mfma_f32_16x16x32_bf16 v[44:47], a[76:79], a[52:55], v[44:47]\n"
        "  ds_read_b64_tr_b16 a[34:35], v166 offset:3072\n"
        "  v_and_b32 v211, 0xffff0000, v193\n"
        "  v_mfma_f32_16x16x32_bf16 v[76:79], a[76:79], a[56:59], v[76:79]\n"
        "  buffer_load_dwordx4 v172, s[16:19], s60 offen offset:0 lds\n"
        "  v_add_u32 v212, 147456, v182\n"
        "  v_mfma_f32_16x16x32_bf16 v[108:111], a[76:79], a[60:63], v[108:111]\n"
        "  ds_read_b64_tr_b16 a[36:37], v167 offset:0\n"
        "  s_mov_b32 exec_hi, 0xffffff\n"
        "  ds_write_b128 v212, v[208:211]\n"
        "  s_mov_b32 exec_hi, -1\n"
        "  v_mfma_f32_16x16x32_bf16 v[140:143], a[76:79], a[128:131], v[140:143]\n"
        "  ds_read_b64_tr_b16 a[38:39], v167 offset:3072\n"
        "  v_mfma_f32_16x16x32_bf16 v[16:19], a[80:83], a[48:51], v[16:19]\n"
        "  ds_read_b64_tr_b16 a[40:41], v168 offset:0\n"
        "  v_mfma_f32_16x16x32_bf16 v[48:51], a[80:83], a[52:55], v[48:51]\n"
        "  buffer_load_dwordx4 v173, s[16:19], s60 offen offset:1024 lds\n"
        "  v_mfma_f32_16x16x32_bf16 v[80:83], a[80:83], a[56:59], v[80:83]\n"
        "  ds_read_b64_tr_b16 a[42:43], v168 offset:3072\n"
        "  v_mfma_f32_16x16x32_bf16 v[112:115], a[80:83], a[60:63], v[112:115]\n"
        "  ds_read_b64_tr_b16 a[44:45], v169 offset:0\n"
        "  v_mfma_f32_16x16x32_bf16 v[20:23], a[84:87], a[48:51], v[20:23]\n"
        "  ds_read_b64_tr_b16 a[46:47], v169 offset:3072\n"
        "  v_mfma_f32_16x16x32_bf16 v[52:55], a[84:87], a[52:55], v[52:55]\n"
        "  ds_read_b64_tr_b16 a[64:65], v162 offset:14336\n"
        "  v_mfma_f32_16x16x32_bf16 v[84:87], a[84:87], a[56:59], v[84:87]\n"
        "  buffer_load_dwordx4 v174, s[16:19], s60 offen offset:2048 lds\n"
        "  v_mfma_f32_16x16x32_bf16 v[116:119], a[84:87], a[60:63], v[116:119]\n"
        "  ds_read_b64_tr_b16 a[66:67], v162 offset:17408\n"
        "  v_mfma_f32_16x16x32_bf16 v[24:27], a[88:91], a[48:51], v[24:27]\n"
        "  ds_read_b64_tr_b16 a[68:69], v163 offset:14336\n"
        "  v_mfma_f32_16x16x32_bf16 v[56:59], a[88:91], a[52:55], v[56:59]\n"
        "  ds_read_b64_tr_b16 a[70:71], v163 offset:17408\n"
        "  v_mfma_f32_16x16x32_bf16 v[88:91], a[88:91], a[56:59], v[88:91]\n"
        "  buffer_load_dwordx4 v175, s[16:19], s60 offen offset:3072 lds\n"
        "  v_mfma_f32_16x16x32_bf16 v[120:123], a[88:91], a[60:63], v[120:123]\n"
        "  s_add_u32 s60, s60, 28672\n"
        "  ds_read_b64_tr_b16 a[72:73], v164 offset:14336\n"
        "  s_add_u32 s55, s54, 49152\n"
        "  v_mfma_f32_16x16x32_bf16 v[28:31], a[92:95], a[48:51], v[28:31]\n"
        "  ds_read_b64_tr_b16 a[74:75], v164 offset:17408\n"
        "  v_mfma_f32_16x16x32_bf16 v[60:63], a[92:95], a[52:55], v[60:63]\n"
        "  s_add_u32 m0, s59, 53248\n"
        "  ds_read_b64_tr_b16 a[76:77], v165 offset:14336\n"
        "  s_cmp_eq_u32 s54, 98304\n"
        "  s_cselect_b32 s55, 0, s55\n"
        "  v_mfma_f32_16x16x32_bf16 v[92:95], a[92:95], a[56:59], v[92:95]\n"
        "  ds_read_b64_tr_b16 a[78:79], v165 offset:17408\n"
        "  s_sub_u32 s52, s55, s54\n"
        "  v_mfma_f32_16x16x32_bf16 v[124:127], a[92:95], a[60:63], v[124:127]\n"
        "  buffer_load_dwordx4 v172, s[16:19], s60 offen offset:0 lds\n"
        "  s_waitcnt lgkmcnt(8)\n"
        "  v_mfma_f32_16x16x32_bf16 v[0:3], a[16:19], a[0:3], v[0:3]\n"
        "  ds_read_b64_tr_b16 a[80:81], v166 offset:14336\n"
        "  v_mfma_f32_16x16x32_bf16 v[32:35], a[16:19], a[4:7], v[32:35]\n"
        "  ds_read_b64_tr_b16 a[82:83], v166 offset:17408\n"
        "  v_mfma_f32_16x16x32_bf16 v[64:67], a[16:19], a[8:11], v[64:67]\n"
        "  ds_read_b64_tr_b16 a[84:85], v167 offset:14336\n"
        "  v_mfma_f32_16x16x32_bf16 v[96:99], a[16:19], a[12:15], v[96:99]\n"
        "  buffer_load_dwordx4 v173, s[16:19], s60 offen offset:1024 lds\n"
        "  v_mfma_f32_16x16x32_bf16 v[128:131], a[16:19], a[128:131], v[128:131]\n"
        "  ds_read_b64_tr_b16 a[86:87], v167 offset:17408\n"
        "  v_mfma_f32_16x16x32_bf16 v[4:7], a[20:23], a[0:3], v[4:7]\n"
        "  ds_read_b64_tr_b16 a[88:89], v168 offset:14336\n"
        "  v_mfma_f32_16x16x32_bf16 v[36:39], a[20:23], a[4:7], v[36:39]\n"
        "  ds_read_b64_tr_b16 a[90:91], v168 offset:17408\n"
        "  v_mfma_f32_16x16x32_bf16 v[68:71], a[20:23], a[8:11], v[68:71]\n"
        "  buffer_load_dwordx4 v174, s[16:19], s60 offen offset:2048 lds\n"
        "  v_mfma_f32_16x16x32_bf16 v[100:103], a[20:23], a[12:15], v[100:103]\n"
        "  ds_read_b128 a[48:51], v161 offset:0\n"
        "  v_mfma_f32_16x16x32_bf16 v[132:135], a[20:23], a[128:131], v[132:135]\n"
        "  ds_read_b128 a[52:55], v161 offset:2048\n"
        "  v_mfma_f32_16x16x32_bf16 v[8:11], a[24:27], a[0:3], v[8:11]\n"
        "  ds_read_b128 a[56:59], v161 offset:4096\n"
        "  v_mfma_f32_16x16x32_bf16 v[40:43], a[24:27], a[4:7], v[40:43]\n"
        "  ds_read_b128 a[60:63], v161 offset:6144\n"
        "  v_mfma_f32_16x16x32_bf16 v[72:75], a[24:27], a[8:11], v[72:75]\n"
        "  buffer_load_dwordx4 v175, s[16:19], s60 offen offset:3072 lds\n"
        "  v_mfma_f32_16x16x32_bf16 v[104:107], a[24:27], a[12:15], v[104:107]\n"
        "  s_add_u32 s60, s60, 28672\n"
        "  ds_read_b64_tr_b16 a[92:93], v169 offset:14336\n"
        "  v_mfma_f32_16x16x32_bf16 v[136:139], a[24:27], a[128:131], v[136:139]\n"
        "  s_add_u32 m0, s59, 57344\n"
        "  ds_read_b64_tr_b16 a[94:95], v169 offset:17408\n"
        "  v_add_u32 v160, s52, v160\n"
        "  v_mfma_f32_16x16x32_bf16 v[12:15], a[28:31], a[0:3], v[12:15]\n"
        "  v_add_u32 v161, s52, v161\n"
        "  v_add_u32 v162, s52, v162\n"
        "  v_add_u32 v163, s52, v163\n"
        "  v_mfma_f32_16x16x32_bf16 v[44:47], a[28:31], a[4:7], v[44:47]\n"
        "  buffer_load_dwordx4 v172, s[16:19], s60 offen offset:0 lds\n"
        "  v_add_u32 v164, s52, v164\n"
        "  v_add_u32 v165, s52, v165\n"
        "  v_mfma_f32_16x16x32_bf16 v[76:79], a[28:31], a[8:11], v[76:79]\n"
        "  v_add_u32 v166, s52, v166\n"
        "  v_add_u32 v167, s52, v167\n"
        "  v_add_u32 v168, s52, v168\n"
        "  v_mfma_f32_16x16x32_bf16 v[108:111], a[28:31], a[12:15], v[108:111]\n"
        "  v_add_u32 v169, s52, v169\n"
        "  v_mfma_f32_16x16x32_bf16 v[140:143], a[28:31], a[128:131], v[140:143]\n"
        "  v_mfma_f32_16x16x32_bf16 v[16:19], a[32:35], a[0:3], v[16:19]\n"
        "  v_mfma_f32_16x16x32_bf16 v[48:51], a[32:35], a[4:7], v[48:51]\n"
        "  buffer_load_dwordx4 v173, s[16:19], s60 offen offset:1024 lds\n"
        "  v_mfma_f32_16x16x32_bf16 v[80:83], a[32:35], a[8:11], v[80:83]\n"
        "  v_mfma_f32_16x16x32_bf16 v[112:115], a[32:35], a[12:15], v[112:115]\n"
        "  v_mfma_f32_16x16x32_bf16 v[20:23], a[36:39], a[0:3], v[20:23]\n"
        "  v_mfma_f32_16x16x32_bf16 v[52:55], a[36:39], a[4:7], v[52:55]\n"
        "  buffer_load_dwordx4 v174, s[16:19], s60 offen offset:2048 lds\n"
        "  v_mfma_f32_16x16x32_bf16 v[84:87], a[36:39], a[8:11], v[84:87]\n"
        "  v_mfma_f32_16x16x32_bf16 v[116:119], a[36:39], a[12:15], v[116:119]\n"
        "  v_mfma_f32_16x16x32_bf16 v[24:27], a[40:43], a[0:3], v[24:27]\n"
        "  v_mfma_f32_16x16x32_bf16 v[56:59], a[40:43], a[4:7], v[56:59]\n"
        "  v_mfma_f32_16x16x32_bf16 v[88:91], a[40:43], a[8:11], v[88:91]\n"
        "  buffer_load_dwordx4 v175, s[16:19], s60 offen offset:3072 lds\n"
        "  s_add_u32 s60, s60, 28672\n"
        "  v_mfma_f32_16x16x32_bf16 v[120:123], a[40:43], a[12:15], v[120:123]\n"
        "  v_mfma_f32_16x16x32_bf16 v[28:31], a[44:47], a[0:3], v[28:31]\n"
        "  v_mfma_f32_16x16x32_bf16 v[60:63], a[44:47], a[4:7], v[60:63]\n"
        "  s_mov_b32 s51, s54\n"
        "  s_mov_b32 s54, s55\n"
        "  v_mfma_f32_16x16x32_bf16 v[92:95], a[44:47], a[8:11], v[92:95]\n"
        "  v_mfma_f32_16x16x32_bf16 v[124:127], a[44:47], a[12:15], v[124:127]\n"
        "  s_waitcnt vmcnt(12) lgkmcnt(0)\n"
        "  s_barrier\n"
        "  v_mfma_f32_16x16x32_bf16 v[0:3], a[64:67], a[48:51], v[0:3]\n"
        "  s_add_u32 m0, s59, 98304\n"
        "  ds_read_b64_tr_b16 a[16:17], v162 offset:0\n"
        "  v_mfma_f32_16x16x32_bf16 v[32:35], a[64:67], a[52:55], v[32:35]\n"
        "  buffer_load_dwordx4 v172, s[16:19], s60 offen offset:0 lds\n"
        "  v_mfma_f32_16x16x32_bf16 v[64:67], a[64:67], a[56:59], v[64:67]\n"
        "  ds_read_b64_tr_b16 a[18:19], v162 offset:3072\n"
        "  v_mfma_f32_16x16x32_bf16 v[96:99], a[64:67], a[60:63], v[96:99]\n"
        "  ds_read_b128 a[0:3], v160 offset:0\n"
        "  v_mfma_f32_16x16x32_bf16 v[128:131], a[64:67], a[128:131], v[128:131]\n"
        "  ds_read_b128 a[4:7], v160 offset:2048\n"
        "  v_mfma_f32_16x16x32_bf16 v[4:7], a[68:71], a[48:51], v[4:7]\n"
        "  buffer_load_dwordx4 v173, s[16:19], s60 offen offset:1024 lds\n"
        "  v_mfma_f32_16x16x32_bf16 v[36:39], a[68:71], a[52:55], v[36:39]\n"
        "  ds_read_b128 a[8:11], v160 offset:4096\n"
        "  v_mfma_f32_16x16x32_bf16 v[68:71], a[68:71], a[56:59], v[68:71]\n"
        "  ds_read_b128 a[12:15], v160 offset:6144\n"
        "  v_mfma_f32_16x16x32_bf16 v[100:103], a[68:71], a[60:63], v[100:103]\n"
        "  ds_read_b64_tr_b16 a[20:21], v163 offset:0\n"
        "  v_mfma_f32_16x16x32_bf16 v[132:135], a[68:71], a[128:131], v[132:135]\n"
        "  ds_read_b64_tr_b16 a[22:23], v163 offset:3072\n"
        "  v_mfma_f32_16x16x32_bf16 v[8:11], a[72:75], a[48:51], v[8:11]\n"
        "  buffer_load_dwordx4 v174, s[16:19], s60 offen offset:2048 lds\n"
        "  v_mfma_f32_16x16x32_bf16 v[40:43], a[72:75], a[52:55], v[40:43]\n"
        "  ds_read_b64_tr_b16 a[24:25], v164 offset:0\n"
        "  v_mfma_f32_16x16x32_bf16 v[72:75], a[72:75], a[56:59], v[72:75]\n"
        "  ds_read_b64_tr_b16 a[26:27], v164 offset:3072\n"
        "  v_mfma_f32_16x16x32_bf16 v[104:107], a[72:75], a[60:63], v[104:107]\n"
        "  ds_read_b64_tr_b16 a[28:29], v165 offset:0\n"
        "  v_mfma_f32_16x16x32_bf16 v[136:139], a[72:75], a[128:131], v[136:139]\n"
        "  ds_read_b64_tr_b16 a[30:31], v165 offset:3072\n"
        "  v_mfma_f32_16x16x32_bf16 v[12:15], a[76:79], a[48:51], v[12:15]\n"
        "  buffer_load_dwordx4 v175, s[16:19], s60 offen offset:3072 lds\n"
        "  v_mfma_f32_16x16x32_bf16 v[44:47], a[76:79], a[52:55], v[44:47]\n"
        "  s_add_u32 s60, s60, 28672\n"
        "  ds_read_b64_tr_b16 a[32:33], v166 offset:0\n"
        "  v_mfma_f32_16x16x32_bf16 v[76:79], a[76:79], a[56:59], v[76:79]\n"
        "  ds_read_b64_tr_b16 a[34:35], v166 offset:3072\n"
        "  v_mfma_f32_16x16x32_bf16 v[108:111], a[76:79], a[60:63], v[108:111]\n"
        "  s_add_u32 m0, s59, 102400\n"
        "  ds_read_b64_tr_b16 a[36:37], v167 offset:0\n"
        "  v_mfma_f32_16x16x32_bf16 v[140:143], a[76:79], a[128:131], v[140:143]\n"
        "  ds_read_b64_tr_b16 a[38:39], v167 offset:3072\n"
        "  v_mfma_f32_16x16x32_bf16 v[16:19], a[80:83], a[48:51], v[16:19]\n"
        "  buffer_load_dwordx4 v172, s[16:19], s60 offen offset:0 lds\n"
        "  v_mfma_f32_16x16x32_bf16 v[48:51], a[80:83], a[52:55], v[48:51]\n"
        "  ds_read_b64_tr_b16 a[40:41], v168 offset:0\n"
        "  v_mfma_f32_16x16x32_bf16 v[80:83], a[80:83], a[56:59], v[80:83]\n"
        "  ds_read_b64_tr_b16 a[42:43], v168 offset:3072\n"
        "  v_mfma_f32_16x16x32_bf16 v[112:115], a[80:83], a[60:63], v[112:115]\n"
        "  ds_read_b64_tr_b16 a[44:45], v169 offset:0\n"
        "  v_mfma_f32_16x16x32_bf16 v[20:23], a[84:87], a[48:51], v[20:23]\n"
        "  ds_read_b64_tr_b16 a[46:47], v169 offset:3072\n"
        "  v_mfma_f32_16x16x32_bf16 v[52:55], a[84:87], a[52:55], v[52:55]\n"
        "  buffer_load_dwordx4 v173, s[16:19], s60 offen offset:1024 lds\n"
        "  v_mfma_f32_16x16x32_bf16 v[84:87], a[84:87], a[56:59], v[84:87]\n"
        "  ds_read_b64_tr_b16 a[64:65], v162 offset:14336\n"
        "  v_mfma_f32_16x16x32_bf16 v[116:119], a[84:87], a[60:63], v[116:119]\n"
        "  ds_read_b64_tr_b16 a[66:67], v162 offset:17408\n"
        "  v_mfma_f32_16x16x32_bf16 v[24:27], a[88:91], a[48:51], v[24:27]\n"
        "  ds_read_b64_tr_b16 a[68:69], v163 offset:14336\n"
        "  v_mfma_f32_16x16x32_bf16 v[56:59], a[88:91], a[52:55], v[56:59]\n"
        "  buffer_load_dwordx4 v174, s[16:19], s60 offen offset:2048 lds\n"
        "  v_mfma_f32_16x16x32_bf16 v[88:91], a[88:91], a[56:59], v[88:91]\n"
        "  ds_read_b64_tr_b16 a[70:71], v163 offset:17408\n"
        "  v_mfma_f32_16x16x32_bf16 v[120:123], a[88:91], a[60:63], v[120:123]\n"
        "  ds_read_b64_tr_b16 a[72:73], v164 offset:14336\n"
        "  s_add_u32 s55, s54, 49152\n"
        "  v_mfma_f32_16x16x32_bf16 v[28:31], a[92:95], a[48:51], v[28:31]\n"
        "  ds_read_b64_tr_b16 a[74:75], v164 offset:17408\n"
        "  v_mfma_f32_16x16x32_bf16 v[60:63], a[92:95], a[52:55], v[60:63]\n"
        "  ds_read_b64_tr_b16 a[76:77], v165 offset:14336\n"
        "  s_cmp_eq_u32 s54, 98304\n"
        "  s_cselect_b32 s55, 0, s55\n"
        "  v_mfma_f32_16x16x32_bf16 v[92:95], a[92:95], a[56:59], v[92:95]\n"
        "  buffer_load_dwordx4 v175, s[16:19], s60 offen offset:3072 lds\n"
        "  s_sub_u32 s52, s55, s54\n"
        "  v_mfma_f32_16x16x32_bf16 v[124:127], a[92:95], a[60:63], v[124:127]\n"
        "  s_add_u32 s60, s60, 28672\n"
        "  ds_read_b64_tr_b16 a[78:79], v165 offset:17408\n"
        "  s_waitcnt lgkmcnt(8)\n"
        "  v_mfma_f32_16x16x32_bf16 v[128:131], a[16:19], a[128:131], v[128:131]\n"
        "  ds_read_b64_tr_b16 a[80:81], v166 offset:14336\n"
        "  v_mfma_f32_16x16x32_bf16 v[132:135], a[20:23], a[128:131], v[132:135]\n"
        "  s_add_u32 m0, s59, 106496\n"
        "  ds_read_b64_tr_b16 a[82:83], v166 offset:17408\n"
        "  v_mfma_f32_16x16x32_bf16 v[136:139], a[24:27], a[128:131], v[136:139]\n"
        "  ds_read_b64_tr_b16 a[84:85], v167 offset:14336\n"
        "  v_mfma_f32_16x16x32_bf16 v[140:143], a[28:31], a[128:131], v[140:143]\n"
        "  buffer_load_dwordx4 v172, s[16:19], s60 offen offset:0 lds\n"
        "  v_mfma_f32_16x16x32_bf16 v[0:3], a[16:19], a[0:3], v[0:3]\n"
        "  ds_read_b64_tr_b16 a[86:87], v167 offset:17408\n"
        "  v_mfma_f32_16x16x32_bf16 v[32:35], a[16:19], a[4:7], v[32:35]\n"
        "  ds_read_b64_tr_b16 a[88:89], v168 offset:14336\n"
        "  v_mfma_f32_16x16x32_bf16 v[64:67], a[16:19], a[8:11], v[64:67]\n"
        "  ds_read_b64_tr_b16 a[90:91], v168 offset:17408\n"
        "  v_mfma_f32_16x16x32_bf16 v[96:99], a[16:19], a[12:15], v[96:99]\n"
        "  ds_read_b128 a[48:51], v161 offset:0\n"
        "  v_mfma_f32_16x16x32_bf16 v[4:7], a[20:23], a[0:3], v[4:7]\n"
        "  buffer_load_dwordx4 v173, s[16:19], s60 offen offset:1024 lds\n"
        "  v_mfma_f32_16x16x32_bf16 v[36:39], a[20:23], a[4:7], v[36:39]\n"
        "  ds_read_b128 a[52:55], v161 offset:2048\n"
        "  v_mfma_f32_16x16x32_bf16 v[68:71], a[20:23], a[8:11], v[68:71]\n"
        "  ds_read_b128 a[56:59], v161 offset:4096\n"
        "  v_mfma_f32_16x16x32_bf16 v[100:103], a[20:23], a[12:15], v[100:103]\n"
        "  ds_read_b128 a[60:63], v161 offset:6144\n"
        "  v_mfma_f32_16x16x32_bf16 v[8:11], a[24:27], a[0:3], v[8:11]\n"
        "  ds_read_b64_tr_b16 a[92:93], v169 offset:14336\n"
        "  v_mfma_f32_16x16x32_bf16 v[40:43], a[24:27], a[4:7], v[40:43]\n"
        "  buffer_load_dwordx4 v174, s[16:19], s60 offen offset:2048 lds\n"
        "  v_mfma_f32_16x16x32_bf16 v[72:75], a[24:27], a[8:11], v[72:75]\n"
        "  ds_read_b64_tr_b16 a[94:95], v169 offset:17408\n"
        "  v_mfma_f32_16x16x32_bf16 v[104:107], a[24:27], a[12:15], v[104:107]\n"
        "  v_mfma_f32_16x16x32_bf16 v[12:15], a[28:31], a[0:3], v[12:15]\n"
        "  v_mfma_f32_16x16x32_bf16 v[44:47], a[28:31], a[4:7], v[44:47]\n"
        "  v_mfma_f32_16x16x32_bf16 v[76:79], a[28:31], a[8:11], v[76:79]\n"
        "  buffer_load_dwordx4 v175, s[16:19], s60 offen offset:3072 lds\n"
        "  s_add_u32 s60, s60, 28672\n"
        "  v_mfma_f32_16x16x32_bf16 v[108:111], a[28:31], a[12:15], v[108:111]\n"
        "  v_mfma_f32_16x16x32_bf16 v[16:19], a[32:35], a[0:3], v[16:19]\n"
        "  v_mfma_f32_16x16x32_bf16 v[48:51], a[32:35], a[4:7], v[48:51]\n"
        "  s_mov_b32 s51, s54\n"
        "  s_mov_b32 s54, s55\n"
        "  v_mfma_f32_16x16x32_bf16 v[80:83], a[32:35], a[8:11], v[80:83]\n"
        "  v_mfma_f32_16x16x32_bf16 v[112:115], a[32:35], a[12:15], v[112:115]\n"
        "  s_waitcnt lgkmcnt(0)\n"
        "  s_barrier\n"
        "  v_mfma_f32_16x16x32_bf16 v[128:131], a[64:67], a[128:131], v[128:131]\n"
        "  v_mfma_f32_16x16x32_bf16 v[132:135], a[68:71], a[128:131], v[132:135]\n"
        "  s_mov_b64 exec, s[78:79]\n"
        "  v_mov_b32 v188, 0\n"
        "  v_mov_b32 v189, 0\n"
        "  buffer_atomic_or_x2 v[188:189], v184, s[24:27], 0 offen sc0 sc1\n"
        "  s_mov_b64 exec, -1\n"
        "  v_mfma_f32_16x16x32_bf16 v[136:139], a[72:75], a[128:131], v[136:139]\n"
        "  v_mfma_f32_16x16x32_bf16 v[140:143], a[76:79], a[128:131], v[140:143]\n"
        "  s_mul_i32 s95, s88, 896\n"
        "  s_add_u32 s95, s95, s63\n"
        "  s_lshl_b32 s94, s40, 11\n"
        "  s_add_u32 m0, s94, 155648\n"
        "  s_lshl_b32 s94, s40, 7\n"
        "  buffer_load_dwordx4 v252, s[4:7], s95 offen offset:0 lds\n"
        "  v_mfma_f32_16x16x32_bf16 v[20:23], a[36:39], a[0:3], v[20:23]\n"
        "  v_mfma_f32_16x16x32_bf16 v[52:55], a[36:39], a[4:7], v[52:55]\n"
        "  v_mfma_f32_16x16x32_bf16 v[84:87], a[36:39], a[8:11], v[84:87]\n"
        "  buffer_load_dwordx4 v253, s[4:7], s95 offen offset:1024 lds\n"
        "  v_mfma_f32_16x16x32_bf16 v[116:119], a[36:39], a[12:15], v[116:119]\n"
        "  v_mfma_f32_16x16x32_bf16 v[24:27], a[40:43], a[0:3], v[24:27]\n"
        "  v_mfma_f32_16x16x32_bf16 v[56:59], a[40:43], a[4:7], v[56:59]\n"
        "  s_add_u32 m0, s94, 149504\n"
        "  s_mov_b64 exec, 0xff\n"
        "  buffer_load_dwordx4 v182, s[36:39], s63 offen offset:0 lds\n"
        "  s_mov_b64 exec, -1\n"
        "  v_mfma_f32_16x16x32_bf16 v[88:91], a[40:43], a[8:11], v[88:91]\n"
        "  s_mov_b32 exec_lo, 0x10001\n"
        "  s_mov_b32 exec_hi, 0x10001\n"
        "  ds_write_b128 v197, v[128:131] offset:0\n"
        "  ds_write_b128 v197, v[132:135] offset:64\n"
        "  ds_write_b128 v197, v[136:139] offset:128\n"
        "  ds_write_b128 v197, v[140:143] offset:192\n"
        "  s_mov_b64 exec, -1\n"
        "  v_mfma_f32_16x16x32_bf16 v[120:123], a[40:43], a[12:15], v[120:123]\n"
        "  v_mfma_f32_16x16x32_bf16 v[28:31], a[44:47], a[0:3], v[28:31]\n"
        "  s_add_u32 m0, s59, 0\n"
        "  s_nop 0  ; hazard\n"
        "  buffer_load_dwordx4 v172, s[16:19], s60 offen offset:0 lds\n"
        "  v_mfma_f32_16x16x32_bf16 v[60:63], a[44:47], a[4:7], v[60:63]\n"
        "  v_mfma_f32_16x16x32_bf16 v[92:95], a[44:47], a[8:11], v[92:95]\n"
        "  v_mfma_f32_16x16x32_bf16 v[124:127], a[44:47], a[12:15], v[124:127]\n"
        "  buffer_load_dwordx4 v173, s[16:19], s60 offen offset:1024 lds\n"
        "  v_mfma_f32_16x16x32_bf16 v[0:3], a[64:67], a[48:51], v[0:3]\n"
        "  v_mfma_f32_16x16x32_bf16 v[32:35], a[64:67], a[52:55], v[32:35]\n"
        "  buffer_load_dwordx4 v174, s[16:19], s60 offen offset:2048 lds\n"
        "  v_mfma_f32_16x16x32_bf16 v[64:67], a[64:67], a[56:59], v[64:67]\n"
        "  v_mfma_f32_16x16x32_bf16 v[96:99], a[64:67], a[60:63], v[96:99]\n"
        "  v_mfma_f32_16x16x32_bf16 v[4:7], a[68:71], a[48:51], v[4:7]\n"
        "  buffer_load_dwordx4 v175, s[16:19], s60 offen offset:3072 lds\n"
        "  v_mfma_f32_16x16x32_bf16 v[36:39], a[68:71], a[52:55], v[36:39]\n"
        "  v_mfma_f32_16x16x32_bf16 v[68:71], a[68:71], a[56:59], v[68:71]\n"
        "  v_mfma_f32_16x16x32_bf16 v[100:103], a[68:71], a[60:63], v[100:103]\n"
        "  s_add_u32 s60, s60, 28672\n"
        "  s_add_u32 m0, s59, 4096\n"
        "  s_nop 0  ; hazard\n"
        "  buffer_load_dwordx4 v172, s[16:19], s60 offen offset:0 lds\n"
        "  v_mfma_f32_16x16x32_bf16 v[8:11], a[72:75], a[48:51], v[8:11]\n"
        "  v_mfma_f32_16x16x32_bf16 v[40:43], a[72:75], a[52:55], v[40:43]\n"
        "  v_mfma_f32_16x16x32_bf16 v[72:75], a[72:75], a[56:59], v[72:75]\n"
        "  buffer_load_dwordx4 v173, s[16:19], s60 offen offset:1024 lds\n"
        "  v_mfma_f32_16x16x32_bf16 v[104:107], a[72:75], a[60:63], v[104:107]\n"
        "  v_mfma_f32_16x16x32_bf16 v[12:15], a[76:79], a[48:51], v[12:15]\n"
        "  v_mfma_f32_16x16x32_bf16 v[44:47], a[76:79], a[52:55], v[44:47]\n"
        "  buffer_load_dwordx4 v174, s[16:19], s60 offen offset:2048 lds\n"
        "  v_mfma_f32_16x16x32_bf16 v[76:79], a[76:79], a[56:59], v[76:79]\n"
        "  v_mfma_f32_16x16x32_bf16 v[108:111], a[76:79], a[60:63], v[108:111]\n"
        "  buffer_load_dwordx4 v175, s[16:19], s60 offen offset:3072 lds\n"
        "  v_mfma_f32_16x16x32_bf16 v[16:19], a[80:83], a[48:51], v[16:19]\n"
        "  v_mfma_f32_16x16x32_bf16 v[48:51], a[80:83], a[52:55], v[48:51]\n"
        "  v_mfma_f32_16x16x32_bf16 v[80:83], a[80:83], a[56:59], v[80:83]\n"
        "  s_add_u32 s60, s60, 28672\n"
        "  s_add_u32 m0, s59, 8192\n"
        "  s_nop 0  ; hazard\n"
        "  buffer_load_dwordx4 v172, s[16:19], s60 offen offset:0 lds\n"
        "  v_mfma_f32_16x16x32_bf16 v[112:115], a[80:83], a[60:63], v[112:115]\n"
        "  v_mfma_f32_16x16x32_bf16 v[20:23], a[84:87], a[48:51], v[20:23]\n"
        "  v_mfma_f32_16x16x32_bf16 v[52:55], a[84:87], a[52:55], v[52:55]\n"
        "  buffer_load_dwordx4 v173, s[16:19], s60 offen offset:1024 lds\n"
        "  v_mfma_f32_16x16x32_bf16 v[84:87], a[84:87], a[56:59], v[84:87]\n"
        "  v_mfma_f32_16x16x32_bf16 v[116:119], a[84:87], a[60:63], v[116:119]\n"
        "  v_mfma_f32_16x16x32_bf16 v[24:27], a[88:91], a[48:51], v[24:27]\n"
        "  buffer_load_dwordx4 v174, s[16:19], s60 offen offset:2048 lds\n"
        "  v_mfma_f32_16x16x32_bf16 v[56:59], a[88:91], a[52:55], v[56:59]\n"
        "  v_mfma_f32_16x16x32_bf16 v[88:91], a[88:91], a[56:59], v[88:91]\n"
        "  v_mfma_f32_16x16x32_bf16 v[120:123], a[88:91], a[60:63], v[120:123]\n"
        "  buffer_load_dwordx4 v175, s[16:19], s60 offen offset:3072 lds\n"
        "  s_add_u32 s60, s60, 28672\n"
        "  v_mfma_f32_16x16x32_bf16 v[28:31], a[92:95], a[48:51], v[28:31]\n"
        "  v_mfma_f32_16x16x32_bf16 v[60:63], a[92:95], a[52:55], v[60:63]\n"
        "  v_mfma_f32_16x16x32_bf16 v[92:95], a[92:95], a[56:59], v[92:95]\n"
        "  v_mfma_f32_16x16x32_bf16 v[124:127], a[92:95], a[60:63], v[124:127]\n"
        "  s_waitcnt lgkmcnt(0)\n"
        "  s_barrier\n"
        "  ds_read2_b32 v[232:233], v207 offset0:0 offset1:128\n"
        "  ds_read2_b32 v[234:235], v207 offset0:16 offset1:144\n"
        "  ds_read_b128 v[144:147], v197 offset:0\n"
        "  ds_read_b128 v[152:155], v194 offset:0\n"
        "  ds_read_b128 v[148:151], v197 offset:64\n"
        "  ds_read_b128 v[156:159], v194 offset:64\n"
        "  ds_read2_b32 v[236:237], v207 offset0:32 offset1:160\n"
        "  ds_read2_b32 v[238:239], v207 offset0:48 offset1:176\n"
        "  ds_read_b128 v[216:219], v197 offset:128\n"
        "  ds_read_b128 v[224:227], v194 offset:128\n"
        "  ds_read_b128 v[220:223], v197 offset:192\n"
        "  ds_read_b128 v[228:231], v194 offset:192\n"
        "  s_add_u32 s94, s82, 0\n"
        "  s_waitcnt lgkmcnt(8)  ; auto\n"
        "  v_pk_fma_f32 v[208:209], v[232:233], v[144:145], v[152:153] op_sel:[1,0,0] op_sel_hi:[1,1,1]\n"
        "  v_pk_fma_f32 v[212:213], v[234:235], v[144:145], v[152:153] op_sel:[1,0,0] op_sel_hi:[1,1,1]\n"
        "  v_pk_fma_f32 v[210:211], v[232:233], v[146:147], v[154:155] op_sel:[1,0,0] op_sel_hi:[1,1,1]\n"
        "  v_pk_fma_f32 v[214:215], v[234:235], v[146:147], v[154:155] op_sel:[1,0,0] op_sel_hi:[1,1,1]\n"
        "  v_pk_fma_f32 v[0:1], v[232:233], v[0:1], v[208:209] op_sel:[0,0,0] op_sel_hi:[0,1,1]\n"
        "  v_pk_fma_f32 v[32:33], v[234:235], v[32:33], v[212:213] op_sel:[0,0,0] op_sel_hi:[0,1,1]\n"
        "  v_pk_fma_f32 v[2:3], v[232:233], v[2:3], v[210:211] op_sel:[0,0,0] op_sel_hi:[0,1,1]\n"
        "  v_pk_fma_f32 v[34:35], v[234:235], v[34:35], v[214:215] op_sel:[0,0,0] op_sel_hi:[0,1,1]\n"
        "  v_max_f32 v0, 0, v0\n"
        "  v_max_f32 v32, 0, v32\n"
        "  v_max_f32 v1, 0, v1\n"
        "  v_max_f32 v33, 0, v33\n"
        "  v_max_f32 v2, 0, v2\n"
        "  v_max_f32 v34, 0, v34\n"
        "  v_max_f32 v3, 0, v3\n"
        "  v_max_f32 v35, 0, v35\n"
        "  v_pk_mul_f32 v[0:1], v[0:1], v[0:1] op_sel:[0,0] op_sel_hi:[1,1]\n"
        "  v_pk_mul_f32 v[32:33], v[32:33], v[32:33] op_sel:[0,0] op_sel_hi:[1,1]\n"
        "  v_pk_mul_f32 v[2:3], v[2:3], v[2:3] op_sel:[0,0] op_sel_hi:[1,1]\n"
        "  v_pk_mul_f32 v[34:35], v[34:35], v[34:35] op_sel:[0,0] op_sel_hi:[1,1]\n"
        "  s_waitcnt lgkmcnt(6)  ; auto\n"
        "  v_pk_fma_f32 v[208:209], v[232:233], v[148:149], v[156:157] op_sel:[1,0,0] op_sel_hi:[1,1,1]\n"
        "  v_pk_fma_f32 v[212:213], v[234:235], v[148:149], v[156:157] op_sel:[1,0,0] op_sel_hi:[1,1,1]\n"
        "  v_pk_fma_f32 v[210:211], v[232:233], v[150:151], v[158:159] op_sel:[1,0,0] op_sel_hi:[1,1,1]\n"
        "  v_pk_fma_f32 v[214:215], v[234:235], v[150:151], v[158:159] op_sel:[1,0,0] op_sel_hi:[1,1,1]\n"
        "  v_pk_fma_f32 v[4:5], v[232:233], v[4:5], v[208:209] op_sel:[0,0,0] op_sel_hi:[0,1,1]\n"
        "  v_pk_fma_f32 v[36:37], v[234:235], v[36:37], v[212:213] op_sel:[0,0,0] op_sel_hi:[0,1,1]\n"
        "  v_pk_fma_f32 v[6:7], v[232:233], v[6:7], v[210:211] op_sel:[0,0,0] op_sel_hi:[0,1,1]\n"
        "  v_pk_fma_f32 v[38:39], v[234:235], v[38:39], v[214:215] op_sel:[0,0,0] op_sel_hi:[0,1,1]\n"
        "  v_max_f32 v4, 0, v4\n"
        "  v_max_f32 v36, 0, v36\n"
        "  v_max_f32 v5, 0, v5\n"
        "  v_max_f32 v37, 0, v37\n"
        "  v_max_f32 v6, 0, v6\n"
        "  v_max_f32 v38, 0, v38\n"
        "  v_max_f32 v7, 0, v7\n"
        "  v_max_f32 v39, 0, v39\n"
        "  v_pk_mul_f32 v[4:5], v[4:5], v[4:5] op_sel:[0,0] op_sel_hi:[1,1]\n"
        "  v_pk_mul_f32 v[36:37], v[36:37], v[36:37] op_sel:[0,0] op_sel_hi:[1,1]\n"
        "  v_pk_mul_f32 v[6:7], v[6:7], v[6:7] op_sel:[0,0] op_sel_hi:[1,1]\n"
        "  v_pk_mul_f32 v[38:39], v[38:39], v[38:39] op_sel:[0,0] op_sel_hi:[1,1]\n"
        "  v_cvt_pk_bf16_f32 v0, v0, v1\n"
        "  v_cvt_pk_bf16_f32 v32, v32, v33\n"
        "  v_cvt_pk_bf16_f32 v1, v2, v3\n"
        "  v_cvt_pk_bf16_f32 v33, v34, v35\n"
        "  v_cvt_pk_bf16_f32 v2, v4, v5\n"
        "  v_cvt_pk_bf16_f32 v34, v36, v37\n"
        "  v_cvt_pk_bf16_f32 v3, v6, v7\n"
        "  v_cvt_pk_bf16_f32 v35, v38, v39\n"
        "  buffer_store_dwordx4 v[0:3], v182, s[24:27], s94 offen offset:0 sc1\n"
        "  buffer_store_dwordx4 v[32:35], v182, s[24:27], s94 offen offset:1024 sc1\n"
        "  s_waitcnt lgkmcnt(5)  ; auto\n"
        "  v_pk_fma_f32 v[208:209], v[236:237], v[144:145], v[152:153] op_sel:[1,0,0] op_sel_hi:[1,1,1]\n"
        "  s_waitcnt lgkmcnt(4)  ; auto\n"
        "  v_pk_fma_f32 v[212:213], v[238:239], v[144:145], v[152:153] op_sel:[1,0,0] op_sel_hi:[1,1,1]\n"
        "  v_pk_fma_f32 v[210:211], v[236:237], v[146:147], v[154:155] op_sel:[1,0,0] op_sel_hi:[1,1,1]\n"
        "  v_pk_fma_f32 v[214:215], v[238:239], v[146:147], v[154:155] op_sel:[1,0,0] op_sel_hi:[1,1,1]\n"
        "  v_pk_fma_f32 v[64:65], v[236:237], v[64:65], v[208:209] op_sel:[0,0,0] op_sel_hi:[0,1,1]\n"
        "  v_pk_fma_f32 v[96:97], v[238:239], v[96:97], v[212:213] op_sel:[0,0,0] op_sel_hi:[0,1,1]\n"
        "  v_pk_fma_f32 v[66:67], v[236:237], v[66:67], v[210:211] op_sel:[0,0,0] op_sel_hi:[0,1,1]\n"
        "  v_pk_fma_f32 v[98:99], v[238:239], v[98:99], v[214:215] op_sel:[0,0,0] op_sel_hi:[0,1,1]\n"
        "  v_max_f32 v64, 0, v64\n"
        "  v_max_f32 v96, 0, v96\n"
        "  v_max_f32 v65, 0, v65\n"
        "  v_max_f32 v97, 0, v97\n"
        "  v_max_f32 v66, 0, v66\n"
        "  v_max_f32 v98, 0, v98\n"
        "  v_max_f32 v67, 0, v67\n"
        "  v_max_f32 v99, 0, v99\n"
        "  v_pk_mul_f32 v[64:65], v[64:65], v[64:65] op_sel:[0,0] op_sel_hi:[1,1]\n"
        "  v_pk_mul_f32 v[96:97], v[96:97], v[96:97] op_sel:[0,0] op_sel_hi:[1,1]\n"
        "  v_pk_mul_f32 v[66:67], v[66:67], v[66:67] op_sel:[0,0] op_sel_hi:[1,1]\n"
        "  v_pk_mul_f32 v[98:99], v[98:99], v[98:99] op_sel:[0,0] op_sel_hi:[1,1]\n"
        "  v_pk_fma_f32 v[208:209], v[236:237], v[148:149], v[156:157] op_sel:[1,0,0] op_sel_hi:[1,1,1]\n"
        "  v_pk_fma_f32 v[212:213], v[238:239], v[148:149], v[156:157] op_sel:[1,0,0] op_sel_hi:[1,1,1]\n"
        "  v_pk_fma_f32 v[210:211], v[236:237], v[150:151], v[158:159] op_sel:[1,0,0] op_sel_hi:[1,1,1]\n"
        "  v_pk_fma_f32 v[214:215], v[238:239], v[150:151], v[158:159] op_sel:[1,0,0] op_sel_hi:[1,1,1]\n"
        "  v_pk_fma_f32 v[68:69], v[236:237], v[68:69], v[208:209] op_sel:[0,0,0] op_sel_hi:[0,1,1]\n"
        "  v_pk_fma_f32 v[100:101], v[238:239], v[100:101], v[212:213] op_sel:[0,0,0] op_sel_hi:[0,1,1]\n"
        "  v_pk_fma_f32 v[70:71], v[236:237], v[70:71], v[210:211] op_sel:[0,0,0] op_sel_hi:[0,1,1]\n"
        "  v_pk_fma_f32 v[102:103], v[238:239], v[102:103], v[214:215] op_sel:[0,0,0] op_sel_hi:[0,1,1]\n"
        "  v_max_f32 v68, 0, v68\n"
        "  v_max_f32 v100, 0, v100\n"
        "  v_max_f32 v69, 0, v69\n"
        "  v_max_f32 v101, 0, v101\n"
        "  v_max_f32 v70, 0, v70\n"
        "  v_max_f32 v102, 0, v102\n"
        "  v_max_f32 v71, 0, v71\n"
        "  v_max_f32 v103, 0, v103\n"
        "  v_pk_mul_f32 v[68:69], v[68:69], v[68:69] op_sel:[0,0] op_sel_hi:[1,1]\n"
        "  v_pk_mul_f32 v[100:101], v[100:101], v[100:101] op_sel:[0,0] op_sel_hi:[1,1]\n"
        "  v_pk_mul_f32 v[70:71], v[70:71], v[70:71] op_sel:[0,0] op_sel_hi:[1,1]\n"
        "  v_pk_mul_f32 v[102:103], v[102:103], v[102:103] op_sel:[0,0] op_sel_hi:[1,1]\n"
        "  v_cvt_pk_bf16_f32 v64, v64, v65\n"
        "  v_cvt_pk_bf16_f32 v96, v96, v97\n"
        "  v_cvt_pk_bf16_f32 v65, v66, v67\n"
        "  v_cvt_pk_bf16_f32 v97, v98, v99\n"
        "  v_cvt_pk_bf16_f32 v66, v68, v69\n"
        "  v_cvt_pk_bf16_f32 v98, v100, v101\n"
        "  v_cvt_pk_bf16_f32 v67, v70, v71\n"
        "  v_cvt_pk_bf16_f32 v99, v102, v103\n"
        "  buffer_store_dwordx4 v[64:67], v182, s[24:27], s94 offen offset:2048 sc1\n"
        "  buffer_store_dwordx4 v[96:99], v182, s[24:27], s94 offen offset:3072 sc1\n"
        "  ds_read_b128 v[144:147], v198 offset:256\n"
        "  ds_read_b128 v[152:155], v195 offset:256\n"
        "  ds_read_b128 v[148:151], v198 offset:320\n"
        "  ds_read_b128 v[156:159], v195 offset:320\n"
        "  s_add_u32 s94, s82, 4096\n"
        "  s_waitcnt lgkmcnt(6)  ; auto\n"
        "  v_pk_fma_f32 v[208:209], v[232:233], v[216:217], v[224:225] op_sel:[1,0,0] op_sel_hi:[1,1,1]\n"
        "  v_pk_fma_f32 v[212:213], v[234:235], v[216:217], v[224:225] op_sel:[1,0,0] op_sel_hi:[1,1,1]\n"
        "  v_pk_fma_f32 v[210:211], v[232:233], v[218:219], v[226:227] op_sel:[1,0,0] op_sel_hi:[1,1,1]\n"
        "  v_pk_fma_f32 v[214:215], v[234:235], v[218:219], v[226:227] op_sel:[1,0,0] op_sel_hi:[1,1,1]\n"
        "  v_pk_fma_f32 v[8:9], v[232:233], v[8:9], v[208:209] op_sel:[0,0,0] op_sel_hi:[0,1,1]\n"
        "  v_pk_fma_f32 v[40:41], v[234:235], v[40:41], v[212:213] op_sel:[0,0,0] op_sel_hi:[0,1,1]\n"
        "  v_pk_fma_f32 v[10:11], v[232:233], v[10:11], v[210:211] op_sel:[0,0,0] op_sel_hi:[0,1,1]\n"
        "  v_pk_fma_f32 v[42:43], v[234:235], v[42:43], v[214:215] op_sel:[0,0,0] op_sel_hi:[0,1,1]\n"
        "  v_max_f32 v8, 0, v8\n"
        "  v_max_f32 v40, 0, v40\n"
        "  v_max_f32 v9, 0, v9\n"
        "  v_max_f32 v41, 0, v41\n"
        "  v_max_f32 v10, 0, v10\n"
        "  v_max_f32 v42, 0, v42\n"
        "  v_max_f32 v11, 0, v11\n"
        "  v_max_f32 v43, 0, v43\n"
        "  v_pk_mul_f32 v[8:9], v[8:9], v[8:9] op_sel:[0,0] op_sel_hi:[1,1]\n"
        "  v_pk_mul_f32 v[40:41], v[40:41], v[40:41] op_sel:[0,0] op_sel_hi:[1,1]\n"
        "  v_pk_mul_f32 v[10:11], v[10:11], v[10:11] op_sel:[0,0] op_sel_hi:[1,1]\n"
        "  v_pk_mul_f32 v[42:43], v[42:43], v[42:43] op_sel:[0,0] op_sel_hi:[1,1]\n"
        "  s_waitcnt lgkmcnt(4)  ; auto\n"
        "  v_pk_fma_f32 v[208:209], v[232:233], v[220:221], v[228:229] op_sel:[1,0,0] op_sel_hi:[1,1,1]\n"
        "  v_pk_fma_f32 v[212:213], v[234:235], v[220:221], v[228:229] op_sel:[1,0,0] op_sel_hi:[1,1,1]\n"
        "  v_pk_fma_f32 v[210:211], v[232:233], v[222:223], v[230:231] op_sel:[1,0,0] op_sel_hi:[1,1,1]\n"
        "  v_pk_fma_f32 v[214:215], v[234:235], v[222:223], v[230:231] op_sel:[1,0,0] op_sel_hi:[1,1,1]\n"
        "  v_pk_fma_f32 v[12:13], v[232:233], v[12:13], v[208:209] op_sel:[0,0,0] op_sel_hi:[0,1,1]\n"
        "  v_pk_fma_f32 v[44:45], v[234:235], v[44:45], v[212:213] op_sel:[0,0,0] op_sel_hi:[0,1,1]\n"
        "  v_pk_fma_f32 v[14:15], v[232:233], v[14:15], v[210:211] op_sel:[0,0,0] op_sel_hi:[0,1,1]\n"
        "  v_pk_fma_f32 v[46:47], v[234:235], v[46:47], v[214:215] op_sel:[0,0,0] op_sel_hi:[0,1,1]\n"
        "  v_max_f32 v12, 0, v12\n"
        "  v_max_f32 v44, 0, v44\n"
        "  v_max_f32 v13, 0, v13\n"
        "  v_max_f32 v45, 0, v45\n"
        "  v_max_f32 v14, 0, v14\n"
        "  v_max_f32 v46, 0, v46\n"
        "  v_max_f32 v15, 0, v15\n"
        "  v_max_f32 v47, 0, v47\n"
        "  v_pk_mul_f32 v[12:13], v[12:13], v[12:13] op_sel:[0,0] op_sel_hi:[1,1]\n"
        "  v_pk_mul_f32 v[44:45], v[44:45], v[44:45] op_sel:[0,0] op_sel_hi:[1,1]\n"
        "  v_pk_mul_f32 v[14:15], v[14:15], v[14:15] op_sel:[0,0] op_sel_hi:[1,1]\n"
        "  v_pk_mul_f32 v[46:47], v[46:47], v[46:47] op_sel:[0,0] op_sel_hi:[1,1]\n"
        "  v_cvt_pk_bf16_f32 v8, v8, v9\n"
        "  v_cvt_pk_bf16_f32 v40, v40, v41\n"
        "  v_cvt_pk_bf16_f32 v9, v10, v11\n"
        "  v_cvt_pk_bf16_f32 v41, v42, v43\n"
        "  v_cvt_pk_bf16_f32 v10, v12, v13\n"
        "  v_cvt_pk_bf16_f32 v42, v44, v45\n"
        "  v_cvt_pk_bf16_f32 v11, v14, v15\n"
        "  v_cvt_pk_bf16_f32 v43, v46, v47\n"
        "  buffer_store_dwordx4 v[8:11], v182, s[24:27], s94 offen offset:0 sc1\n"
        "  buffer_store_dwordx4 v[40:43], v182, s[24:27], s94 offen offset:1024 sc1\n"
        "  v_pk_fma_f32 v[208:209], v[236:237], v[216:217], v[224:225] op_sel:[1,0,0] op_sel_hi:[1,1,1]\n"
        "  v_pk_fma_f32 v[212:213], v[238:239], v[216:217], v[224:225] op_sel:[1,0,0] op_sel_hi:[1,1,1]\n"
        "  v_pk_fma_f32 v[210:211], v[236:237], v[218:219], v[226:227] op_sel:[1,0,0] op_sel_hi:[1,1,1]\n"
        "  v_pk_fma_f32 v[214:215], v[238:239], v[218:219], v[226:227] op_sel:[1,0,0] op_sel_hi:[1,1,1]\n"
        "  v_pk_fma_f32 v[72:73], v[236:237], v[72:73], v[208:209] op_sel:[0,0,0] op_sel_hi:[0,1,1]\n"
        "  v_pk_fma_f32 v[104:105], v[238:239], v[104:105], v[212:213] op_sel:[0,0,0] op_sel_hi:[0,1,1]\n"
        "  v_pk_fma_f32 v[74:75], v[236:237], v[74:75], v[210:211] op_sel:[0,0,0] op_sel_hi:[0,1,1]\n"
        "  v_pk_fma_f32 v[106:107], v[238:239], v[106:107], v[214:215] op_sel:[0,0,0] op_sel_hi:[0,1,1]\n"
        "  v_max_f32 v72, 0, v72\n"
        "  v_max_f32 v104, 0, v104\n"
        "  v_max_f32 v73, 0, v73\n"
        "  v_max_f32 v105, 0, v105\n"
        "  v_max_f32 v74, 0, v74\n"
        "  v_max_f32 v106, 0, v106\n"
        "  v_max_f32 v75, 0, v75\n"
        "  v_max_f32 v107, 0, v107\n"
        "  v_pk_mul_f32 v[72:73], v[72:73], v[72:73] op_sel:[0,0] op_sel_hi:[1,1]\n"
        "  v_pk_mul_f32 v[104:105], v[104:105], v[104:105] op_sel:[0,0] op_sel_hi:[1,1]\n"
        "  v_pk_mul_f32 v[74:75], v[74:75], v[74:75] op_sel:[0,0] op_sel_hi:[1,1]\n"
        "  v_pk_mul_f32 v[106:107], v[106:107], v[106:107] op_sel:[0,0] op_sel_hi:[1,1]\n"
        "  v_pk_fma_f32 v[208:209], v[236:237], v[220:221], v[228:229] op_sel:[1,0,0] op_sel_hi:[1,1,1]\n"
        "  v_pk_fma_f32 v[212:213], v[238:239], v[220:221], v[228:229] op_sel:[1,0,0] op_sel_hi:[1,1,1]\n"
        "  v_pk_fma_f32 v[210:211], v[236:237], v[222:223], v[230:231] op_sel:[1,0,0] op_sel_hi:[1,1,1]\n"
        "  v_pk_fma_f32 v[214:215], v[238:239], v[222:223], v[230:231] op_sel:[1,0,0] op_sel_hi:[1,1,1]\n"
        "  v_pk_fma_f32 v[76:77], v[236:237], v[76:77], v[208:209] op_sel:[0,0,0] op_sel_hi:[0,1,1]\n"
        "  v_pk_fma_f32 v[108:109], v[238:239], v[108:109], v[212:213] op_sel:[0,0,0] op_sel_hi:[0,1,1]\n"
        "  v_pk_fma_f32 v[78:79], v[236:237], v[78:79], v[210:211] op_sel:[0,0,0] op_sel_hi:[0,1,1]\n"
        "  v_pk_fma_f32 v[110:111], v[238:239], v[110:111], v[214:215] op_sel:[0,0,0] op_sel_hi:[0,1,1]\n"
        "  v_max_f32 v76, 0, v76\n"
        "  v_max_f32 v108, 0, v108\n"
        "  v_max_f32 v77, 0, v77\n"
        "  v_max_f32 v109, 0, v109\n"
        "  v_max_f32 v78, 0, v78\n"
        "  v_max_f32 v110, 0, v110\n"
        "  v_max_f32 v79, 0, v79\n"
        "  v_max_f32 v111, 0, v111\n"
        "  v_pk_mul_f32 v[76:77], v[76:77], v[76:77] op_sel:[0,0] op_sel_hi:[1,1]\n"
        "  v_pk_mul_f32 v[108:109], v[108:109], v[108:109] op_sel:[0,0] op_sel_hi:[1,1]\n"
        "  v_pk_mul_f32 v[78:79], v[78:79], v[78:79] op_sel:[0,0] op_sel_hi:[1,1]\n"
        "  v_pk_mul_f32 v[110:111], v[110:111], v[110:111] op_sel:[0,0] op_sel_hi:[1,1]\n"
        "  v_cvt_pk_bf16_f32 v72, v72, v73\n"
        "  v_cvt_pk_bf16_f32 v104, v104, v105\n"
        "  v_cvt_pk_bf16_f32 v73, v74, v75\n"
        "  v_cvt_pk_bf16_f32 v105, v106, v107\n"
        "  v_cvt_pk_bf16_f32 v74, v76, v77\n"
        "  v_cvt_pk_bf16_f32 v106, v108, v109\n"
        "  v_cvt_pk_bf16_f32 v75, v78, v79\n"
        "  v_cvt_pk_bf16_f32 v107, v110, v111\n"
        "  buffer_store_dwordx4 v[72:75], v182, s[24:27], s94 offen offset:2048 sc1\n"
        "  buffer_store_dwordx4 v[104:107], v182, s[24:27], s94 offen offset:3072 sc1\n"
        "  ds_read_b128 v[216:219], v198 offset:384\n"
        "  ds_read_b128 v[224:227], v195 offset:384\n"
        "  ds_read_b128 v[220:223], v198 offset:448\n"
        "  ds_read_b128 v[228:231], v195 offset:448\n"
        "  s_add_u32 s94, s91, 8192\n"
        "  s_waitcnt lgkmcnt(6)  ; auto\n"
        "  v_pk_fma_f32 v[208:209], v[232:233], v[144:145], v[152:153] op_sel:[1,0,0] op_sel_hi:[1,1,1]\n"
        "  v_pk_fma_f32 v[212:213], v[234:235], v[144:145], v[152:153] op_sel:[1,0,0] op_sel_hi:[1,1,1]\n"
        "  v_pk_fma_f32 v[210:211], v[232:233], v[146:147], v[154:155] op_sel:[1,0,0] op_sel_hi:[1,1,1]\n"
        "  v_pk_fma_f32 v[214:215], v[234:235], v[146:147], v[154:155] op_sel:[1,0,0] op_sel_hi:[1,1,1]\n"
        "  v_pk_fma_f32 v[16:17], v[232:233], v[16:17], v[208:209] op_sel:[0,0,0] op_sel_hi:[0,1,1]\n"
        "  v_pk_fma_f32 v[48:49], v[234:235], v[48:49], v[212:213] op_sel:[0,0,0] op_sel_hi:[0,1,1]\n"
        "  v_pk_fma_f32 v[18:19], v[232:233], v[18:19], v[210:211] op_sel:[0,0,0] op_sel_hi:[0,1,1]\n"
        "  v_pk_fma_f32 v[50:51], v[234:235], v[50:51], v[214:215] op_sel:[0,0,0] op_sel_hi:[0,1,1]\n"
        "  v_max_f32 v16, 0, v16\n"
        "  v_max_f32 v48, 0, v48\n"
        "  v_max_f32 v17, 0, v17\n"
        "  v_max_f32 v49, 0, v49\n"
        "  v_max_f32 v18, 0, v18\n"
        "  v_max_f32 v50, 0, v50\n"
        "  v_max_f32 v19, 0, v19\n"
        "  v_max_f32 v51, 0, v51\n"
        "  v_pk_mul_f32 v[16:17], v[16:17], v[16:17] op_sel:[0,0] op_sel_hi:[1,1]\n"
        "  v_pk_mul_f32 v[48:49], v[48:49], v[48:49] op_sel:[0,0] op_sel_hi:[1,1]\n"
        "  v_pk_mul_f32 v[18:19], v[18:19], v[18:19] op_sel:[0,0] op_sel_hi:[1,1]\n"
        "  v_pk_mul_f32 v[50:51], v[50:51], v[50:51] op_sel:[0,0] op_sel_hi:[1,1]\n"
        "  s_waitcnt lgkmcnt(4)  ; auto\n"
        "  v_pk_fma_f32 v[208:209], v[232:233], v[148:149], v[156:157] op_sel:[1,0,0] op_sel_hi:[1,1,1]\n"
        "  v_pk_fma_f32 v[212:213], v[234:235], v[148:149], v[156:157] op_sel:[1,0,0] op_sel_hi:[1,1,1]\n"
        "  v_pk_fma_f32 v[210:211], v[232:233], v[150:151], v[158:159] op_sel:[1,0,0] op_sel_hi:[1,1,1]\n"
        "  v_pk_fma_f32 v[214:215], v[234:235], v[150:151], v[158:159] op_sel:[1,0,0] op_sel_hi:[1,1,1]\n"
        "  v_pk_fma_f32 v[20:21], v[232:233], v[20:21], v[208:209] op_sel:[0,0,0] op_sel_hi:[0,1,1]\n"
        "  v_pk_fma_f32 v[52:53], v[234:235], v[52:53], v[212:213] op_sel:[0,0,0] op_sel_hi:[0,1,1]\n"
        "  v_pk_fma_f32 v[22:23], v[232:233], v[22:23], v[210:211] op_sel:[0,0,0] op_sel_hi:[0,1,1]\n"
        "  v_pk_fma_f32 v[54:55], v[234:235], v[54:55], v[214:215] op_sel:[0,0,0] op_sel_hi:[0,1,1]\n"
        "  v_max_f32 v20, 0, v20\n"
        "  v_max_f32 v52, 0, v52\n"
        "  v_max_f32 v21, 0, v21\n"
        "  v_max_f32 v53, 0, v53\n"
        "  v_max_f32 v22, 0, v22\n"
        "  v_max_f32 v54, 0, v54\n"
        "  v_max_f32 v23, 0, v23\n"
        "  v_max_f32 v55, 0, v55\n"
        "  v_pk_mul_f32 v[20:21], v[20:21], v[20:21] op_sel:[0,0] op_sel_hi:[1,1]\n"
        "  v_pk_mul_f32 v[52:53], v[52:53], v[52:53] op_sel:[0,0] op_sel_hi:[1,1]\n"
        "  v_pk_mul_f32 v[22:23], v[22:23], v[22:23] op_sel:[0,0] op_sel_hi:[1,1]\n"
        "  v_pk_mul_f32 v[54:55], v[54:55], v[54:55] op_sel:[0,0] op_sel_hi:[1,1]\n"
        "  v_cvt_pk_bf16_f32 v16, v16, v17\n"
        "  v_cvt_pk_bf16_f32 v48, v48, v49\n"
        "  v_cvt_pk_bf16_f32 v17, v18, v19\n"
        "  v_cvt_pk_bf16_f32 v49, v50, v51\n"
        "  v_cvt_pk_bf16_f32 v18, v20, v21\n"
        "  v_cvt_pk_bf16_f32 v50, v52, v53\n"
        "  v_cvt_pk_bf16_f32 v19, v22, v23\n"
        "  v_cvt_pk_bf16_f32 v51, v54, v55\n"
        "  buffer_store_dwordx4 v[16:19], v182, s[24:27], s94 offen offset:0 sc1\n"
        "  buffer_store_dwordx4 v[48:51], v182, s[24:27], s94 offen offset:1024 sc1\n"
        "  v_pk_fma_f32 v[208:209], v[236:237], v[144:145], v[152:153] op_sel:[1,0,0] op_sel_hi:[1,1,1]\n"
        "  v_pk_fma_f32 v[212:213], v[238:239], v[144:145], v[152:153] op_sel:[1,0,0] op_sel_hi:[1,1,1]\n"
        "  v_pk_fma_f32 v[210:211], v[236:237], v[146:147], v[154:155] op_sel:[1,0,0] op_sel_hi:[1,1,1]\n"
        "  v_pk_fma_f32 v[214:215], v[238:239], v[146:147], v[154:155] op_sel:[1,0,0] op_sel_hi:[1,1,1]\n"
        "  v_pk_fma_f32 v[80:81], v[236:237], v[80:81], v[208:209] op_sel:[0,0,0] op_sel_hi:[0,1,1]\n"
        "  v_pk_fma_f32 v[112:113], v[238:239], v[112:113], v[212:213] op_sel:[0,0,0] op_sel_hi:[0,1,1]\n"
        "  v_pk_fma_f32 v[82:83], v[236:237], v[82:83], v[210:211] op_sel:[0,0,0] op_sel_hi:[0,1,1]\n"
        "  v_pk_fma_f32 v[114:115], v[238:239], v[114:115], v[214:215] op_sel:[0,0,0] op_sel_hi:[0,1,1]\n"
        "  v_max_f32 v80, 0, v80\n"
        "  v_max_f32 v112, 0, v112\n"
        "  v_max_f32 v81, 0, v81\n"
        "  v_max_f32 v113, 0, v113\n"
        "  v_max_f32 v82, 0, v82\n"
        "  v_max_f32 v114, 0, v114\n"
        "  v_max_f32 v83, 0, v83\n"
        "  v_max_f32 v115, 0, v115\n"
        "  v_pk_mul_f32 v[80:81], v[80:81], v[80:81] op_sel:[0,0] op_sel_hi:[1,1]\n"
        "  v_pk_mul_f32 v[112:113], v[112:113], v[112:113] op_sel:[0,0] op_sel_hi:[1,1]\n"
        "  v_pk_mul_f32 v[82:83], v[82:83], v[82:83] op_sel:[0,0] op_sel_hi:[1,1]\n"
        "  v_pk_mul_f32 v[114:115], v[114:115], v[114:115] op_sel:[0,0] op_sel_hi:[1,1]\n"
        "  v_pk_fma_f32 v[208:209], v[236:237], v[148:149], v[156:157] op_sel:[1,0,0] op_sel_hi:[1,1,1]\n"
        "  v_pk_fma_f32 v[212:213], v[238:239], v[148:149], v[156:157] op_sel:[1,0,0] op_sel_hi:[1,1,1]\n"
        "  v_pk_fma_f32 v[210:211], v[236:237], v[150:151], v[158:159] op_sel:[1,0,0] op_sel_hi:[1,1,1]\n"
        "  v_pk_fma_f32 v[214:215], v[238:239], v[150:151], v[158:159] op_sel:[1,0,0] op_sel_hi:[1,1,1]\n"
        "  v_pk_fma_f32 v[84:85], v[236:237], v[84:85], v[208:209] op_sel:[0,0,0] op_sel_hi:[0,1,1]\n"
        "  v_pk_fma_f32 v[116:117], v[238:239], v[116:117], v[212:213] op_sel:[0,0,0] op_sel_hi:[0,1,1]\n"
        "  v_pk_fma_f32 v[86:87], v[236:237], v[86:87], v[210:211] op_sel:[0,0,0] op_sel_hi:[0,1,1]\n"
        "  v_pk_fma_f32 v[118:119], v[238:239], v[118:119], v[214:215] op_sel:[0,0,0] op_sel_hi:[0,1,1]\n"
        "  v_max_f32 v84, 0, v84\n"
        "  v_max_f32 v116, 0, v116\n"
        "  v_max_f32 v85, 0, v85\n"
        "  v_max_f32 v117, 0, v117\n"
        "  v_max_f32 v86, 0, v86\n"
        "  v_max_f32 v118, 0, v118\n"
        "  v_max_f32 v87, 0, v87\n"
        "  v_max_f32 v119, 0, v119\n"
        "  v_pk_mul_f32 v[84:85], v[84:85], v[84:85] op_sel:[0,0] op_sel_hi:[1,1]\n"
        "  v_pk_mul_f32 v[116:117], v[116:117], v[116:117] op_sel:[0,0] op_sel_hi:[1,1]\n"
        "  v_pk_mul_f32 v[86:87], v[86:87], v[86:87] op_sel:[0,0] op_sel_hi:[1,1]\n"
        "  v_pk_mul_f32 v[118:119], v[118:119], v[118:119] op_sel:[0,0] op_sel_hi:[1,1]\n"
        "  v_cvt_pk_bf16_f32 v80, v80, v81\n"
        "  v_cvt_pk_bf16_f32 v112, v112, v113\n"
        "  v_cvt_pk_bf16_f32 v81, v82, v83\n"
        "  v_cvt_pk_bf16_f32 v113, v114, v115\n"
        "  v_cvt_pk_bf16_f32 v82, v84, v85\n"
        "  v_cvt_pk_bf16_f32 v114, v116, v117\n"
        "  v_cvt_pk_bf16_f32 v83, v86, v87\n"
        "  v_cvt_pk_bf16_f32 v115, v118, v119\n"
        "  buffer_store_dwordx4 v[80:83], v182, s[24:27], s94 offen offset:2048 sc1\n"
        "  buffer_store_dwordx4 v[112:115], v182, s[24:27], s94 offen offset:3072 sc1\n"
        "  s_add_u32 s94, s91, 12288\n"
        "  s_waitcnt lgkmcnt(2)  ; auto\n"
        "  v_pk_fma_f32 v[208:209], v[232:233], v[216:217], v[224:225] op_sel:[1,0,0] op_sel_hi:[1,1,1]\n"
        "  v_pk_fma_f32 v[212:213], v[234:235], v[216:217], v[224:225] op_sel:[1,0,0] op_sel_hi:[1,1,1]\n"
        "  v_pk_fma_f32 v[210:211], v[232:233], v[218:219], v[226:227] op_sel:[1,0,0] op_sel_hi:[1,1,1]\n"
        "  v_pk_fma_f32 v[214:215], v[234:235], v[218:219], v[226:227] op_sel:[1,0,0] op_sel_hi:[1,1,1]\n"
        "  v_pk_fma_f32 v[24:25], v[232:233], v[24:25], v[208:209] op_sel:[0,0,0] op_sel_hi:[0,1,1]\n"
        "  v_pk_fma_f32 v[56:57], v[234:235], v[56:57], v[212:213] op_sel:[0,0,0] op_sel_hi:[0,1,1]\n"
        "  v_pk_fma_f32 v[26:27], v[232:233], v[26:27], v[210:211] op_sel:[0,0,0] op_sel_hi:[0,1,1]\n"
        "  v_pk_fma_f32 v[58:59], v[234:235], v[58:59], v[214:215] op_sel:[0,0,0] op_sel_hi:[0,1,1]\n"
        "  v_max_f32 v24, 0, v24\n"
        "  v_max_f32 v56, 0, v56\n"
        "  v_max_f32 v25, 0, v25\n"
        "  v_max_f32 v57, 0, v57\n"
        "  v_max_f32 v26, 0, v26\n"
        "  v_max_f32 v58, 0, v58\n"
        "  v_max_f32 v27, 0, v27\n"
        "  v_max_f32 v59, 0, v59\n"
        "  v_pk_mul_f32 v[24:25], v[24:25], v[24:25] op_sel:[0,0] op_sel_hi:[1,1]\n"
        "  v_pk_mul_f32 v[56:57], v[56:57], v[56:57] op_sel:[0,0] op_sel_hi:[1,1]\n"
        "  v_pk_mul_f32 v[26:27], v[26:27], v[26:27] op_sel:[0,0] op_sel_hi:[1,1]\n"
        "  v_pk_mul_f32 v[58:59], v[58:59], v[58:59] op_sel:[0,0] op_sel_hi:[1,1]\n"
        "  s_waitcnt lgkmcnt(0)  ; auto\n"
        "  v_pk_fma_f32 v[208:209], v[232:233], v[220:221], v[228:229] op_sel:[1,0,0] op_sel_hi:[1,1,1]\n"
        "  v_pk_fma_f32 v[212:213], v[234:235], v[220:221], v[228:229] op_sel:[1,0,0] op_sel_hi:[1,1,1]\n"
        "  v_pk_fma_f32 v[210:211], v[232:233], v[222:223], v[230:231] op_sel:[1,0,0] op_sel_hi:[1,1,1]\n"
        "  v_pk_fma_f32 v[214:215], v[234:235], v[222:223], v[230:231] op_sel:[1,0,0] op_sel_hi:[1,1,1]\n"
        "  v_pk_fma_f32 v[28:29], v[232:233], v[28:29], v[208:209] op_sel:[0,0,0] op_sel_hi:[0,1,1]\n"
        "  v_pk_fma_f32 v[60:61], v[234:235], v[60:61], v[212:213] op_sel:[0,0,0] op_sel_hi:[0,1,1]\n"
        "  v_pk_fma_f32 v[30:31], v[232:233], v[30:31], v[210:211] op_sel:[0,0,0] op_sel_hi:[0,1,1]\n"
        "  v_pk_fma_f32 v[62:63], v[234:235], v[62:63], v[214:215] op_sel:[0,0,0] op_sel_hi:[0,1,1]\n"
        "  v_max_f32 v28, 0, v28\n"
        "  v_max_f32 v60, 0, v60\n"
        "  v_max_f32 v29, 0, v29\n"
        "  v_max_f32 v61, 0, v61\n"
        "  v_max_f32 v30, 0, v30\n"
        "  v_max_f32 v62, 0, v62\n"
        "  v_max_f32 v31, 0, v31\n"
        "  v_max_f32 v63, 0, v63\n"
        "  v_pk_mul_f32 v[28:29], v[28:29], v[28:29] op_sel:[0,0] op_sel_hi:[1,1]\n"
        "  v_pk_mul_f32 v[60:61], v[60:61], v[60:61] op_sel:[0,0] op_sel_hi:[1,1]\n"
        "  v_pk_mul_f32 v[30:31], v[30:31], v[30:31] op_sel:[0,0] op_sel_hi:[1,1]\n"
        "  v_pk_mul_f32 v[62:63], v[62:63], v[62:63] op_sel:[0,0] op_sel_hi:[1,1]\n"
        "  v_cvt_pk_bf16_f32 v24, v24, v25\n"
        "  v_cvt_pk_bf16_f32 v56, v56, v57\n"
        "  v_cvt_pk_bf16_f32 v25, v26, v27\n"
        "  v_cvt_pk_bf16_f32 v57, v58, v59\n"
        "  v_cvt_pk_bf16_f32 v26, v28, v29\n"
        "  v_cvt_pk_bf16_f32 v58, v60, v61\n"
        "  v_cvt_pk_bf16_f32 v27, v30, v31\n"
        "  v_cvt_pk_bf16_f32 v59, v62, v63\n"
        "  buffer_store_dwordx4 v[24:27], v182, s[24:27], s94 offen offset:0 sc1\n"
        "  buffer_store_dwordx4 v[56:59], v182, s[24:27], s94 offen offset:1024 sc1\n"
        "  v_pk_fma_f32 v[208:209], v[236:237], v[216:217], v[224:225] op_sel:[1,0,0] op_sel_hi:[1,1,1]\n"
        "  v_pk_fma_f32 v[212:213], v[238:239], v[216:217], v[224:225] op_sel:[1,0,0] op_sel_hi:[1,1,1]\n"
        "  v_pk_fma_f32 v[210:211], v[236:237], v[218:219], v[226:227] op_sel:[1,0,0] op_sel_hi:[1,1,1]\n"
        "  v_pk_fma_f32 v[214:215], v[238:239], v[218:219], v[226:227] op_sel:[1,0,0] op_sel_hi:[1,1,1]\n"
        "  v_pk_fma_f32 v[88:89], v[236:237], v[88:89], v[208:209] op_sel:[0,0,0] op_sel_hi:[0,1,1]\n"
        "  v_pk_fma_f32 v[120:121], v[238:239], v[120:121], v[212:213] op_sel:[0,0,0] op_sel_hi:[0,1,1]\n"
        "  v_pk_fma_f32 v[90:91], v[236:237], v[90:91], v[210:211] op_sel:[0,0,0] op_sel_hi:[0,1,1]\n"
        "  v_pk_fma_f32 v[122:123], v[238:239], v[122:123], v[214:215] op_sel:[0,0,0] op_sel_hi:[0,1,1]\n"
        "  v_max_f32 v88, 0, v88\n"
        "  v_max_f32 v120, 0, v120\n"
        "  v_max_f32 v89, 0, v89\n"
        "  v_max_f32 v121, 0, v121\n"
        "  v_max_f32 v90, 0, v90\n"
        "  v_max_f32 v122, 0, v122\n"
        "  v_max_f32 v91, 0, v91\n"
        "  v_max_f32 v123, 0, v123\n"
        "  v_pk_mul_f32 v[88:89], v[88:89], v[88:89] op_sel:[0,0] op_sel_hi:[1,1]\n"
        "  v_pk_mul_f32 v[120:121], v[120:121], v[120:121] op_sel:[0,0] op_sel_hi:[1,1]\n"
        "  v_pk_mul_f32 v[90:91], v[90:91], v[90:91] op_sel:[0,0] op_sel_hi:[1,1]\n"
        "  v_pk_mul_f32 v[122:123], v[122:123], v[122:123] op_sel:[0,0] op_sel_hi:[1,1]\n"
        "  v_pk_fma_f32 v[208:209], v[236:237], v[220:221], v[228:229] op_sel:[1,0,0] op_sel_hi:[1,1,1]\n"
        "  v_pk_fma_f32 v[212:213], v[238:239], v[220:221], v[228:229] op_sel:[1,0,0] op_sel_hi:[1,1,1]\n"
        "  v_pk_fma_f32 v[210:211], v[236:237], v[222:223], v[230:231] op_sel:[1,0,0] op_sel_hi:[1,1,1]\n"
        "  v_pk_fma_f32 v[214:215], v[238:239], v[222:223], v[230:231] op_sel:[1,0,0] op_sel_hi:[1,1,1]\n"
        "  v_pk_fma_f32 v[92:93], v[236:237], v[92:93], v[208:209] op_sel:[0,0,0] op_sel_hi:[0,1,1]\n"
        "  v_pk_fma_f32 v[124:125], v[238:239], v[124:125], v[212:213] op_sel:[0,0,0] op_sel_hi:[0,1,1]\n"
        "  v_pk_fma_f32 v[94:95], v[236:237], v[94:95], v[210:211] op_sel:[0,0,0] op_sel_hi:[0,1,1]\n"
        "  v_pk_fma_f32 v[126:127], v[238:239], v[126:127], v[214:215] op_sel:[0,0,0] op_sel_hi:[0,1,1]\n"
        "  v_max_f32 v92, 0, v92\n"
        "  v_max_f32 v124, 0, v124\n"
        "  v_max_f32 v93, 0, v93\n"
        "  v_max_f32 v125, 0, v125\n"
        "  v_max_f32 v94, 0, v94\n"
        "  v_max_f32 v126, 0, v126\n"
        "  v_max_f32 v95, 0, v95\n"
        "  v_max_f32 v127, 0, v127\n"
        "  v_pk_mul_f32 v[92:93], v[92:93], v[92:93] op_sel:[0,0] op_sel_hi:[1,1]\n"
        "  v_pk_mul_f32 v[124:125], v[124:125], v[124:125] op_sel:[0,0] op_sel_hi:[1,1]\n"
        "  v_pk_mul_f32 v[94:95], v[94:95], v[94:95] op_sel:[0,0] op_sel_hi:[1,1]\n"
        "  v_pk_mul_f32 v[126:127], v[126:127], v[126:127] op_sel:[0,0] op_sel_hi:[1,1]\n"
        "  v_cvt_pk_bf16_f32 v88, v88, v89\n"
        "  v_cvt_pk_bf16_f32 v120, v120, v121\n"
        "  v_cvt_pk_bf16_f32 v89, v90, v91\n"
        "  v_cvt_pk_bf16_f32 v121, v122, v123\n"
        "  v_cvt_pk_bf16_f32 v90, v92, v93\n"
        "  v_cvt_pk_bf16_f32 v122, v124, v125\n"
        "  v_cvt_pk_bf16_f32 v91, v94, v95\n"
        "  v_cvt_pk_bf16_f32 v123, v126, v127\n"
        "  buffer_store_dwordx4 v[88:91], v182, s[24:27], s94 offen offset:2048 sc1\n"
        "  buffer_store_dwordx4 v[120:123], v182, s[24:27], s94 offen offset:3072 sc1\n"
        "  s_waitcnt vmcnt(16)\n"
        "  s_mov_b32 s87, 262144\n"
        "  ds_read_b64_tr_b16 a[96:97], v178 offset:49152\n"
        "  ds_read_b64_tr_b16 a[98:99], v178 offset:51200\n"
        "  ds_read_b64_tr_b16 a[100:101], v179 offset:49152\n"
        "  ds_read_b64_tr_b16 a[102:103], v179 offset:51200\n"
        "  ds_read_b64_tr_b16 a[104:105], v180 offset:49152\n"
        "  ds_read_b64_tr_b16 a[106:107], v180 offset:51200\n"
        "  ds_read_b64_tr_b16 a[108:109], v181 offset:49152\n"
        "  ds_read_b64_tr_b16 a[110:111], v181 offset:51200\n"
        "  ds_read_b64_tr_b16 a[112:113], v178 offset:53248\n"
        "  ds_read_b64_tr_b16 a[114:115], v178 offset:55296\n"
        "  ds_read_b64_tr_b16 a[116:117], v179 offset:53248\n"
        "  ds_read_b64_tr_b16 a[118:119], v179 offset:55296\n"
        "  ds_read_b64_tr_b16 a[120:121], v180 offset:53248\n"
        "  ds_read_b64_tr_b16 a[122:123], v180 offset:55296\n"
        "  ds_read_b64_tr_b16 a[124:125], v181 offset:53248\n"
        "  ds_read_b64_tr_b16 a[126:127], v181 offset:55296\n"
        "  ds_read_b64_tr_b16 a[128:129], v178 offset:57344\n"
        "  ds_read_b64_tr_b16 a[130:131], v178 offset:59392\n"
        "  ds_read_b64_tr_b16 a[132:133], v179 offset:57344\n"
        "  ds_read_b64_tr_b16 a[134:135], v179 offset:59392\n"
        "  ds_read_b64_tr_b16 a[136:137], v180 offset:57344\n"
        "  ds_read_b64_tr_b16 a[138:139], v180 offset:59392\n"
        "  ds_read_b64_tr_b16 a[140:141], v181 offset:57344\n"
        "  ds_read_b64_tr_b16 a[142:143], v181 offset:59392\n"
        "  s_add_u32 m0, s59, 49152\n"
        "  s_waitcnt lgkmcnt(15)  ; auto\n"
        "  buffer_load_dwordx4 v172, s[16:19], s60 offen offset:0 lds\n"
        "  buffer_load_dwordx4 v173, s[16:19], s60 offen offset:1024 lds\n"
        "  buffer_load_dwordx4 v174, s[16:19], s60 offen offset:2048 lds\n"
        "  buffer_load_dwordx4 v175, s[16:19], s60 offen offset:3072 lds\n"
        "  s_add_u32 s60, s60, 28672\n"
        "  s_add_u32 m0, s59, 53248\n"
        "  s_waitcnt lgkmcnt(8)  ; auto\n"
        "  buffer_load_dwordx4 v172, s[16:19], s60 offen offset:0 lds\n"
        "  buffer_load_dwordx4 v173, s[16:19], s60 offen offset:1024 lds\n"
        "  buffer_load_dwordx4 v174, s[16:19], s60 offen offset:2048 lds\n"
        "  buffer_load_dwordx4 v175, s[16:19], s60 offen offset:3072 lds\n"
        "  s_add_u32 s60, s60, 28672\n"
        "  s_add_u32 m0, s59, 57344\n"
        "  s_waitcnt lgkmcnt(0)  ; auto\n"
        "  buffer_load_dwordx4 v172, s[16:19], s60 offen offset:0 lds\n"
        "  buffer_load_dwordx4 v173, s[16:19], s60 offen offset:1024 lds\n"
        "  buffer_load_dwordx4 v174, s[16:19], s60 offen offset:2048 lds\n"
        "  buffer_load_dwordx4 v175, s[16:19], s60 offen offset:3072 lds\n"
        "  s_add_u32 s60, s60, 28672\n"
        "  s_waitcnt vmcnt(12)  ; auto\n"
        "  s_barrier\n"
        "  s_mov_b64 exec, s[78:79]\n"
        "  v_mov_b32 v190, v188\n"
        "  v_mov_b32 v186, v188\n"
        "  buffer_store_dwordx2 v[190:191], v185, s[24:27], 0 offen sc1\n"
        "  s_mov_b64 exec, -1\n"
        "  s_cmp_eq_u32 s86, 0\n"
        "  s_cbranch_scc1 .Lfin\n"
        ".Lpub:\n"
        "  s_waitcnt vmcnt(13)\n"
        "  v_add_u32 v168, 65536, v178\n"
        "  v_add_u32 v169, 65536, v179\n"
        "  v_add_u32 v170, 65536, v180\n"
        "  v_add_u32 v183, 65536, v181\n"
        "  s_waitcnt vmcnt(45)  ; auto\n"
        "  ds_read_b64_tr_b16 a[144:145], v168 offset:32768\n"
        "  ds_read_b64_tr_b16 a[146:147], v168 offset:34816\n"
        "  ds_read_b64_tr_b16 a[148:149], v169 offset:32768\n"
        "  ds_read_b64_tr_b16 a[150:151], v169 offset:34816\n"
        "  ds_read_b64_tr_b16 a[152:153], v170 offset:32768\n"
        "  ds_read_b64_tr_b16 a[154:155], v170 offset:34816\n"
        "  ds_read_b64_tr_b16 a[156:157], v183 offset:32768\n"
        "  ds_read_b64_tr_b16 a[158:159], v183 offset:34816\n"
        "  ds_read_b64_tr_b16 a[160:161], v168 offset:36864\n"
        "  ds_read_b64_tr_b16 a[162:163], v168 offset:38912\n"
        "  ds_read_b64_tr_b16 a[164:165], v169 offset:36864\n"
        "  ds_read_b64_tr_b16 a[166:167], v169 offset:38912\n"
        "  ds_read_b64_tr_b16 a[168:169], v170 offset:36864\n"
        "  ds_read_b64_tr_b16 a[170:171], v170 offset:38912\n"
        "  ds_read_b64_tr_b16 a[172:173], v183 offset:36864\n"
        "  ds_read_b64_tr_b16 a[174:175], v183 offset:38912\n"
        "  ds_read_b64_tr_b16 a[176:177], v168 offset:40960\n"
        "  ds_read_b64_tr_b16 a[178:179], v168 offset:43008\n"
        "  ds_read_b64_tr_b16 a[180:181], v169 offset:40960\n"
        "  ds_read_b64_tr_b16 a[182:183], v169 offset:43008\n"
        "  ds_read_b64_tr_b16 a[184:185], v170 offset:40960\n"
        "  ds_read_b64_tr_b16 a[186:187], v170 offset:43008\n"
        "  ds_read_b64_tr_b16 a[188:189], v183 offset:40960\n"
        "  ds_read_b64_tr_b16 a[190:191], v183 offset:43008\n"
        "  ds_read_b64_tr_b16 a[192:193], v178 offset:0\n"
        "  s_add_u32 s54, s89, 1\n"
        "  ds_read_b64_tr_b16 a[194:195], v178 offset:2048\n"
        "  s_and_b32 s54, s54, 3\n"
        "  ds_read_b64_tr_b16 a[196:197], v179 offset:0\n"
        "  s_lshl_b32 s54, s54, 10\n"
        "  ds_read_b64_tr_b16 a[198:199], v179 offset:2048\n"
        "  v_add_u32 v196, s54, v182\n"
        "  s_add_u32 m0, s59, 98304\n"
        "  s_waitcnt lgkmcnt(15)  ; auto\n"
        "  buffer_load_dwordx4 v172, s[16:19], s60 offen offset:0 lds\n"
        "  ds_read_b64_tr_b16 a[200:201], v180 offset:0\n"
        "  s_add_u32 s54, s89, 2\n"
        "  ds_read_b64_tr_b16 a[202:203], v180 offset:2048\n"
        "  s_and_b32 s54, s54, 3\n"
        "  ds_read_b64_tr_b16 a[204:205], v181 offset:0\n"
        "  s_lshl_b32 s54, s54, 10\n"
        "  ds_read_b64_tr_b16 a[206:207], v181 offset:2048\n"
        "  v_add_u32 v197, s54, v182\n"
        "  buffer_load_dwordx4 v173, s[16:19], s60 offen offset:1024 lds\n"
        "  ds_read_b64_tr_b16 a[208:209], v178 offset:4096\n"
        "  s_add_u32 s54, s89, 3\n"
        "  ds_read_b64_tr_b16 a[210:211], v178 offset:6144\n"
        "  s_and_b32 s54, s54, 3\n"
        "  ds_read_b64_tr_b16 a[212:213], v179 offset:4096\n"
        "  s_lshl_b32 s54, s54, 10\n"
        "  ds_read_b64_tr_b16 a[214:215], v179 offset:6144\n"
        "  v_add_u32 v198, s54, v182\n"
        "  buffer_load_dwordx4 v174, s[16:19], s60 offen offset:2048 lds\n"
        "  ds_read_b64_tr_b16 a[216:217], v180 offset:4096\n"
        "  s_lshl_b32 s54, s89, 10\n"
        "  ds_read_b64_tr_b16 a[218:219], v180 offset:6144\n"
        "  v_add_u32 v199, s54, v182\n"
        "  ds_read_b64_tr_b16 a[220:221], v181 offset:4096\n"
        "  v_lshrrev_b32 v224, 1, v206\n"
        "  ds_read_b64_tr_b16 a[222:223], v181 offset:6144\n"
        "  v_and_b32 v225, 7, v205\n"
        "  buffer_load_dwordx4 v175, s[16:19], s60 offen offset:3072 lds\n"
        "  ds_read_b64_tr_b16 a[224:225], v178 offset:8192\n"
        "  v_xor_b32 v225, v225, v224\n"
        "  ds_read_b64_tr_b16 a[226:227], v178 offset:10240\n"
        "  v_lshlrev_b32 v225, 4, v225\n"
        "  ds_read_b64_tr_b16 a[228:229], v179 offset:8192\n"
        "  v_lshl_add_u32 v225, v205, 7, v225\n"
        "  ds_read_b64_tr_b16 a[230:231], v179 offset:10240\n"
        "  v_and_b32 v226, 1, v206\n"
        "  s_add_u32 s60, s60, 28672\n"
        "  s_add_u32 m0, s59, 102400\n"
        "  s_waitcnt lgkmcnt(15)  ; auto\n"
        "  buffer_load_dwordx4 v172, s[16:19], s60 offen offset:0 lds\n"
        "  ds_read_b64_tr_b16 a[232:233], v180 offset:8192\n"
        "  v_lshl_add_u32 v225, v226, 3, v225\n"
        "  ds_read_b64_tr_b16 a[234:235], v180 offset:10240\n"
        "  s_lshl_b32 s54, s40, 11\n"
        "  ds_read_b64_tr_b16 a[236:237], v181 offset:8192\n"
        "  s_add_u32 s54, s54, 155648\n"
        "  ds_read_b64_tr_b16 a[238:239], v181 offset:10240\n"
        "  v_add_u32 v228, s54, v225\n"
        "  buffer_load_dwordx4 v173, s[16:19], s60 offen offset:1024 lds\n"
        "  v_xor_b32 v229, 32, v228\n"
        "  v_xor_b32 v230, 64, v228\n"
        "  v_xor_b32 v231, 96, v228\n"
        "  s_lshl_b32 s54, s40, 7\n"
        "  s_add_u32 s54, s54, 149504\n"
        "  v_lshl_add_u32 v227, v206, 3, s54\n"
        "  ds_read_b64 v[232:233], v228\n"
        "  ds_read_b64 v[240:241], v227 offset:0\n"
        "  buffer_load_dwordx4 v174, s[16:19], s60 offen offset:2048 lds\n"
        "  ds_read_b64 v[234:235], v229\n"
        "  ds_read_b64 v[242:243], v227 offset:32\n"
        "  ds_read_b64 v[236:237], v230\n"
        "  ds_read_b64 v[244:245], v227 offset:64\n"
        "  ds_read_b64 v[238:239], v231\n"
        "  ds_read_b64 v[246:247], v227 offset:96\n"
        "  v_lshl_add_u32 v225, v205, 1, s88\n"
        "  v_mul_u32_u24 v225, 896, v225\n"
        "  buffer_load_dwordx4 v175, s[16:19], s60 offen offset:3072 lds\n"
        "  v_lshl_add_u32 v225, v226, 5, v225\n"
        "  v_lshl_add_u32 v200, v224, 4, v225\n"
        "  s_waitcnt lgkmcnt(7)  ; auto\n"
        "  v_lshlrev_b32 v208, 16, v232\n"
        "  v_and_b32 v209, 0xffff0000, v232\n"
        "  s_waitcnt lgkmcnt(6)  ; auto\n"
        "  v_lshlrev_b32 v64, 16, v240\n"
        "  v_and_b32 v65, 0xffff0000, v240\n"
        "  v_lshlrev_b32 v210, 16, v233\n"
        "  v_and_b32 v211, 0xffff0000, v233\n"
        "  s_add_u32 s60, s60, 28672\n"
        "  s_add_u32 m0, s59, 106496\n"
        "  s_nop 0  ; hazard\n"
        "  buffer_load_dwordx4 v172, s[20:23], s61 offen offset:0 lds\n"
        "  v_lshlrev_b32 v66, 16, v241\n"
        "  v_and_b32 v67, 0xffff0000, v241\n"
        "  s_waitcnt lgkmcnt(5)  ; auto\n"
        "  v_lshlrev_b32 v212, 16, v234\n"
        "  v_and_b32 v213, 0xffff0000, v234\n"
        "  s_waitcnt lgkmcnt(4)  ; auto\n"
        "  v_lshlrev_b32 v68, 16, v242\n"
        "  v_and_b32 v69, 0xffff0000, v242\n"
        "  v_lshlrev_b32 v214, 16, v235\n"
        "  v_and_b32 v215, 0xffff0000, v235\n"
        "  buffer_load_dwordx4 v173, s[20:23], s61 offen offset:1024 lds\n"
        "  v_lshlrev_b32 v70, 16, v243\n"
        "  v_and_b32 v71, 0xffff0000, v243\n"
        "  s_waitcnt lgkmcnt(3)  ; auto\n"
        "  v_lshlrev_b32 v216, 16, v236\n"
        "  v_and_b32 v217, 0xffff0000, v236\n"
        "  s_waitcnt lgkmcnt(2)  ; auto\n"
        "  v_lshlrev_b32 v72, 16, v244\n"
        "  v_and_b32 v73, 0xffff0000, v244\n"
        "  v_lshlrev_b32 v218, 16, v237\n"
        "  v_and_b32 v219, 0xffff0000, v237\n"
        "  buffer_load_dwordx4 v176, s[20:23], s61 offen offset:2048 lds\n"
        "  v_lshlrev_b32 v74, 16, v245\n"
        "  v_and_b32 v75, 0xffff0000, v245\n"
        "  s_waitcnt lgkmcnt(1)  ; auto\n"
        "  v_lshlrev_b32 v220, 16, v238\n"
        "  v_and_b32 v221, 0xffff0000, v238\n"
        "  s_waitcnt lgkmcnt(0)  ; auto\n"
        "  v_lshlrev_b32 v76, 16, v246\n"
        "  v_and_b32 v77, 0xffff0000, v246\n"
        "  v_lshlrev_b32 v222, 16, v239\n"
        "  v_and_b32 v223, 0xffff0000, v239\n"
        "  buffer_load_dwordx4 v177, s[20:23], s61 offen offset:3072 lds\n"
        "  s_add_u32 s61, s61, 25088\n"
        "  v_lshlrev_b32 v78, 16, v247\n"
        "  v_and_b32 v79, 0xffff0000, v247\n"
        "  v_pk_add_f32 v[208:209], v[208:209], v[64:65] op_sel:[0,0] op_sel_hi:[1,1]\n"
        "  v_pk_add_f32 v[210:211], v[210:211], v[66:67] op_sel:[0,0] op_sel_hi:[1,1]\n"
        "  v_pk_add_f32 v[212:213], v[212:213], v[68:69] op_sel:[0,0] op_sel_hi:[1,1]\n"
        "  v_pk_add_f32 v[214:215], v[214:215], v[70:71] op_sel:[0,0] op_sel_hi:[1,1]\n"
        "  v_pk_add_f32 v[216:217], v[216:217], v[72:73] op_sel:[0,0] op_sel_hi:[1,1]\n"
        "  v_pk_add_f32 v[218:219], v[218:219], v[74:75] op_sel:[0,0] op_sel_hi:[1,1]\n"
        "  v_pk_add_f32 v[220:221], v[220:221], v[76:77] op_sel:[0,0] op_sel_hi:[1,1]\n"
        "  v_pk_add_f32 v[222:223], v[222:223], v[78:79] op_sel:[0,0] op_sel_hi:[1,1]\n"
        "  s_lshl_b32 s48, s85, 12\n"
        "  s_add_u32 s48, s48, s62\n"
        "  s_add_u32 s85, s85, 1\n"
        "  s_cmp_eq_u32 s85, 14\n"
        "  s_cselect_b32 s85, 0, s85\n"
        "  s_mov_b64 exec, s[80:81]\n"
        "  v_mov_b32 v188, 0\n"
        "  v_mov_b32 v189, 0\n"
        "  buffer_atomic_or_x2 v[188:189], v184, s[24:27], 0 offen sc0 sc1\n"
        "  s_mov_b64 exec, -1\n"
        "  s_add_u32 m0, s59, 0\n"
        "  s_nop 0  ; hazard\n"
        "  buffer_load_dwordx4 v172, s[20:23], s61 offen offset:0 lds\n"
        "  buffer_load_dwordx4 v173, s[20:23], s61 offen offset:1024 lds\n"
        "  buffer_load_dwordx4 v176, s[20:23], s61 offen offset:2048 lds\n"
        "  buffer_load_dwordx4 v177, s[20:23], s61 offen offset:3072 lds\n"
        "  s_add_u32 s61, s61, 25088\n"
        "  s_add_u32 m0, s59, 4096\n"
        "  s_nop 0  ; hazard\n"
        "  buffer_load_dwordx4 v172, s[20:23], s61 offen offset:0 lds\n"
        "  buffer_load_dwordx4 v173, s[20:23], s61 offen offset:1024 lds\n"
        "  buffer_load_dwordx4 v176, s[20:23], s61 offen offset:2048 lds\n"
        "  buffer_load_dwordx4 v177, s[20:23], s61 offen offset:3072 lds\n"
        "  s_add_u32 s61, s61, 25088\n"
        "  s_add_u32 m0, s59, 8192\n"
        "  s_nop 0  ; hazard\n"
        "  buffer_load_dwordx4 v172, s[20:23], s61 offen offset:0 lds\n"
        "  buffer_load_dwordx4 v173, s[20:23], s61 offen offset:1024 lds\n"
        "  buffer_load_dwordx4 v176, s[20:23], s61 offen offset:2048 lds\n"
        "  buffer_load_dwordx4 v177, s[20:23], s61 offen offset:3072 lds\n"
        "  s_add_u32 s61, s61, 25088\n"
        "  s_mov_b64 exec, s[80:81]\n"
        "  s_waitcnt vmcnt(12)\n"
        ".Lchk:\n"
        "  v_cmp_ne_u32 vcc, v188, v186\n"
        "  s_nop 0  ; hazard\n"
        "  s_and_b64 s[92:93], vcc, exec\n"
        "  s_cbranch_scc1 .Lslow\n"
        ".Lgo:\n"
        "  s_mov_b64 exec, -1\n"
        "  s_mov_b32 s87, 262144\n"
        "  s_waitcnt lgkmcnt(0)\n"
        "  buffer_load_dwordx4 v[64:67], v196, s[24:27], s48 offen sc1\n"
        "  buffer_load_dwordx4 v[68:71], v197, s[24:27], s48 offen sc1\n"
        "  s_lshl_b32 s49, s85, 12\n"
        "  s_add_u32 s49, s49, s62\n"
        "  s_add_u32 s85, s85, 1\n"
        "  s_cmp_eq_u32 s85, 14\n"
        "  s_cselect_b32 s85, 0, s85\n"
        "  buffer_load_dwordx4 v[72:75], v198, s[24:27], s48 offen sc1\n"
        "  buffer_load_dwordx4 v[76:79], v199, s[24:27], s48 offen sc1\n"
        "  buffer_load_dwordx4 v[80:83], v196, s[24:27], s49 offen sc1\n"
        "  buffer_load_dwordx4 v[84:87], v197, s[24:27], s49 offen sc1\n"
        "  s_lshl_b32 s48, s85, 12\n"
        "  s_add_u32 s48, s48, s62\n"
        "  s_add_u32 s85, s85, 1\n"
        "  s_cmp_eq_u32 s85, 14\n"
        "  s_cselect_b32 s85, 0, s85\n"
        "  buffer_load_dwordx4 v[88:91], v198, s[24:27], s49 offen sc1\n"
        "  buffer_load_dwordx4 v[92:95], v199, s[24:27], s49 offen sc1\n"
        "  s_mov_b64 exec, s[76:77]\n"
        "  buffer_store_dword v189, v185, s[24:27], 0 offen offset:4 sc1\n"
        "  s_mov_b64 exec, -1\n"
        "  buffer_load_dwordx4 v[96:99], v196, s[24:27], s48 offen sc1\n"
        "  buffer_load_dwordx4 v[100:103], v197, s[24:27], s48 offen sc1\n"
        "  s_lshl_b32 s49, s85, 12\n"
        "  s_add_u32 s49, s49, s62\n"
        "  s_add_u32 s85, s85, 1\n"
        "  s_cmp_eq_u32 s85, 14\n"
        "  s_cselect_b32 s85, 0, s85\n"
        "  buffer_load_dwordx4 v[104:107], v198, s[24:27], s48 offen sc1\n"
        "  buffer_load_dwordx4 v[108:111], v199, s[24:27], s48 offen sc1\n"
        "  s_waitcnt vmcnt(12)  ; auto\n"
        "  v_mfma_f32_16x16x32_bf16 v[0:3], a[96:99], v[64:67], 0\n"
        "  v_mfma_f32_16x16x32_bf16 v[4:7], a[100:103], v[64:67], 0\n"
        "  buffer_load_dwordx4 v[112:115], v196, s[24:27], s49 offen sc1\n"
        "  v_mfma_f32_16x16x32_bf16 v[8:11], a[104:107], v[64:67], 0\n"
        "  v_mfma_f32_16x16x32_bf16 v[12:15], a[108:111], v[64:67], 0\n"
        "  s_waitcnt vmcnt(12)  ; auto\n"
        "  v_mfma_f32_16x16x32_bf16 v[16:19], a[96:99], v[68:71], 0\n"
        "  v_mfma_f32_16x16x32_bf16 v[20:23], a[100:103], v[68:71], 0\n"
        "  buffer_load_dwordx4 v[116:119], v197, s[24:27], s49 offen sc1\n"
        "  v_mfma_f32_16x16x32_bf16 v[24:27], a[104:107], v[68:71], 0\n"
        "  s_lshl_b32 s48, s85, 12\n"
        "  s_add_u32 s48, s48, s62\n"
        "  v_mfma_f32_16x16x32_bf16 v[28:31], a[108:111], v[68:71], 0\n"
        "  s_add_u32 s85, s85, 1\n"
        "  s_cmp_eq_u32 s85, 14\n"
        "  s_cselect_b32 s85, 0, s85\n"
        "  s_waitcnt vmcnt(12)  ; auto\n"
        "  v_mfma_f32_16x16x32_bf16 v[32:35], a[96:99], v[72:75], 0\n"
        "  v_mfma_f32_16x16x32_bf16 v[36:39], a[100:103], v[72:75], 0\n"
        "  buffer_load_dwordx4 v[120:123], v198, s[24:27], s49 offen sc1\n"
        "  v_mfma_f32_16x16x32_bf16 v[40:43], a[104:107], v[72:75], 0\n"
        "  v_mfma_f32_16x16x32_bf16 v[44:47], a[108:111], v[72:75], 0\n"
        "  s_waitcnt vmcnt(12)  ; auto\n"
        "  v_mfma_f32_16x16x32_bf16 v[48:51], a[96:99], v[76:79], v[208:211]\n"
        "  v_mfma_f32_16x16x32_bf16 v[52:55], a[100:103], v[76:79], v[212:215]\n"
        "  buffer_load_dwordx4 v[124:127], v199, s[24:27], s49 offen sc1\n"
        "  v_mfma_f32_16x16x32_bf16 v[56:59], a[104:107], v[76:79], v[216:219]\n"
        "  v_mfma_f32_16x16x32_bf16 v[60:63], a[108:111], v[76:79], v[220:223]\n"
        "  s_waitcnt vmcnt(12)  ; auto\n"
        "  v_mfma_f32_16x16x32_bf16 v[0:3], a[112:115], v[80:83], v[0:3]\n"
        "  v_mfma_f32_16x16x32_bf16 v[4:7], a[116:119], v[80:83], v[4:7]\n"
        "  buffer_load_dwordx4 v[128:131], v196, s[24:27], s48 offen sc1\n"
        "  v_mfma_f32_16x16x32_bf16 v[8:11], a[120:123], v[80:83], v[8:11]\n"
        "  v_mfma_f32_16x16x32_bf16 v[12:15], a[124:127], v[80:83], v[12:15]\n"
        "  s_waitcnt vmcnt(12)  ; auto\n"
        "  v_mfma_f32_16x16x32_bf16 v[16:19], a[112:115], v[84:87], v[16:19]\n"
        "  v_mfma_f32_16x16x32_bf16 v[20:23], a[116:119], v[84:87], v[20:23]\n"
        "  buffer_load_dwordx4 v[132:135], v197, s[24:27], s48 offen sc1\n"
        "  v_mfma_f32_16x16x32_bf16 v[24:27], a[120:123], v[84:87], v[24:27]\n"
        "  s_lshl_b32 s49, s85, 12\n"
        "  s_add_u32 s49, s49, s62\n"
        "  v_mfma_f32_16x16x32_bf16 v[28:31], a[124:127], v[84:87], v[28:31]\n"
        "  s_add_u32 s85, s85, 1\n"
        "  s_cmp_eq_u32 s85, 14\n"
        "  s_cselect_b32 s85, 0, s85\n"
        "  s_waitcnt vmcnt(12)  ; auto\n"
        "  v_mfma_f32_16x16x32_bf16 v[32:35], a[112:115], v[88:91], v[32:35]\n"
        "  v_mfma_f32_16x16x32_bf16 v[36:39], a[116:119], v[88:91], v[36:39]\n"
        "  buffer_load_dwordx4 v[136:139], v198, s[24:27], s48 offen sc1\n"
        "  v_mfma_f32_16x16x32_bf16 v[40:43], a[120:123], v[88:91], v[40:43]\n"
        "  v_mfma_f32_16x16x32_bf16 v[44:47], a[124:127], v[88:91], v[44:47]\n"
        "  s_waitcnt vmcnt(12)  ; auto\n"
        "  v_mfma_f32_16x16x32_bf16 v[48:51], a[112:115], v[92:95], v[48:51]\n"
        "  v_mfma_f32_16x16x32_bf16 v[52:55], a[116:119], v[92:95], v[52:55]\n"
        "  buffer_load_dwordx4 v[140:143], v199, s[24:27], s48 offen sc1\n"
        "  v_mfma_f32_16x16x32_bf16 v[56:59], a[120:123], v[92:95], v[56:59]\n"
        "  v_mfma_f32_16x16x32_bf16 v[60:63], a[124:127], v[92:95], v[60:63]\n"
        "  s_waitcnt vmcnt(11)  ; auto\n"
        "  v_mfma_f32_16x16x32_bf16 v[0:3], a[128:131], v[96:99], v[0:3]\n"
        "  v_mfma_f32_16x16x32_bf16 v[4:7], a[132:135], v[96:99], v[4:7]\n"
        "  buffer_load_dwordx4 v[144:147], v196, s[24:27], s49 offen sc1\n"
        "  v_mfma_f32_16x16x32_bf16 v[8:11], a[136:139], v[96:99], v[8:11]\n"
        "  v_mfma_f32_16x16x32_bf16 v[12:15], a[140:143], v[96:99], v[12:15]\n"
        "  s_waitcnt vmcnt(11)  ; auto\n"
        "  v_mfma_f32_16x16x32_bf16 v[16:19], a[128:131], v[100:103], v[16:19]\n"
        "  v_mfma_f32_16x16x32_bf16 v[20:23], a[132:135], v[100:103], v[20:23]\n"
        "  buffer_load_dwordx4 v[148:151], v197, s[24:27], s49 offen sc1\n"
        "  v_mfma_f32_16x16x32_bf16 v[24:27], a[136:139], v[100:103], v[24:27]\n"
        "  s_lshl_b32 s48, s85, 12\n"
        "  s_add_u32 s48, s48, s62\n"
        "  v_mfma_f32_16x16x32_bf16 v[28:31], a[140:143], v[100:103], v[28:31]\n"
        "  s_add_u32 s85, s85, 1\n"
        "  s_cmp_eq_u32 s85, 14\n"
        "  s_cselect_b32 s85, 0, s85\n"
        "  s_waitcnt vmcnt(11)  ; auto\n"
        "  v_mfma_f32_16x16x32_bf16 v[32:35], a[128:131], v[104:107], v[32:35]\n"
        "  v_mfma_f32_16x16x32_bf16 v[36:39], a[132:135], v[104:107], v[36:39]\n"
        "  buffer_load_dwordx4 v[152:155], v198, s[24:27], s49 offen sc1\n"
        "  v_mfma_f32_16x16x32_bf16 v[40:43], a[136:139], v[104:107], v[40:43]\n"
        "  v_mfma_f32_16x16x32_bf16 v[44:47], a[140:143], v[104:107], v[44:47]\n"
        "  s_waitcnt vmcnt(11)  ; auto\n"
        "  v_mfma_f32_16x16x32_bf16 v[48:51], a[128:131], v[108:111], v[48:51]\n"
        "  v_mfma_f32_16x16x32_bf16 v[52:55], a[132:135], v[108:111], v[52:55]\n"
        "  buffer_load_dwordx4 v[156:159], v199, s[24:27], s49 offen sc1\n"
        "  v_mfma_f32_16x16x32_bf16 v[56:59], a[136:139], v[108:111], v[56:59]\n"
        "  v_mfma_f32_16x16x32_bf16 v[60:63], a[140:143], v[108:111], v[60:63]\n"
        "  s_waitcnt vmcnt(11)  ; auto\n"
        "  v_mfma_f32_16x16x32_bf16 v[0:3], a[144:147], v[112:115], v[0:3]\n"
        "  v_mfma_f32_16x16x32_bf16 v[4:7], a[148:151], v[112:115], v[4:7]\n"
        "  buffer_load_dwordx4 v[64:67], v196, s[24:27], s48 offen sc1\n"
        "  v_mfma_f32_16x16x32_bf16 v[8:11], a[152:155], v[112:115], v[8:11]\n"
        "  v_mfma_f32_16x16x32_bf16 v[12:15], a[156:159], v[112:115], v[12:15]\n"
        "  s_waitcnt vmcnt(11)  ; auto\n"
        "  v_mfma_f32_16x16x32_bf16 v[16:19], a[144:147], v[116:119], v[16:19]\n"
        "  v_mfma_f32_16x16x32_bf16 v[20:23], a[148:151], v[116:119], v[20:23]\n"
        "  buffer_load_dwordx4 v[68:71], v197, s[24:27], s48 offen sc1\n"
        "  v_mfma_f32_16x16x32_bf16 v[24:27], a[152:155], v[116:119], v[24:27]\n"
        "  s_lshl_b32 s49, s85, 12\n"
        "  s_add_u32 s49, s49, s62\n"
        "  v_mfma_f32_16x16x32_bf16 v[28:31], a[156:159], v[116:119], v[28:31]\n"
        "  s_add_u32 s85, s85, 1\n"
        "  s_cmp_eq_u32 s85, 14\n"
        "  s_cselect_b32 s85, 0, s85\n"
        "  s_waitcnt vmcnt(11)  ; auto\n"
        "  v_mfma_f32_16x16x32_bf16 v[32:35], a[144:147], v[120:123], v[32:35]\n"
        "  v_mfma_f32_16x16x32_bf16 v[36:39], a[148:151], v[120:123], v[36:39]\n"
        "  buffer_load_dwordx4 v[72:75], v198, s[24:27], s48 offen sc1\n"
        "  v_mfma_f32_16x16x32_bf16 v[40:43], a[152:155], v[120:123], v[40:43]\n"
        "  v_mfma_f32_16x16x32_bf16 v[44:47], a[156:159], v[120:123], v[44:47]\n"
        "  s_waitcnt vmcnt(11)  ; auto\n"
        "  v_mfma_f32_16x16x32_bf16 v[48:51], a[144:147], v[124:127], v[48:51]\n"
        "  v_mfma_f32_16x16x32_bf16 v[52:55], a[148:151], v[124:127], v[52:55]\n"
        "  buffer_load_dwordx4 v[76:79], v199, s[24:27], s48 offen sc1\n"
        "  v_mfma_f32_16x16x32_bf16 v[56:59], a[152:155], v[124:127], v[56:59]\n"
        "  v_mfma_f32_16x16x32_bf16 v[60:63], a[156:159], v[124:127], v[60:63]\n"
        "  s_waitcnt vmcnt(11)  ; auto\n"
        "  v_mfma_f32_16x16x32_bf16 v[0:3], a[160:163], v[128:131], v[0:3]\n"
        "  s_add_u32 s54, s59, 49152\n"
        "  v_add_u32 v224, s54, v182\n"
        "  v_mfma_f32_16x16x32_bf16 v[4:7], a[164:167], v[128:131], v[4:7]\n"
        "  buffer_load_dwordx4 v[80:83], v196, s[24:27], s49 offen sc1\n"
        "  s_add_u32 s54, s40, 1\n"
        "  v_mfma_f32_16x16x32_bf16 v[8:11], a[168:171], v[128:131], v[8:11]\n"
        "  s_and_b32 s54, s54, 3\n"
        "  s_mul_i32 s54, s54, 12288\n"
        "  v_mfma_f32_16x16x32_bf16 v[12:15], a[172:175], v[128:131], v[12:15]\n"
        "  v_add_u32 v225, s54, v182\n"
        "  s_add_u32 s54, s40, 2\n"
        "  s_waitcnt vmcnt(11)  ; auto\n"
        "  v_mfma_f32_16x16x32_bf16 v[16:19], a[160:163], v[132:135], v[16:19]\n"
        "  s_and_b32 s54, s54, 3\n"
        "  s_mul_i32 s54, s54, 12288\n"
        "  v_mfma_f32_16x16x32_bf16 v[20:23], a[164:167], v[132:135], v[20:23]\n"
        "  buffer_load_dwordx4 v[84:87], v197, s[24:27], s49 offen sc1\n"
        "  v_add_u32 v226, s54, v182\n"
        "  v_mfma_f32_16x16x32_bf16 v[24:27], a[168:171], v[132:135], v[24:27]\n"
        "  s_lshl_b32 s48, s85, 12\n"
        "  s_add_u32 s48, s48, s62\n"
        "  v_mfma_f32_16x16x32_bf16 v[28:31], a[172:175], v[132:135], v[28:31]\n"
        "  s_add_u32 s85, s85, 1\n"
        "  s_cmp_eq_u32 s85, 14\n"
        "  s_cselect_b32 s85, 0, s85\n"
        "  s_waitcnt vmcnt(11)  ; auto\n"
        "  v_mfma_f32_16x16x32_bf16 v[32:35], a[160:163], v[136:139], v[32:35]\n"
        "  s_add_u32 s54, s40, 3\n"
        "  s_and_b32 s54, s54, 3\n"
        "  v_mfma_f32_16x16x32_bf16 v[36:39], a[164:167], v[136:139], v[36:39]\n"
        "  buffer_load_dwordx4 v[88:91], v198, s[24:27], s49 offen sc1\n"
        "  s_mul_i32 s54, s54, 12288\n"
        "  v_mfma_f32_16x16x32_bf16 v[40:43], a[168:171], v[136:139], v[40:43]\n"
        "  v_add_u32 v227, s54, v182\n"
        "  v_mfma_f32_16x16x32_bf16 v[44:47], a[172:175], v[136:139], v[44:47]\n"
        "  s_waitcnt vmcnt(11)  ; auto\n"
        "  v_mfma_f32_16x16x32_bf16 v[48:51], a[160:163], v[140:143], v[48:51]\n"
        "  v_mfma_f32_16x16x32_bf16 v[52:55], a[164:167], v[140:143], v[52:55]\n"
        "  buffer_load_dwordx4 v[92:95], v199, s[24:27], s49 offen sc1\n"
        "  v_mfma_f32_16x16x32_bf16 v[56:59], a[168:171], v[140:143], v[56:59]\n"
        "  v_mfma_f32_16x16x32_bf16 v[60:63], a[172:175], v[140:143], v[60:63]\n"
        "  s_waitcnt vmcnt(11)  ; auto\n"
        "  v_mfma_f32_16x16x32_bf16 v[0:3], a[176:179], v[144:147], v[0:3]\n"
        "  v_mfma_f32_16x16x32_bf16 v[4:7], a[180:183], v[144:147], v[4:7]\n"
        "  buffer_load_dwordx4 v[96:99], v196, s[24:27], s48 offen sc1\n"
        "  v_mfma_f32_16x16x32_bf16 v[8:11], a[184:187], v[144:147], v[8:11]\n"
        "  v_mfma_f32_16x16x32_bf16 v[12:15], a[188:191], v[144:147], v[12:15]\n"
        "  s_waitcnt vmcnt(11)  ; auto\n"
        "  v_mfma_f32_16x16x32_bf16 v[16:19], a[176:179], v[148:151], v[16:19]\n"
        "  v_mfma_f32_16x16x32_bf16 v[20:23], a[180:183], v[148:151], v[20:23]\n"
        "  buffer_load_dwordx4 v[100:103], v197, s[24:27], s48 offen sc1\n"
        "  v_mfma_f32_16x16x32_bf16 v[24:27], a[184:187], v[148:151], v[24:27]\n"
        "  s_lshl_b32 s49, s85, 12\n"
        "  s_add_u32 s49, s49, s62\n"
        "  v_mfma_f32_16x16x32_bf16 v[28:31], a[188:191], v[148:151], v[28:31]\n"
        "  s_add_u32 s85, s85, 1\n"
        "  s_cmp_eq_u32 s85, 14\n"
        "  s_cselect_b32 s85, 0, s85\n"
        "  s_waitcnt vmcnt(11)  ; auto\n"
        "  v_mfma_f32_16x16x32_bf16 v[32:35], a[176:179], v[152:155], v[32:35]\n"
        "  v_mfma_f32_16x16x32_bf16 v[36:39], a[180:183], v[152:155], v[36:39]\n"
        "  buffer_load_dwordx4 v[104:107], v198, s[24:27], s48 offen sc1\n"
        "  v_mfma_f32_16x16x32_bf16 v[40:43], a[184:187], v[152:155], v[40:43]\n"
        "  v_mfma_f32_16x16x32_bf16 v[44:47], a[188:191], v[152:155], v[44:47]\n"
        "  s_waitcnt vmcnt(11)  ; auto\n"
        "  v_mfma_f32_16x16x32_bf16 v[48:51], a[176:179], v[156:159], v[48:51]\n"
        "  v_mfma_f32_16x16x32_bf16 v[52:55], a[180:183], v[156:159], v[52:55]\n"
        "  buffer_load_dwordx4 v[108:111], v199, s[24:27], s48 offen sc1\n"
        "  v_mfma_f32_16x16x32_bf16 v[56:59], a[184:187], v[156:159], v[56:59]\n"
        "  v_mfma_f32_16x16x32_bf16 v[60:63], a[188:191], v[156:159], v[60:63]\n"
        "  s_waitcnt vmcnt(11)  ; auto\n"
        "  v_mfma_f32_16x16x32_bf16 v[0:3], a[192:195], v[64:67], v[0:3]\n"
        "  ds_read_b64_tr_b16 a[96:97], v178 offset:49152\n"
        "  v_mfma_f32_16x16x32_bf16 v[4:7], a[196:199], v[64:67], v[4:7]\n"
        "  buffer_load_dwordx4 v[112:115], v196, s[24:27], s49 offen sc1\n"
        "  v_mfma_f32_16x16x32_bf16 v[8:11], a[200:203], v[64:67], v[8:11]\n"
        "  ds_read_b64_tr_b16 a[98:99], v178 offset:51200\n"
        "  v_mfma_f32_16x16x32_bf16 v[12:15], a[204:207], v[64:67], v[12:15]\n"
        "  ds_read_b64_tr_b16 a[100:101], v179 offset:49152\n"
        "  s_waitcnt vmcnt(11)  ; auto\n"
        "  v_mfma_f32_16x16x32_bf16 v[16:19], a[192:195], v[68:71], v[16:19]\n"
        "  ds_read_b64_tr_b16 a[102:103], v179 offset:51200\n"
        "  v_mfma_f32_16x16x32_bf16 v[20:23], a[196:199], v[68:71], v[20:23]\n"
        "  buffer_load_dwordx4 v[116:119], v197, s[24:27], s49 offen sc1\n"
        "  v_mfma_f32_16x16x32_bf16 v[24:27], a[200:203], v[68:71], v[24:27]\n"
        "  s_lshl_b32 s48, s85, 12\n"
        "  s_add_u32 s48, s48, s62\n"
        "  ds_read_b64_tr_b16 a[104:105], v180 offset:49152\n"
        "  v_mfma_f32_16x16x32_bf16 v[28:31], a[204:207], v[68:71], v[28:31]\n"
        "  s_add_u32 s85, s85, 1\n"
        "  s_cmp_eq_u32 s85, 14\n"
        "  s_cselect_b32 s85, 0, s85\n"
        "  ds_read_b64_tr_b16 a[106:107], v180 offset:51200\n"
        "  s_waitcnt vmcnt(11)  ; auto\n"
        "  v_mfma_f32_16x16x32_bf16 v[32:35], a[192:195], v[72:75], v[32:35]\n"
        "  ds_read_b64_tr_b16 a[108:109], v181 offset:49152\n"
        "  v_mfma_f32_16x16x32_bf16 v[36:39], a[196:199], v[72:75], v[36:39]\n"
        "  buffer_load_dwordx4 v[120:123], v198, s[24:27], s49 offen sc1\n"
        "  v_mfma_f32_16x16x32_bf16 v[40:43], a[200:203], v[72:75], v[40:43]\n"
        "  ds_read_b64_tr_b16 a[110:111], v181 offset:51200\n"
        "  v_mfma_f32_16x16x32_bf16 v[44:47], a[204:207], v[72:75], v[44:47]\n"
        "  s_waitcnt vmcnt(11)  ; auto\n"
        "  v_mfma_f32_16x16x32_bf16 v[48:51], a[192:195], v[76:79], v[48:51]\n"
        "  v_mfma_f32_16x16x32_bf16 v[52:55], a[196:199], v[76:79], v[52:55]\n"
        "  buffer_load_dwordx4 v[124:127], v199, s[24:27], s49 offen sc1\n"
        "  v_mfma_f32_16x16x32_bf16 v[56:59], a[200:203], v[76:79], v[56:59]\n"
        "  v_mfma_f32_16x16x32_bf16 v[60:63], a[204:207], v[76:79], v[60:63]\n"
        "  s_waitcnt vmcnt(11)  ; auto\n"
        "  v_mfma_f32_16x16x32_bf16 v[0:3], a[208:211], v[80:83], v[0:3]\n"
        "  ds_read_b64_tr_b16 a[112:113], v178 offset:53248\n"
        "  v_mfma_f32_16x16x32_bf16 v[4:7], a[212:215], v[80:83], v[4:7]\n"
        "  buffer_load_dwordx4 v[128:131], v196, s[24:27], s48 offen sc1\n"
        "  v_mfma_f32_16x16x32_bf16 v[8:11], a[216:219], v[80:83], v[8:11]\n"
        "  ds_read_b64_tr_b16 a[114:115], v178 offset:55296\n"
        "  v_mfma_f32_16x16x32_bf16 v[12:15], a[220:223], v[80:83], v[12:15]\n"
        "  ds_read_b64_tr_b16 a[116:117], v179 offset:53248\n"
        "  s_waitcnt vmcnt(11)  ; auto\n"
        "  v_mfma_f32_16x16x32_bf16 v[16:19], a[208:211], v[84:87], v[16:19]\n"
        "  ds_read_b64_tr_b16 a[118:119], v179 offset:55296\n"
        "  v_mfma_f32_16x16x32_bf16 v[20:23], a[212:215], v[84:87], v[20:23]\n"
        "  buffer_load_dwordx4 v[132:135], v197, s[24:27], s48 offen sc1\n"
        "  v_mfma_f32_16x16x32_bf16 v[24:27], a[216:219], v[84:87], v[24:27]\n"
        "  s_lshl_b32 s49, s85, 12\n"
        "  s_add_u32 s49, s49, s62\n"
        "  ds_read_b64_tr_b16 a[120:121], v180 offset:53248\n"
        "  v_mfma_f32_16x16x32_bf16 v[28:31], a[220:223], v[84:87], v[28:31]\n"
        "  s_add_u32 s85, s85, 1\n"
        "  s_cmp_eq_u32 s85, 14\n"
        "  s_cselect_b32 s85, 0, s85\n"
        "  ds_read_b64_tr_b16 a[122:123], v180 offset:55296\n"
        "  s_waitcnt vmcnt(11)  ; auto\n"
        "  v_mfma_f32_16x16x32_bf16 v[32:35], a[208:211], v[88:91], v[32:35]\n"
        "  ds_read_b64_tr_b16 a[124:125], v181 offset:53248\n"
        "  v_mfma_f32_16x16x32_bf16 v[36:39], a[212:215], v[88:91], v[36:39]\n"
        "  buffer_load_dwordx4 v[136:139], v198, s[24:27], s48 offen sc1\n"
        "  v_mfma_f32_16x16x32_bf16 v[40:43], a[216:219], v[88:91], v[40:43]\n"
        "  ds_read_b64_tr_b16 a[126:127], v181 offset:55296\n"
        "  v_mfma_f32_16x16x32_bf16 v[44:47], a[220:223], v[88:91], v[44:47]\n"
        "  s_waitcnt vmcnt(11)  ; auto\n"
        "  v_mfma_f32_16x16x32_bf16 v[48:51], a[208:211], v[92:95], v[48:51]\n"
        "  v_mfma_f32_16x16x32_bf16 v[52:55], a[212:215], v[92:95], v[52:55]\n"
        "  buffer_load_dwordx4 v[140:143], v199, s[24:27], s48 offen sc1\n"
        "  v_mfma_f32_16x16x32_bf16 v[56:59], a[216:219], v[92:95], v[56:59]\n"
        "  v_mfma_f32_16x16x32_bf16 v[60:63], a[220:223], v[92:95], v[60:63]\n"
        "  s_waitcnt vmcnt(11)  ; auto\n"
        "  v_mfma_f32_16x16x32_bf16 v[0:3], a[224:227], v[96:99], v[0:3]\n"
        "  ds_read_b64_tr_b16 a[128:129], v178 offset:57344\n"
        "  v_mfma_f32_16x16x32_bf16 v[4:7], a[228:231], v[96:99], v[4:7]\n"
        "  buffer_load_dwordx4 v[144:147], v196, s[24:27], s49 offen sc1\n"
        "  v_mfma_f32_16x16x32_bf16 v[8:11], a[232:235], v[96:99], v[8:11]\n"
        "  ds_read_b64_tr_b16 a[130:131], v178 offset:59392\n"
        "  v_mfma_f32_16x16x32_bf16 v[12:15], a[236:239], v[96:99], v[12:15]\n"
        "  ds_read_b64_tr_b16 a[132:133], v179 offset:57344\n"
        "  s_waitcnt vmcnt(11)  ; auto\n"
        "  v_mfma_f32_16x16x32_bf16 v[16:19], a[224:227], v[100:103], v[16:19]\n"
        "  ds_read_b64_tr_b16 a[134:135], v179 offset:59392\n"
        "  v_mfma_f32_16x16x32_bf16 v[20:23], a[228:231], v[100:103], v[20:23]\n"
        "  buffer_load_dwordx4 v[148:151], v197, s[24:27], s49 offen sc1\n"
        "  v_mfma_f32_16x16x32_bf16 v[24:27], a[232:235], v[100:103], v[24:27]\n"
        "  s_lshl_b32 s48, s85, 12\n"
        "  s_add_u32 s48, s48, s62\n"
        "  ds_read_b64_tr_b16 a[136:137], v180 offset:57344\n"
        "  v_mfma_f32_16x16x32_bf16 v[28:31], a[236:239], v[100:103], v[28:31]\n"
        "  s_add_u32 s85, s85, 1\n"
        "  s_cmp_eq_u32 s85, 14\n"
        "  s_cselect_b32 s85, 0, s85\n"
        "  ds_read_b64_tr_b16 a[138:139], v180 offset:59392\n"
        "  s_waitcnt vmcnt(11)  ; auto\n"
        "  v_mfma_f32_16x16x32_bf16 v[32:35], a[224:227], v[104:107], v[32:35]\n"
        "  ds_read_b64_tr_b16 a[140:141], v181 offset:57344\n"
        "  v_mfma_f32_16x16x32_bf16 v[36:39], a[228:231], v[104:107], v[36:39]\n"
        "  buffer_load_dwordx4 v[152:155], v198, s[24:27], s49 offen sc1\n"
        "  v_mfma_f32_16x16x32_bf16 v[40:43], a[232:235], v[104:107], v[40:43]\n"
        "  ds_read_b64_tr_b16 a[142:143], v181 offset:59392\n"
        "  v_mfma_f32_16x16x32_bf16 v[44:47], a[236:239], v[104:107], v[44:47]\n"
        "  s_waitcnt vmcnt(11)  ; auto\n"
        "  v_mfma_f32_16x16x32_bf16 v[48:51], a[224:227], v[108:111], v[48:51]\n"
        "  v_mfma_f32_16x16x32_bf16 v[52:55], a[228:231], v[108:111], v[52:55]\n"
        "  buffer_load_dwordx4 v[156:159], v199, s[24:27], s49 offen sc1\n"
        "  v_mfma_f32_16x16x32_bf16 v[56:59], a[232:235], v[108:111], v[56:59]\n"
        "  v_mfma_f32_16x16x32_bf16 v[60:63], a[236:239], v[108:111], v[60:63]\n"
        "  s_waitcnt vmcnt(11) lgkmcnt(15)  ; auto\n"
        "  v_mfma_f32_16x16x32_bf16 v[0:3], a[96:99], v[112:115], v[0:3]\n"
        "  ds_read_b64_tr_b16 a[144:145], v168 offset:32768\n"
        "  v_mfma_f32_16x16x32_bf16 v[4:7], a[100:103], v[112:115], v[4:7]\n"
        "  buffer_load_dwordx4 v[64:67], v196, s[24:27], s48 offen sc1\n"
        "  v_mfma_f32_16x16x32_bf16 v[8:11], a[104:107], v[112:115], v[8:11]\n"
        "  ds_read_b64_tr_b16 a[146:147], v168 offset:34816\n"
        "  v_mfma_f32_16x16x32_bf16 v[12:15], a[108:111], v[112:115], v[12:15]\n"
        "  ds_read_b64_tr_b16 a[148:149], v169 offset:32768\n"
        "  s_waitcnt vmcnt(11)  ; auto\n"
        "  v_mfma_f32_16x16x32_bf16 v[16:19], a[96:99], v[116:119], v[16:19]\n"
        "  ds_read_b64_tr_b16 a[150:151], v169 offset:34816\n"
        "  v_mfma_f32_16x16x32_bf16 v[20:23], a[100:103], v[116:119], v[20:23]\n"
        "  buffer_load_dwordx4 v[68:71], v197, s[24:27], s48 offen sc1\n"
        "  v_mfma_f32_16x16x32_bf16 v[24:27], a[104:107], v[116:119], v[24:27]\n"
        "  s_lshl_b32 s49, s85, 12\n"
        "  s_add_u32 s49, s49, s62\n"
        "  ds_read_b64_tr_b16 a[152:153], v170 offset:32768\n"
        "  v_mfma_f32_16x16x32_bf16 v[28:31], a[108:111], v[116:119], v[28:31]\n"
        "  s_add_u32 s85, s85, 1\n"
        "  s_cmp_eq_u32 s85, 14\n"
        "  s_cselect_b32 s85, 0, s85\n"
        "  ds_read_b64_tr_b16 a[154:155], v170 offset:34816\n"
        "  s_waitcnt vmcnt(11)  ; auto\n"
        "  v_mfma_f32_16x16x32_bf16 v[32:35], a[96:99], v[120:123], v[32:35]\n"
        "  ds_read_b64_tr_b16 a[156:157], v183 offset:32768\n"
        "  v_mfma_f32_16x16x32_bf16 v[36:39], a[100:103], v[120:123], v[36:39]\n"
        "  buffer_load_dwordx4 v[72:75], v198, s[24:27], s48 offen sc1\n"
        "  v_mfma_f32_16x16x32_bf16 v[40:43], a[104:107], v[120:123], v[40:43]\n"
        "  ds_read_b64_tr_b16 a[158:159], v183 offset:34816\n"
        "  v_mfma_f32_16x16x32_bf16 v[44:47], a[108:111], v[120:123], v[44:47]\n"
        "  s_waitcnt vmcnt(11)  ; auto\n"
        "  v_mfma_f32_16x16x32_bf16 v[48:51], a[96:99], v[124:127], v[48:51]\n"
        "  v_mfma_f32_16x16x32_bf16 v[52:55], a[100:103], v[124:127], v[52:55]\n"
        "  buffer_load_dwordx4 v[76:79], v199, s[24:27], s48 offen sc1\n"
        "  v_mfma_f32_16x16x32_bf16 v[56:59], a[104:107], v[124:127], v[56:59]\n"
        "  v_mfma_f32_16x16x32_bf16 v[60:63], a[108:111], v[124:127], v[60:63]\n"
        "  s_waitcnt vmcnt(11) lgkmcnt(15)  ; auto\n"
        "  v_mfma_f32_16x16x32_bf16 v[0:3], a[112:115], v[128:131], v[0:3]\n"
        "  ds_read_b64_tr_b16 a[160:161], v168 offset:36864\n"
        "  v_mfma_f32_16x16x32_bf16 v[4:7], a[116:119], v[128:131], v[4:7]\n"
        "  buffer_load_dwordx4 v[80:83], v196, s[24:27], s49 offen sc1\n"
        "  v_mfma_f32_16x16x32_bf16 v[8:11], a[120:123], v[128:131], v[8:11]\n"
        "  ds_read_b64_tr_b16 a[162:163], v168 offset:38912\n"
        "  v_mfma_f32_16x16x32_bf16 v[12:15], a[124:127], v[128:131], v[12:15]\n"
        "  ds_read_b64_tr_b16 a[164:165], v169 offset:36864\n"
        "  s_waitcnt vmcnt(11)  ; auto\n"
        "  v_mfma_f32_16x16x32_bf16 v[16:19], a[112:115], v[132:135], v[16:19]\n"
        "  ds_read_b64_tr_b16 a[166:167], v169 offset:38912\n"
        "  v_mfma_f32_16x16x32_bf16 v[20:23], a[116:119], v[132:135], v[20:23]\n"
        "  buffer_load_dwordx4 v[84:87], v197, s[24:27], s49 offen sc1\n"
        "  v_mfma_f32_16x16x32_bf16 v[24:27], a[120:123], v[132:135], v[24:27]\n"
        "  s_add_u32 s50, s62, 61440\n"
        "  s_add_u32 s51, s62, 65536\n"
        "  ds_read_b64_tr_b16 a[168:169], v170 offset:36864\n"
        "  v_mfma_f32_16x16x32_bf16 v[28:31], a[124:127], v[132:135], v[28:31]\n"
        "  s_add_u32 s52, s62, 69632\n"
        "  s_add_u32 s62, s62, 57344\n"
        "  ds_read_b64_tr_b16 a[170:171], v170 offset:38912\n"
        "  s_waitcnt vmcnt(11)  ; auto\n"
        "  v_mfma_f32_16x16x32_bf16 v[32:35], a[112:115], v[136:139], v[32:35]\n"
        "  ds_read_b64_tr_b16 a[172:173], v183 offset:36864\n"
        "  v_mfma_f32_16x16x32_bf16 v[36:39], a[116:119], v[136:139], v[36:39]\n"
        "  buffer_load_dwordx4 v[88:91], v198, s[24:27], s49 offen sc1\n"
        "  v_mfma_f32_16x16x32_bf16 v[40:43], a[120:123], v[136:139], v[40:43]\n"
        "  ds_read_b64_tr_b16 a[174:175], v183 offset:38912\n"
        "  v_mfma_f32_16x16x32_bf16 v[44:47], a[124:127], v[136:139], v[44:47]\n"
        "  s_waitcnt vmcnt(11)  ; auto\n"
        "  v_mfma_f32_16x16x32_bf16 v[48:51], a[112:115], v[140:143], v[48:51]\n"
        "  v_mfma_f32_16x16x32_bf16 v[52:55], a[116:119], v[140:143], v[52:55]\n"
        "  buffer_load_dwordx4 v[92:95], v199, s[24:27], s49 offen sc1\n"
        "  v_mfma_f32_16x16x32_bf16 v[56:59], a[120:123], v[140:143], v[56:59]\n"
        "  v_mfma_f32_16x16x32_bf16 v[60:63], a[124:127], v[140:143], v[60:63]\n"
        "  s_waitcnt vmcnt(11) lgkmcnt(15)  ; auto\n"
        "  v_mfma_f32_16x16x32_bf16 v[0:3], a[128:131], v[144:147], v[0:3]\n"
        "  ds_read_b64_tr_b16 a[176:177], v168 offset:40960\n"
        "  v_mfma_f32_16x16x32_bf16 v[4:7], a[132:135], v[144:147], v[4:7]\n"
        "  buffer_load_dwordx4 v[96:99], v196, s[24:27], s62 offen sc1\n"
        "  v_mfma_f32_16x16x32_bf16 v[8:11], a[136:139], v[144:147], v[8:11]\n"
        "  ds_read_b64_tr_b16 a[178:179], v168 offset:43008\n"
        "  v_mfma_f32_16x16x32_bf16 v[12:15], a[140:143], v[144:147], v[12:15]\n"
        "  ds_read_b64_tr_b16 a[180:181], v169 offset:40960\n"
        "  s_waitcnt vmcnt(11)  ; auto\n"
        "  v_mfma_f32_16x16x32_bf16 v[16:19], a[128:131], v[148:151], v[16:19]\n"
        "  ds_read_b64_tr_b16 a[182:183], v169 offset:43008\n"
        "  v_mfma_f32_16x16x32_bf16 v[20:23], a[132:135], v[148:151], v[20:23]\n"
        "  buffer_load_dwordx4 v[100:103], v197, s[24:27], s62 offen sc1\n"
        "  v_mfma_f32_16x16x32_bf16 v[24:27], a[136:139], v[148:151], v[24:27]\n"
        "  ds_read_b64_tr_b16 a[184:185], v170 offset:40960\n"
        "  v_mfma_f32_16x16x32_bf16 v[28:31], a[140:143], v[148:151], v[28:31]\n"
        "  ds_read_b64_tr_b16 a[186:187], v170 offset:43008\n"
        "  s_waitcnt vmcnt(11)  ; auto\n"
        "  v_mfma_f32_16x16x32_bf16 v[32:35], a[128:131], v[152:155], v[32:35]\n"
        "  ds_read_b64_tr_b16 a[188:189], v183 offset:40960\n"
        "  v_mfma_f32_16x16x32_bf16 v[36:39], a[132:135], v[152:155], v[36:39]\n"
        "  buffer_load_dwordx4 v[104:107], v198, s[24:27], s62 offen sc1\n"
        "  v_mfma_f32_16x16x32_bf16 v[40:43], a[136:139], v[152:155], v[40:43]\n"
        "  ds_read_b64_tr_b16 a[190:191], v183 offset:43008\n"
        "  v_mfma_f32_16x16x32_bf16 v[44:47], a[140:143], v[152:155], v[44:47]\n"
        "  s_waitcnt vmcnt(11)  ; auto\n"
        "  v_mfma_f32_16x16x32_bf16 v[48:51], a[128:131], v[156:159], v[48:51]\n"
        "  v_mfma_f32_16x16x32_bf16 v[52:55], a[132:135], v[156:159], v[52:55]\n"
        "  buffer_load_dwordx4 v[108:111], v199, s[24:27], s62 offen sc1\n"
        "  v_mfma_f32_16x16x32_bf16 v[56:59], a[136:139], v[156:159], v[56:59]\n"
        "  v_mfma_f32_16x16x32_bf16 v[60:63], a[140:143], v[156:159], v[60:63]\n"
        "  s_waitcnt vmcnt(11) lgkmcnt(15)  ; auto\n"
        "  v_mfma_f32_16x16x32_bf16 v[0:3], a[144:147], v[64:67], v[0:3]\n"
        "  ds_read_b64_tr_b16 a[192:193], v178 offset:0\n"
        "  v_mfma_f32_16x16x32_bf16 v[4:7], a[148:151], v[64:67], v[4:7]\n"
        "  buffer_load_dwordx4 v[112:115], v196, s[24:27], s50 offen sc1\n"
        "  v_mfma_f32_16x16x32_bf16 v[8:11], a[152:155], v[64:67], v[8:11]\n"
        "  ds_read_b64_tr_b16 a[194:195], v178 offset:2048\n"
        "  v_mfma_f32_16x16x32_bf16 v[12:15], a[156:159], v[64:67], v[12:15]\n"
        "  s_mov_b64 exec, s[78:79]\n"
        "  v_mov_b32 v188, 0\n"
        "  v_mov_b32 v189, 0\n"
        "  buffer_atomic_or_x2 v[188:189], v184, s[24:27], 0 offen sc0 sc1\n"
        "  s_mov_b64 exec, -1\n"
        "  s_waitcnt vmcnt(12)  ; auto\n"
        "  v_mfma_f32_16x16x32_bf16 v[16:19], a[144:147], v[68:71], v[16:19]\n"
        "  ds_read_b64_tr_b16 a[196:197], v179 offset:0\n"
        "  v_mfma_f32_16x16x32_bf16 v[20:23], a[148:151], v[68:71], v[20:23]\n"
        "  buffer_load_dwordx4 v[128:131], v196, s[24:27], s51 offen sc1\n"
        "  v_mfma_f32_16x16x32_bf16 v[24:27], a[152:155], v[68:71], v[24:27]\n"
        "  ds_read_b64_tr_b16 a[198:199], v179 offset:2048\n"
        "  v_mfma_f32_16x16x32_bf16 v[28:31], a[156:159], v[68:71], v[28:31]\n"
        "  ds_read_b64_tr_b16 a[200:201], v180 offset:0\n"
        "  s_waitcnt vmcnt(12)  ; auto\n"
        "  v_mfma_f32_16x16x32_bf16 v[32:35], a[144:147], v[72:75], v[32:35]\n"
        "  ds_read_b64_tr_b16 a[202:203], v180 offset:2048\n"
        "  v_mfma_f32_16x16x32_bf16 v[36:39], a[148:151], v[72:75], v[36:39]\n"
        "  buffer_load_dwordx4 v[144:147], v196, s[24:27], s52 offen sc1\n"
        "  v_mfma_f32_16x16x32_bf16 v[40:43], a[152:155], v[72:75], v[40:43]\n"
        "  ds_read_b64_tr_b16 a[204:205], v181 offset:0\n"
        "  v_mfma_f32_16x16x32_bf16 v[44:47], a[156:159], v[72:75], v[44:47]\n"
        "  ds_read_b64_tr_b16 a[206:207], v181 offset:2048\n"
        "  s_waitcnt vmcnt(12)  ; auto\n"
        "  v_mfma_f32_16x16x32_bf16 v[48:51], a[144:147], v[76:79], v[48:51]\n"
        "  v_mfma_f32_16x16x32_bf16 v[52:55], a[148:151], v[76:79], v[52:55]\n"
        "  buffer_load_dwordx4 v[116:119], v197, s[24:27], s50 offen sc1\n"
        "  v_mfma_f32_16x16x32_bf16 v[56:59], a[152:155], v[76:79], v[56:59]\n"
        "  v_mfma_f32_16x16x32_bf16 v[60:63], a[156:159], v[76:79], v[60:63]\n"
        "  s_waitcnt vmcnt(12) lgkmcnt(15)  ; auto\n"
        "  v_mfma_f32_16x16x32_bf16 v[0:3], a[160:163], v[80:83], v[0:3]\n"
        "  ds_read_b64_tr_b16 a[208:209], v178 offset:4096\n"
        "  v_mfma_f32_16x16x32_bf16 v[4:7], a[164:167], v[80:83], v[4:7]\n"
        "  buffer_load_dwordx4 v[132:135], v197, s[24:27], s51 offen sc1\n"
        "  v_mfma_f32_16x16x32_bf16 v[8:11], a[168:171], v[80:83], v[8:11]\n"
        "  ds_read_b64_tr_b16 a[210:211], v178 offset:6144\n"
        "  v_mfma_f32_16x16x32_bf16 v[12:15], a[172:175], v[80:83], v[12:15]\n"
        "  ds_read_b64_tr_b16 a[212:213], v179 offset:4096\n"
        "  s_waitcnt vmcnt(12)  ; auto\n"
        "  v_mfma_f32_16x16x32_bf16 v[16:19], a[160:163], v[84:87], v[16:19]\n"
        "  ds_read_b64_tr_b16 a[214:215], v179 offset:6144\n"
        "  v_mfma_f32_16x16x32_bf16 v[20:23], a[164:167], v[84:87], v[20:23]\n"
        "  buffer_load_dwordx4 v[148:151], v197, s[24:27], s52 offen sc1\n"
        "  v_mfma_f32_16x16x32_bf16 v[24:27], a[168:171], v[84:87], v[24:27]\n"
        "  ds_read_b64_tr_b16 a[216:217], v180 offset:4096\n"
        "  v_mfma_f32_16x16x32_bf16 v[28:31], a[172:175], v[84:87], v[28:31]\n"
        "  ds_read_b64_tr_b16 a[218:219], v180 offset:6144\n"
        "  s_waitcnt vmcnt(12)  ; auto\n"
        "  v_mfma_f32_16x16x32_bf16 v[32:35], a[160:163], v[88:91], v[32:35]\n"
        "  ds_read_b64_tr_b16 a[220:221], v181 offset:4096\n"
        "  v_mfma_f32_16x16x32_bf16 v[36:39], a[164:167], v[88:91], v[36:39]\n"
        "  buffer_load_dwordx4 v[120:123], v198, s[24:27], s50 offen sc1\n"
        "  v_mfma_f32_16x16x32_bf16 v[40:43], a[168:171], v[88:91], v[40:43]\n"
        "  ds_read_b64_tr_b16 a[222:223], v181 offset:6144\n"
        "  v_mfma_f32_16x16x32_bf16 v[44:47], a[172:175], v[88:91], v[44:47]\n"
        "  s_waitcnt vmcnt(12)  ; auto\n"
        "  v_mfma_f32_16x16x32_bf16 v[48:51], a[160:163], v[92:95], v[48:51]\n"
        "  v_mfma_f32_16x16x32_bf16 v[52:55], a[164:167], v[92:95], v[52:55]\n"
        "  buffer_load_dwordx4 v[136:139], v198, s[24:27], s51 offen sc1\n"
        "  v_mfma_f32_16x16x32_bf16 v[56:59], a[168:171], v[92:95], v[56:59]\n"
        "  v_mfma_f32_16x16x32_bf16 v[60:63], a[172:175], v[92:95], v[60:63]\n"
        "  s_waitcnt vmcnt(12) lgkmcnt(15)  ; auto\n"
        "  v_mfma_f32_16x16x32_bf16 v[0:3], a[176:179], v[96:99], v[0:3]\n"
        "  ds_read_b64_tr_b16 a[224:225], v178 offset:8192\n"
        "  v_mfma_f32_16x16x32_bf16 v[4:7], a[180:183], v[96:99], v[4:7]\n"
        "  buffer_load_dwordx4 v[152:155], v198, s[24:27], s52 offen sc1\n"
        "  v_mfma_f32_16x16x32_bf16 v[8:11], a[184:187], v[96:99], v[8:11]\n"
        "  ds_read_b64_tr_b16 a[226:227], v178 offset:10240\n"
        "  v_mfma_f32_16x16x32_bf16 v[12:15], a[188:191], v[96:99], v[12:15]\n"
        "  ds_read_b64_tr_b16 a[228:229], v179 offset:8192\n"
        "  s_waitcnt vmcnt(12)  ; auto\n"
        "  v_mfma_f32_16x16x32_bf16 v[16:19], a[176:179], v[100:103], v[16:19]\n"
        "  ds_read_b64_tr_b16 a[230:231], v179 offset:10240\n"
        "  v_mfma_f32_16x16x32_bf16 v[20:23], a[180:183], v[100:103], v[20:23]\n"
        "  buffer_load_dwordx4 v[124:127], v199, s[24:27], s50 offen sc1\n"
        "  v_mfma_f32_16x16x32_bf16 v[24:27], a[184:187], v[100:103], v[24:27]\n"
        "  ds_read_b64_tr_b16 a[232:233], v180 offset:8192\n"
        "  v_mfma_f32_16x16x32_bf16 v[28:31], a[188:191], v[100:103], v[28:31]\n"
        "  ds_read_b64_tr_b16 a[234:235], v180 offset:10240\n"
        "  s_waitcnt vmcnt(12)  ; auto\n"
        "  v_mfma_f32_16x16x32_bf16 v[32:35], a[176:179], v[104:107], v[32:35]\n"
        "  ds_read_b64_tr_b16 a[236:237], v181 offset:8192\n"
        "  v_mfma_f32_16x16x32_bf16 v[36:39], a[180:183], v[104:107], v[36:39]\n"
        "  buffer_load_dwordx4 v[140:143], v199, s[24:27], s51 offen sc1\n"
        "  v_mfma_f32_16x16x32_bf16 v[40:43], a[184:187], v[104:107], v[40:43]\n"
        "  ds_read_b64_tr_b16 a[238:239], v181 offset:10240\n"
        "  v_mfma_f32_16x16x32_bf16 v[44:47], a[188:191], v[104:107], v[44:47]\n"
        "  s_waitcnt vmcnt(12)  ; auto\n"
        "  v_mfma_f32_16x16x32_bf16 v[48:51], a[176:179], v[108:111], v[48:51]\n"
        "  v_mfma_f32_16x16x32_bf16 v[52:55], a[180:183], v[108:111], v[52:55]\n"
        "  buffer_load_dwordx4 v[156:159], v199, s[24:27], s52 offen sc1\n"
        "  v_mfma_f32_16x16x32_bf16 v[56:59], a[184:187], v[108:111], v[56:59]\n"
        "  v_mfma_f32_16x16x32_bf16 v[60:63], a[188:191], v[108:111], v[60:63]\n"
        "  s_waitcnt vmcnt(12) lgkmcnt(15)  ; auto\n"
        "  v_mfma_f32_16x16x32_bf16 v[0:3], a[192:195], v[112:115], v[0:3]\n"
        "  v_mfma_f32_16x16x32_bf16 v[4:7], a[196:199], v[112:115], v[4:7]\n"
        "  v_mfma_f32_16x16x32_bf16 v[8:11], a[200:203], v[112:115], v[8:11]\n"
        "  v_mfma_f32_16x16x32_bf16 v[12:15], a[204:207], v[112:115], v[12:15]\n"
        "  s_waitcnt vmcnt(10) lgkmcnt(14)  ; auto\n"
        "  v_mfma_f32_16x16x32_bf16 v[0:3], a[208:211], v[128:131], v[0:3]\n"
        "  s_waitcnt lgkmcnt(12)  ; auto\n"
        "  v_mfma_f32_16x16x32_bf16 v[4:7], a[212:215], v[128:131], v[4:7]\n"
        "  s_waitcnt lgkmcnt(10)  ; auto\n"
        "  v_mfma_f32_16x16x32_bf16 v[8:11], a[216:219], v[128:131], v[8:11]\n"
        "  s_waitcnt lgkmcnt(8)  ; auto\n"
        "  v_mfma_f32_16x16x32_bf16 v[12:15], a[220:223], v[128:131], v[12:15]\n"
        "  s_waitcnt vmcnt(9) lgkmcnt(6)  ; auto\n"
        "  v_mfma_f32_16x16x32_bf16 v[0:3], a[224:227], v[144:147], v[0:3]\n"
        "  s_waitcnt lgkmcnt(4)  ; auto\n"
        "  v_mfma_f32_16x16x32_bf16 v[4:7], a[228:231], v[144:147], v[4:7]\n"
        "  s_waitcnt lgkmcnt(2)  ; auto\n"
        "  v_mfma_f32_16x16x32_bf16 v[8:11], a[232:235], v[144:147], v[8:11]\n"
        "  s_waitcnt lgkmcnt(0)  ; auto\n"
        "  v_mfma_f32_16x16x32_bf16 v[12:15], a[236:239], v[144:147], v[12:15]\n"
        "  s_waitcnt vmcnt(8)  ; auto\n"
        "  v_mfma_f32_16x16x32_bf16 v[16:19], a[192:195], v[116:119], v[16:19]\n"
        "  v_mfma_f32_16x16x32_bf16 v[20:23], a[196:199], v[116:119], v[20:23]\n"
        "  v_mfma_f32_16x16x32_bf16 v[24:27], a[200:203], v[116:119], v[24:27]\n"
        "  ds_write_b128 v224, v[0:3] offset:0\n"
        "  v_mfma_f32_16x16x32_bf16 v[28:31], a[204:207], v[116:119], v[28:31]\n"
        "  s_waitcnt vmcnt(7)  ; auto\n"
        "  v_mfma_f32_16x16x32_bf16 v[16:19], a[208:211], v[132:135], v[16:19]\n"
        "  ds_write_b128 v224, v[4:7] offset:1024\n"
        "  v_mfma_f32_16x16x32_bf16 v[20:23], a[212:215], v[132:135], v[20:23]\n"
        "  v_mfma_f32_16x16x32_bf16 v[24:27], a[216:219], v[132:135], v[24:27]\n"
        "  ds_write_b128 v224, v[8:11] offset:2048\n"
        "  v_mfma_f32_16x16x32_bf16 v[28:31], a[220:223], v[132:135], v[28:31]\n"
        "  s_waitcnt vmcnt(6)  ; auto\n"
        "  v_mfma_f32_16x16x32_bf16 v[16:19], a[224:227], v[148:151], v[16:19]\n"
        "  ds_write_b128 v224, v[12:15] offset:3072\n"
        "  v_mfma_f32_16x16x32_bf16 v[20:23], a[228:231], v[148:151], v[20:23]\n"
        "  v_mfma_f32_16x16x32_bf16 v[24:27], a[232:235], v[148:151], v[24:27]\n"
        "  v_mfma_f32_16x16x32_bf16 v[28:31], a[236:239], v[148:151], v[28:31]\n"
        "  s_waitcnt vmcnt(5)  ; auto\n"
        "  v_mfma_f32_16x16x32_bf16 v[32:35], a[192:195], v[120:123], v[32:35]\n"
        "  v_mfma_f32_16x16x32_bf16 v[36:39], a[196:199], v[120:123], v[36:39]\n"
        "  v_mfma_f32_16x16x32_bf16 v[40:43], a[200:203], v[120:123], v[40:43]\n"
        "  ds_write_b128 v224, v[16:19] offset:4096\n"
        "  v_mfma_f32_16x16x32_bf16 v[44:47], a[204:207], v[120:123], v[44:47]\n"
        "  s_waitcnt vmcnt(4)  ; auto\n"
        "  v_mfma_f32_16x16x32_bf16 v[32:35], a[208:211], v[136:139], v[32:35]\n"
        "  ds_write_b128 v224, v[20:23] offset:5120\n"
        "  v_mfma_f32_16x16x32_bf16 v[36:39], a[212:215], v[136:139], v[36:39]\n"
        "  v_mfma_f32_16x16x32_bf16 v[40:43], a[216:219], v[136:139], v[40:43]\n"
        "  ds_write_b128 v224, v[24:27] offset:6144\n"
        "  v_mfma_f32_16x16x32_bf16 v[44:47], a[220:223], v[136:139], v[44:47]\n"
        "  s_waitcnt vmcnt(3)  ; auto\n"
        "  v_mfma_f32_16x16x32_bf16 v[32:35], a[224:227], v[152:155], v[32:35]\n"
        "  ds_write_b128 v224, v[28:31] offset:7168\n"
        "  v_mfma_f32_16x16x32_bf16 v[36:39], a[228:231], v[152:155], v[36:39]\n"
        "  v_mfma_f32_16x16x32_bf16 v[40:43], a[232:235], v[152:155], v[40:43]\n"
        "  v_mfma_f32_16x16x32_bf16 v[44:47], a[236:239], v[152:155], v[44:47]\n"
        "  s_waitcnt vmcnt(2)  ; auto\n"
        "  v_mfma_f32_16x16x32_bf16 v[48:51], a[192:195], v[124:127], v[48:51]\n"
        "  v_mfma_f32_16x16x32_bf16 v[52:55], a[196:199], v[124:127], v[52:55]\n"
        "  v_mfma_f32_16x16x32_bf16 v[56:59], a[200:203], v[124:127], v[56:59]\n"
        "  ds_write_b128 v224, v[32:35] offset:8192\n"
        "  v_mfma_f32_16x16x32_bf16 v[60:63], a[204:207], v[124:127], v[60:63]\n"
        "  s_waitcnt vmcnt(1)  ; auto\n"
        "  v_mfma_f32_16x16x32_bf16 v[48:51], a[208:211], v[140:143], v[48:51]\n"
        "  ds_write_b128 v224, v[36:39] offset:9216\n"
        "  v_mfma_f32_16x16x32_bf16 v[52:55], a[212:215], v[140:143], v[52:55]\n"
        "  v_mfma_f32_16x16x32_bf16 v[56:59], a[216:219], v[140:143], v[56:59]\n"
        "  ds_write_b128 v224, v[40:43] offset:10240\n"
        "  v_mfma_f32_16x16x32_bf16 v[60:63], a[220:223], v[140:143], v[60:63]\n"
        "  s_waitcnt vmcnt(0)  ; auto\n"
        "  v_mfma_f32_16x16x32_bf16 v[48:51], a[224:227], v[156:159], v[48:51]\n"
        "  ds_write_b128 v224, v[44:47] offset:11264\n"
        "  v_mfma_f32_16x16x32_bf16 v[52:55], a[228:231], v[156:159], v[52:55]\n"
        "  v_mfma_f32_16x16x32_bf16 v[56:59], a[232:235], v[156:159], v[56:59]\n"
        "  v_mfma_f32_16x16x32_bf16 v[60:63], a[236:239], v[156:159], v[60:63]\n"
        "  s_waitcnt lgkmcnt(0)\n"
        "  s_barrier\n"
        "  ds_read_b128 v[80:83], v225 offset:57344\n"
        "  ds_read_b128 v[96:99], v226 offset:53248\n"
        "  ds_read_b128 v[112:115], v227 offset:49152\n"
        "  ds_read_b128 v[84:87], v225 offset:58368\n"
        "  ds_read_b128 v[100:103], v226 offset:54272\n"
        "  ds_read_b128 v[116:119], v227 offset:50176\n"
        "  ds_read_b128 v[88:91], v225 offset:59392\n"
        "  ds_read_b128 v[104:107], v226 offset:55296\n"
        "  ds_read_b128 v[120:123], v227 offset:51200\n"
        "  ds_read_b128 v[92:95], v225 offset:60416\n"
        "  ds_read_b128 v[108:111], v226 offset:56320\n"
        "  ds_read_b128 v[124:127], v227 offset:52224\n"
        "  s_waitcnt lgkmcnt(11)  ; auto\n"
        "  v_pk_add_f32 v[48:49], v[48:49], v[80:81] op_sel:[0,0] op_sel_hi:[1,1]\n"
        "  v_pk_add_f32 v[50:51], v[50:51], v[82:83] op_sel:[0,0] op_sel_hi:[1,1]\n"
        "  s_waitcnt lgkmcnt(8)  ; auto\n"
        "  v_pk_add_f32 v[52:53], v[52:53], v[84:85] op_sel:[0,0] op_sel_hi:[1,1]\n"
        "  v_pk_add_f32 v[54:55], v[54:55], v[86:87] op_sel:[0,0] op_sel_hi:[1,1]\n"
        "  v_pk_add_f32 v[48:49], v[48:49], v[96:97] op_sel:[0,0] op_sel_hi:[1,1]\n"
        "  v_pk_add_f32 v[50:51], v[50:51], v[98:99] op_sel:[0,0] op_sel_hi:[1,1]\n"
        "  s_waitcnt lgkmcnt(7)  ; auto\n"
        "  v_pk_add_f32 v[52:53], v[52:53], v[100:101] op_sel:[0,0] op_sel_hi:[1,1]\n"
        "  v_pk_add_f32 v[54:55], v[54:55], v[102:103] op_sel:[0,0] op_sel_hi:[1,1]\n"
        "  v_pk_add_f32 v[48:49], v[48:49], v[112:113] op_sel:[0,0] op_sel_hi:[1,1]\n"
        "  v_pk_add_f32 v[50:51], v[50:51], v[114:115] op_sel:[0,0] op_sel_hi:[1,1]\n"
        "  s_waitcnt lgkmcnt(6)  ; auto\n"
        "  v_pk_add_f32 v[52:53], v[52:53], v[116:117] op_sel:[0,0] op_sel_hi:[1,1]\n"
        "  v_pk_add_f32 v[54:55], v[54:55], v[118:119] op_sel:[0,0] op_sel_hi:[1,1]\n"
        "  v_cvt_pk_bf16_f32 v0, v48, v49\n"
        "  v_cvt_pk_bf16_f32 v1, v50, v51\n"
        "  v_cvt_pk_bf16_f32 v2, v52, v53\n"
        "  v_cvt_pk_bf16_f32 v3, v54, v55\n"
        "  s_nop 0  ; hazard\n"
        "  v_permlane16_swap_b32 v0, v2\n"
        "  v_permlane16_swap_b32 v1, v3\n"
        "  buffer_store_dwordx4 v[0:3], v200, s[28:31], s63 offen offset:0\n"
        "  s_waitcnt lgkmcnt(5)  ; auto\n"
        "  v_pk_add_f32 v[56:57], v[56:57], v[88:89] op_sel:[0,0] op_sel_hi:[1,1]\n"
        "  v_pk_add_f32 v[58:59], v[58:59], v[90:91] op_sel:[0,0] op_sel_hi:[1,1]\n"
        "  s_waitcnt lgkmcnt(2)  ; auto\n"
        "  v_pk_add_f32 v[60:61], v[60:61], v[92:93] op_sel:[0,0] op_sel_hi:[1,1]\n"
        "  v_pk_add_f32 v[62:63], v[62:63], v[94:95] op_sel:[0,0] op_sel_hi:[1,1]\n"
        "  v_pk_add_f32 v[56:57], v[56:57], v[104:105] op_sel:[0,0] op_sel_hi:[1,1]\n"
        "  v_pk_add_f32 v[58:59], v[58:59], v[106:107] op_sel:[0,0] op_sel_hi:[1,1]\n"
        "  s_waitcnt lgkmcnt(1)  ; auto\n"
        "  v_pk_add_f32 v[60:61], v[60:61], v[108:109] op_sel:[0,0] op_sel_hi:[1,1]\n"
        "  v_pk_add_f32 v[62:63], v[62:63], v[110:111] op_sel:[0,0] op_sel_hi:[1,1]\n"
        "  v_pk_add_f32 v[56:57], v[56:57], v[120:121] op_sel:[0,0] op_sel_hi:[1,1]\n"
        "  v_pk_add_f32 v[58:59], v[58:59], v[122:123] op_sel:[0,0] op_sel_hi:[1,1]\n"
        "  s_waitcnt lgkmcnt(0)  ; auto\n"
        "  v_pk_add_f32 v[60:61], v[60:61], v[124:125] op_sel:[0,0] op_sel_hi:[1,1]\n"
        "  v_pk_add_f32 v[62:63], v[62:63], v[126:127] op_sel:[0,0] op_sel_hi:[1,1]\n"
        "  v_cvt_pk_bf16_f32 v4, v56, v57\n"
        "  v_cvt_pk_bf16_f32 v5, v58, v59\n"
        "  v_cvt_pk_bf16_f32 v6, v60, v61\n"
        "  v_cvt_pk_bf16_f32 v7, v62, v63\n"
        "  s_nop 0  ; hazard\n"
        "  v_permlane16_swap_b32 v4, v6\n"
        "  v_permlane16_swap_b32 v5, v7\n"
        "  buffer_store_dwordx4 v[4:7], v200, s[28:31], s63 offen offset:64\n"
        "  s_mov_b64 exec, s[78:79]\n"
        "  v_cmp_ne_u32 vcc, v189, v191\n"
        "  s_nop 0  ; hazard\n"
        "  s_and_b64 s[92:93], vcc, exec\n"
        "  s_cbranch_scc0 .Ldone\n"
        "  s_branch .Lfin\n"
        ".Lslow:\n"
        "  s_and_b64 s[92:93], vcc, s[78:79]\n"
        "  s_cbranch_scc0 .Lslow1\n"
        "  s_mov_b64 exec, s[92:93]\n"
        "  v_mov_b32 v190, v188\n"
        "  v_mov_b32 v186, v188\n"
        "  buffer_store_dwordx2 v[190:191], v185, s[24:27], 0 offen sc1\n"
        ".Lslow1:\n"
        "  s_and_b64 s[92:93], vcc, s[76:77]\n"
        "  s_cbranch_scc0 .Lgo\n"
        "  s_sub_u32 s87, s87, 1  ; spin\n"
        "  s_cbranch_scc1 .Lgo\n"
        "  s_mov_b64 exec, s[80:81]\n"
        "  v_mov_b32 v188, 0\n"
        "  v_mov_b32 v189, 0\n"
        "  buffer_atomic_or_x2 v[188:189], v184, s[24:27], 0 offen sc0 sc1\n"
        "  s_waitcnt vmcnt(0)\n"
        "  s_branch .Lchk\n"
        ".Lfin:\n"
        "  s_mov_b64 exec, s[78:79]\n"
        "  v_mov_b32 v188, 0\n"
        "  v_mov_b32 v189, 0\n"
        "  buffer_atomic_or_x2 v[188:189], v184, s[24:27], 0 offen sc0 sc1\n"
        "  s_waitcnt vmcnt(0)\n"
        "  v_cmp_ne_u32 vcc, v188, v186\n"
        "  s_nop 0  ; hazard\n"
        "  s_and_b64 s[92:93], vcc, exec\n"
        "  s_cbranch_scc0 .Lfin1\n"
        "  s_mov_b64 exec, s[92:93]\n"
        "  v_mov_b32 v190, v188\n"
        "  v_mov_b32 v186, v188\n"
        "  buffer_store_dwordx2 v[190:191], v185, s[24:27], 0 offen sc1\n"
        "  s_mov_b64 exec, s[78:79]\n"
        ".Lfin1:\n"
        "  v_cmp_ne_u32 vcc, v189, v191\n"
        "  s_nop 0  ; hazard\n"
        "  s_and_b64 s[92:93], vcc, exec\n"
        "  s_cbranch_scc0 .Ldone\n"
        "  s_sleep 2\n"
        "  s_sub_u32 s87, s87, 1  ; spin\n"
        "  s_cbranch_scc0 .Lfin\n"
        ".Ldone:\n"
        ".Lend:\n"
        : "+{v0}"(tid)
        : [ka] "s"(__builtin_amdgcn_kernarg_segment_ptr()), [wg] "s"(__builtin_amdgcn_workgroup_id_x()),
          [lds] "s"((unsigned)(uintptr_t)(__attribute__((address_space(3))) char*)lds)
        : "v1", "v2", "v3", "v4", "v5", "v6", "v7", "v8", "v9", "v10", "v11", "v12", "v13", "v14", "v15", "v16",
          "v17", "v18", "v19", "v20", "v21", "v22", "v23", "v24", "v25", "v26", "v27", "v28", "v29", "v30", "v31", "v32",
          "v33", "v34", "v35", "v36", "v37", "v38", "v39", "v40", "v41", "v42", "v43", "v44", "v45", "v46", "v47", "v48",
          "v49", "v50", "v51", "v52", "v53", "v54", "v55", "v56", "v57", "v58", "v59", "v60", "v61", "v62", "v63", "v64",
          "v65", "v66", "v67", "v68", "v69", "v70", "v71", "v72", "v73", "v74", "v75", "v76", "v77", "v78", "v79", "v80",
          "v81", "v82", "v83", "v84", "v85", "v86", "v87", "v88", "v89", "v90", "v91", "v92", "v93", "v94", "v95", "v96",
          "v97", "v98", "v99", "v100", "v101", "v102", "v103", "v104", "v105", "v106", "v107", "v108", "v109", "v110", "v111", "v112",
          "v113", "v114", "v115", "v116", "v117", "v118", "v119", "v120", "v121", "v122", "v123", "v124", "v125", "v126", "v127", "v128",
          "v129", "v130", "v131", "v132", "v133", "v134", "v135", "v136", "v137", "v138", "v139", "v140", "v141", "v142", "v143", "v144",
          "v145", "v146", "v147", "v148", "v149", "v150", "v151", "v152", "v153", "v154", "v155", "v156", "v157", "v158", "v159", "v160",
          "v161", "v162", "v163", "v164", "v165", "v166", "v167", "v168", "v169", "v170", "v171", "v172", "v173", "v174", "v175", "v176",
          "v177", "v178", "v179", "v180", "v181", "v182", "v183", "v184", "v185", "v186", "v187", "v188", "v189", "v190", "v191", "v192",
          "v193", "v194", "v195", "v196", "v197", "v198", "v199", "v200", "v201", "v202", "v203", "v204", "v205", "v206", "v207", "v208",
          "v209", "v210", "v211", "v212", "v213", "v214", "v215", "v216", "v217", "v218", "v219", "v220", "v221", "v222", "v223", "v224",
          "v225", "v226", "v227", "v228", "v229", "v230", "v231", "v232", "v233", "v234", "v235", "v236", "v237", "v238", "v239", "v240",
          "v241", "v242", "v243", "v244", "v245", "v246", "v247", "v248", "v249", "v250", "v251", "v252", "v253", "v254", "v255", "a0",
          "a1", "a2", "a3", "a4", "a5", "a6", "a7", "a8", "a9", "a10", "a11", "a12", "a13", "a14", "a15", "a16",
          "a17", "a18", "a19", "a20", "a21", "a22", "a23", "a24", "a25", "a26", "a27", "a28", "a29", "a30", "a31", "a32",
          "a33", "a34", "a35", "a36", "a37", "a38", "a39", "a40", "a41", "a42", "a43", "a44", "a45", "a46", "a47", "a48",
          "a49", "a50", "a51", "a52", "a53", "a54", "a55", "a56", "a57", "a58", "a59", "a60", "a61", "a62", "a63", "a64",
          "a65", "a66", "a67", "a68", "a69", "a70", "a71", "a72", "a73", "a74", "a75", "a76", "a77", "a78", "a79", "a80",
          "a81", "a82", "a83", "a84", "a85", "a86", "a87", "a88", "a89", "a90", "a91", "a92", "a93", "a94", "a95", "a96",
          "a97", "a98", "a99", "a100", "a101", "a102", "a103", "a104", "a105", "a106", "a107", "a108", "a109", "a110", "a111", "a112",
          "a113", "a114", "a115", "a116", "a117", "a118", "a119", "a120", "a121", "a122", "a123", "a124", "a125", "a126", "a127", "a128",
          "a129", "a130", "a131", "a132", "a133", "a134", "a135", "a136", "a137", "a138", "a139", "a140", "a141", "a142", "a143", "a144",
          "a145", "a146", "a147", "a148", "a149", "a150", "a151", "a152", "a153", "a154", "a155", "a156", "a157", "a158", "a159", "a160",
          "a161", "a162", "a163", "a164", "a165", "a166", "a167", "a168", "a169", "a170", "a171", "a172", "a173", "a174", "a175", "a176",
          "a177", "a178", "a179", "a180", "a181", "a182", "a183", "a184", "a185", "a186", "a187", "a188", "a189", "a190", "a191", "a192",
          "a193", "a194", "a195", "a196", "a197", "a198", "a199", "a200", "a201", "a202", "a203", "a204", "a205", "a206", "a207", "a208",
          "a209", "a210", "a211", "a212", "a213", "a214", "a215", "a216", "a217", "a218", "a219", "a220", "a221", "a222", "a223", "a224",
          "a225", "a226", "a227", "a228", "a229", "a230", "a231", "a232", "a233", "a234", "a235", "a236", "a237", "a238", "a239", "a240",
          "a241", "a242", "a243", "a244", "a245", "a246", "a247", "a248", "a249", "a250", "a251", "a252", "a253", "a254", "a255", "s4",
          "s5", "s6", "s7", "s8", "s9", "s10", "s11", "s12", "s13", "s14", "s15", "s16", "s17", "s18", "s19", "s20",
          "s21", "s22", "s23", "s24", "s25", "s26", "s27", "s28", "s29", "s30", "s31", "s33", "s34", "s35", "s36", "s37",
          "s38", "s39", "s40", "s41", "s42", "s43", "s44", "s45", "s46", "s47", "s48", "s49", "s50", "s51", "s52", "s53",
          "s54", "s55", "s56", "s57", "s58", "s59", "s60", "s61", "s62", "s63", "s64", "s65", "s66", "s67", "s68", "s69",
          "s70", "s71", "s72", "s73", "s74", "s75", "s76", "s77", "s78", "s79", "s80", "s81", "s82", "s83", "s84", "s85",
          "s86", "s87", "s88", "s89", "s90", "s91", "s92", "s93", "s94", "s95", "s96", "s97", "s98", "s99", "vcc", "scc",
          "memory");
#endif
}

extern "C" void kernel_launch(void* const* d_in, const int* in_sizes, int n_in, void* d_out, int out_size,
                              void* d_ws, size_t ws_size, hipStream_t stream) {
    const uint32_t F = 0x00020000u;
    Args a;
    a.x = Buf{d_in[0], 2048u * 448u * 2u, F};
    a.wqkv = Buf{d_in[1], 448u * 1344u * 2u, F};
    a.w1 = Buf{d_in[4], 448u * 1792u * 2u, F};
    a.w2 = Buf{d_in[6], 1792u * 448u * 2u, F};
    a.wo = Buf{d_in[2], 448u * 448u * 2u, F};
    a.ws = Buf{d_ws, 10092544u, F};
    a.out = Buf{d_out, 2048u * 448u * 2u, F};
    a.b1 = Buf{d_in[5], 1792u * 2u, F};
    a.b2 = Buf{d_in[7], 448u * 2u, F};
    fused_block_kernel<<<256, 256, 0, stream>>>(a);
}
```
